```python
import torch

def _softmax(x, axis=-1):
    return torch.softmax(x, dim=axis)
def _gelu(x):
    return torch.nn.functional.gelu(x, approximate="tanh")
def _silu(x):
    return torch.nn.functional.silu(x)
def _sigmoid(x):
    return torch.sigmoid(x)
def _layernorm(x, axis=-1, eps=1e-5):
    m = x.mean(dim=axis, keepdim=True)
    v = x.var(dim=axis, keepdim=True, unbiased=False)
    return (x - m) / torch.sqrt(v + eps)
def _scatter_add(tgt, idx, src, axis=0):
    return tgt.clone().index_add_(axis, idx, src)
def _segment_sum(src, seg, n):
    out = torch.zeros((n,) + src.shape[1:], dtype=src.dtype)
    return out.index_add_(0, seg, src)
def _pad(x, pw):
    flat = tuple(v for pair in reversed(pw) for v in pair)
    return torch.nn.functional.pad(x, flat)

kernel_name = 'gated_mix__ssm_conv_s4__softmax_full_mha__prompt'

def setup_inputs():

    _w = (lambda a: (lambda t: t.to(torch.bfloat16) if t.is_floating_point() else t)(torch.as_tensor(a)))
    def _A_setup():
        B, L, D, N = 1, 2048, 512, 128
        A_re = -(lambda _x: torch.exp(torch.as_tensor(_x)))((lambda *s: torch.randn(*s, dtype=torch.bfloat16) * 6.4)(D, N))
        A_im = torch.pi * (lambda *s: torch.randn(*s, dtype=torch.bfloat16) * 0.1)(D, N)
        B_re = (lambda *s: torch.randn(*s, dtype=torch.bfloat16) * 0.2)(D, N)
        B_im = (lambda *s: torch.randn(*s, dtype=torch.bfloat16) * 0.2)(D, N)
        C_re = (lambda *s: torch.randn(*s, dtype=torch.bfloat16) * 0.2)(D, N)
        C_im = (lambda *s: torch.randn(*s, dtype=torch.bfloat16) * 0.2)(D, N)
        log_dt = (lambda *s: torch.randn(*s, dtype=torch.bfloat16) * 0.1)(D) * 0.5 - 4.0
        Dskip = (lambda *s: torch.randn(*s, dtype=torch.bfloat16) * 0.1)(D)
        return {
            "u": _w((lambda *s: torch.randn(*s, dtype=torch.bfloat16) * 0.05)(B, L, D)),
            "A_re": _w(A_re),
            "A_im": _w(A_im),
            "B_re": _w(B_re),
            "B_im": _w(B_im),
            "C_re": _w(C_re),
            "C_im": _w(C_im),
            "log_dt": _w(log_dt),
            "Dskip": _w(Dskip),
        }
    _A_in = _A_setup()

    def _B_setup():
        B, S, D = 1, 2048, 512
        return {
            "x": _w((lambda *s: torch.randn(*s, dtype=torch.bfloat16) * 0.05)(B, S, D)),
            "wqkv": _w((lambda *s: torch.randn(*s, dtype=torch.bfloat16) * 0.8)(D, 3 * D)),
            "wo": _w((lambda *s: torch.randn(*s, dtype=torch.bfloat16) * 0.05)(D, D)),
        }
    _B_in = _B_setup()

    return {
        "A_u": _A_in["u"],
        "A_A_re": _A_in["A_re"],
        "A_A_im": _A_in["A_im"],
        "A_B_re": _A_in["B_re"],
        "A_B_im": _A_in["B_im"],
        "A_C_re": _A_in["C_re"],
        "A_C_im": _A_in["C_im"],
        "A_log_dt": _A_in["log_dt"],
        "A_Dskip": _A_in["Dskip"],
        "B_x": _B_in["x"],
        "B_wqkv": _B_in["wqkv"],
        "B_wo": _B_in["wo"],
        "gate_w": _w((lambda *s: torch.randn(*s, dtype=torch.bfloat16) * 0.05)(512) * 16.0),
    }

def reference(A_u, A_A_re, A_A_im, A_B_re, A_B_im, A_C_re, A_C_im, A_log_dt, A_Dskip, B_x, B_wqkv, B_wo, gate_w):
    def _A_ref(u, A_re, A_im, B_re, B_im, C_re, C_im, log_dt, Dskip):
        Bsz, L, D = u.shape
        dt = torch.exp(log_dt)[:, None]
        A = A_re + 1j * A_im
        Bc = B_re + 1j * B_im
        Cc = C_re + 1j * C_im
        logdA = dt * A
        dB = dt * Bc
        l = (lambda x, dt: x.to(dt))(torch.arange(L), A_re.dtype)
        P = torch.exp(logdA[:, :, None] * l[None, None, :])
        K = torch.einsum("dn,dnl,dn->dl", Cc, P, dB).real
        L2 = 2 * L
        Kp = _pad(K, ((0, 0), (0, L)))
        up = (lambda x, *d: x.permute(*d))(u, 0, 2, 1)
        up = _pad(up, ((0, 0), (0, 0), (0, L)))
        Kf = (lambda x, axes=(-2,-1): torch.fft.rfft2(x.float(), dim=axes))(Kp[:, None, :], axes=(-2, -1))
        Uf = (lambda x, axes=(-2,-1): torch.fft.rfft2(x.float(), dim=axes))(up[:, :, None, :], axes=(-2, -1))
        Yf = Uf * Kf[None, :, :, :]
        y = (lambda x, s, axes=(-2,-1): torch.fft.irfft2(x, s=s, dim=axes).to(torch.bfloat16))(Yf, (1, L2), axes=(-2, -1))[:, :, 0, :L]
        y = (lambda x, *d: x.permute(*d))(y, 0, 2, 1) + u * Dskip[None, None, :]
        return y

    def _B_ref(x, wqkv, wo):
        B, S, D = x.shape
        H = 32; Dh = D // H
        qkv = (lambda x, *d: x.permute(*d))((x @ wqkv).reshape(B, S, 3, H, Dh), 2, 0, 3, 1, 4)
        q, k, v = qkv[0], qkv[1], qkv[2]
        scale = Dh ** -0.5
        scores = torch.einsum("bhqd,bhkd->bhqk", q, k) * scale
        p = _softmax(scores, axis=-1)
        o = torch.einsum("bhqk,bhkd->bhqd", p, v)
        return (lambda x, *d: x.permute(*d))(o, 0, 2, 1, 3).reshape(B, S, D) @ wo

    x = A_u
    g = _sigmoid((x * gate_w).sum(-1, keepdims=True))
    a = _A_ref(x, A_A_re, A_A_im, A_B_re, A_B_im, A_C_re, A_C_im, A_log_dt, A_Dskip)
    b = _B_ref(x, B_wqkv, B_wo)
    return g * a + (1 - g) * b
```

```cpp
#define v1 NLAUNCH 1
#include <hip/hip_runtime.h>
#include <hip/hip_bf16.h>
#include <stdint.h>
#pragma clang diagnostic ignored "-Wint-to-pointer-cast"

#define NLAUNCH 1

#define LAS __attribute__((address_space(3)))
#define GAS __attribute__((address_space(1)))
typedef unsigned u32;
typedef unsigned long long u64;
typedef __attribute__((ext_vector_type(2))) u32 u32x2;
typedef __attribute__((ext_vector_type(4))) u32 u32x4;
typedef __attribute__((ext_vector_type(8))) u32 u32x8;
typedef __attribute__((ext_vector_type(2))) float f32x2;
typedef __attribute__((ext_vector_type(4))) float f32x4;
typedef __attribute__((ext_vector_type(8))) float f32x8;
typedef __attribute__((ext_vector_type(16))) float f32x16;
typedef __attribute__((ext_vector_type(2))) __bf16 bf16x2;
typedef __attribute__((ext_vector_type(8))) __bf16 bf16x8;
typedef __attribute__((ext_vector_type(16))) __bf16 bf16x16;
typedef __attribute__((ext_vector_type(4))) short s16x4;
typedef __attribute__((ext_vector_type(2))) unsigned short u16x2;

constexpr u32 WS_KV = 0;
constexpr u32 WS_OF = 4u << 20;
constexpr u32 WS_YT = 6u << 20;
constexpr u32 WS_KVFLAG = 8u << 20;
constexpr u32 WS_OFLAG = WS_KVFLAG + 32768;
constexpr u32 WS_YFLAG = WS_OFLAG + 32768;
constexpr u32 WS_NONCE = WS_YFLAG + 65536;
constexpr u32 WS_QF = 9u << 20;
constexpr u32 WS_END = 12u << 20;
constexpr u32 WS_STAMP = 16u << 20;

constexpr u32 CHUNK = 28672, L_W = 16384;
constexpr u32 L_TAB = 86016, TAB_ROW = 528, TAB_A = 21 * TAB_ROW, TAB_CH = 53 * TAB_ROW;
constexpr u32 L_KF = L_TAB + 2 * TAB_CH, KF_CH = 4608;
constexpr u32 L_QX = L_KF + 2 * KF_CH;
constexpr u32 L_MISC = L_QX + 8192;
constexpr u32 L_GW = L_MISC;
constexpr u32 L_QN = L_GW + 1024;
constexpr u32 L_G = L_QN + 1024;
constexpr u32 L_KN = L_G + 512;
constexpr u32 L_SYNC = L_KN + 32;
constexpr u32 L_UC = L_TAB, UC_COPY = 4160, UC_CH = 4 * UC_COPY;
constexpr u32 L_KB = L_UC + 2 * UC_CH + 64, KB_CH = 96 * 80;
constexpr u32 L_WO = 0;
constexpr u32 L_CX = 65536;
constexpr u32 L_PART = 65536;
constexpr u32 L_RED = 65536;
constexpr u32 L_YT = L_QX;
constexpr u32 LDS_BYTES = 163840;
static_assert(L_SYNC + 32 <= LDS_BYTES && L_KB + 2 * KB_CH <= L_KF, "lds map");

constexpr float LOG2E = 1.4426950408889634f;
constexpr float QSCALE = 0.25f * LOG2E;
constexpr float SAFE2 = 64.f * 64.f;
constexpr u32 SPIN_CAP = 1u << 17;

#define LD(T, off) (*(const LAS T*)(u32)(off))
#define ST(T, off) (*(LAS T*)(u32)(off))
#define RLX_AGENT __ATOMIC_RELAXED, __HIP_MEMORY_SCOPE_AGENT
#define SC1 16
#define BAR(N) asm volatile("s_waitcnt vmcnt(" #N ") lgkmcnt(0)\n\ts_barrier" ::: "memory")
#define BAR_LDS() asm volatile("s_waitcnt lgkmcnt(0)\n\ts_barrier" ::: "memory")

#define STAMP(i) do { } while (0)
#define STAMP_ARG
#define STAMP_PASS

__device__ __forceinline__ u32 pk2(float a, float b) { bf16x2 v = {(__bf16)a, (__bf16)b}; return __builtin_bit_cast(u32, v); }
__device__ __forceinline__ float dot2(u32 a, u32 b, float c) {
    return __builtin_amdgcn_fdot2_f32_bf16(__builtin_bit_cast(bf16x2, a), __builtin_bit_cast(bf16x2, b), c, false); }
__device__ __forceinline__ float bflo(u32 v) { return __uint_as_float(v << 16); }
__device__ __forceinline__ float bfhi(u32 v) { return __uint_as_float(v & 0xffff0000u); }
__device__ __forceinline__ f32x16 mfma32(u32x4 a, u32x4 b, f32x16 c) {
    return __builtin_amdgcn_mfma_f32_32x32x16_bf16(__builtin_bit_cast(bf16x8, a), __builtin_bit_cast(bf16x8, b), c, 0, 0, 0); }
__device__ __forceinline__ f32x4 mfma16(u32x4 a, u32x4 b, f32x4 c) {
    return __builtin_amdgcn_mfma_f32_16x16x32_bf16(__builtin_bit_cast(bf16x8, a), __builtin_bit_cast(bf16x8, b), c, 0, 0, 0); }
__device__ __forceinline__ u32x2 trd(u32 off) {
    return __builtin_bit_cast(u32x2, __builtin_amdgcn_ds_read_tr16_b64_v4i16((LAS s16x4*)off)); }
__device__ __forceinline__ u32x4 trd2(u32 o0, u32 o1) { const u32x2 a = trd(o0), b = trd(o1); return (u32x4){a.x, a.y, b.x, b.y}; }
__device__ __forceinline__ void dma1(u32 v0, const void* sbase, u32 dst) {
    asm volatile("s_mov_b32 m0, %2\n\ts_nop 0\n\tglobal_load_lds_dwordx4 %0, %1" :: "v"(v0), "s"(sbase), "s"(dst) : "memory"); }
__device__ __forceinline__ void dma3(u32 v0, u32 v1, const void* sbase, u32 dst) {
    asm volatile("s_mov_b32 m0, %3\n\ts_nop 0\n\tglobal_load_lds_dwordx4 %0, %2\n\tglobal_load_lds_dwordx4 %1, %2 offset:1024\n\t"
                 "global_load_lds_dwordx4 %0, %2 offset:2048" :: "v"(v0), "v"(v1), "s"(sbase), "s"(dst) : "memory"); }
__device__ __forceinline__ void dma4(u32 v0, u32 v1, u32 v2, u32 v3, const void* sbase, u32 dst) {
    asm volatile("s_mov_b32 m0, %5\n\ts_nop 0\n\tglobal_load_lds_dwordx4 %0, %4\n\tglobal_load_lds_dwordx4 %1, %4 offset:1024\n\t"
                 "global_load_lds_dwordx4 %2, %4 offset:2048\n\tglobal_load_lds_dwordx4 %3, %4 offset:3072"
                 :: "v"(v0), "v"(v1), "v"(v2), "v"(v3), "s"(sbase), "s"(dst) : "memory"); }
__device__ __forceinline__ float halves_sum(float v) {
    auto r = __builtin_amdgcn_permlane32_swap(__float_as_uint(v), __float_as_uint(v), false, false);
    return __uint_as_float(r[0]) + __uint_as_float(r[1]); }
__device__ __forceinline__ float halves_max(float v) {
    auto r = __builtin_amdgcn_permlane32_swap(__float_as_uint(v), __float_as_uint(v), false, false);
    return fmaxf(__uint_as_float(r[0]), __uint_as_float(r[1])); }
template <int CTRL> __device__ __forceinline__ float dpp_max(float v) {
    return fmaxf(v, __uint_as_float(__builtin_amdgcn_update_dpp(__float_as_uint(v), __float_as_uint(v), CTRL, 0xf, 0xf, false))); }
__device__ __forceinline__ u32x4 widen(u32 a0, u32 a1, u32 b0, u32 b1) {
    auto r0 = __builtin_amdgcn_permlane32_swap(a0, b0, false, false);
    auto r1 = __builtin_amdgcn_permlane32_swap(a1, b1, false, false);
    return (u32x4){r0[0], r1[0], r0[1], r1[1]}; }
__device__ __forceinline__ f32x2 cmul(f32x2 a, f32x2 b) { return (f32x2){a.x * b.x - a.y * b.y, a.x * b.y + a.y * b.x}; }
__device__ __forceinline__ f32x2 cexp_n(float zr2, float th, float n) {
    const float m = __builtin_amdgcn_exp2f(zr2 * n), a = __builtin_amdgcn_fractf(th * n);
    return (f32x2){m * __builtin_amdgcn_cosf(a), m * __builtin_amdgcn_sinf(a)}; }
template <bool WAIT> __device__ __forceinline__ u64 poll_tag(const char* p, bool active, u64 tag) {
    u64 v = tag << 16;
#pragma unroll 1
    for (u32 spins = 0; spins < SPIN_CAP; ++spins) {
        if (active) v = __hip_atomic_load((const GAS u64*)p, RLX_AGENT);
        if (!WAIT || __all((v >> 16) == tag)) break;
        __builtin_amdgcn_s_sleep(1);
    }
    __builtin_amdgcn_fence(__ATOMIC_ACQUIRE, "wavefront");
    return v; }

struct Params {
    const char *x, *a_re, *a_im, *b_re, *b_im, *c_re, *c_im, *log_dt, *dskip, *wqkv, *wo, *gate_w;
    char *out, *ws;
};

template <int ROLE>
__device__ __forceinline__ void phase1(const Params& p, __amdgpu_buffer_rsrc_t wsr, char* ws, int lane, int w, int hp, int qb, int d0, u64& nonce STAMP_ARG) {
    const int tb = w & 3, r = lane & 31, h = lane >> 5;
    const int ch = w & 1, n = ((w >> 1) & 1) * 64 + lane;
    const u32 po = ((d0 + ch) * 128 + n) * 2;
    u32 are, aim, bre = 0, bim = 0, cre = 0, cim = 0;
    asm volatile("global_load_ushort %0, %2, %3\n\tglobal_load_ushort %1, %2, %4" : "=&v"(are), "=&v"(aim) : "v"(po), "s"(p.a_re), "s"(p.a_im));
    if (ROLE == 0)
        asm volatile("global_load_ushort %0, %4, %5\n\tglobal_load_ushort %1, %4, %6\n\tglobal_load_ushort %2, %4, %7\n\tglobal_load_ushort %3, %4, %8"
                     : "=&v"(bre), "=&v"(bim), "=&v"(cre), "=&v"(cim) : "v"(po), "s"(p.b_re), "s"(p.b_im), "s"(p.c_re), "s"(p.c_im));
    else if (tb == 0) dma1(lane * 16, p.gate_w, L_GW);
    u32 dtw, dsw;
    u64 nw;
    asm volatile("s_load_dword %0, %3, 0x0\n\ts_load_dword %1, %4, 0x0\n\ts_load_dwordx2 %2, %5, 0x0"
                 : "=&s"(dtw), "=&s"(dsw), "=&s"(nw) : "s"(p.log_dt + d0 * 2), "s"(p.dskip + d0 * 2), "s"(ws + WS_NONCE));
    u32 vo[4];
    const char* src; u32 step, dst;
    if (ROLE == 0) {
        vo[0] = (lane >> 2) * 3072 + ((lane & 3) << 4); vo[1] = vo[0] ^ 32;
        src = p.wqkv + (tb * 16 * 3072 + hp * 64); step = 64 * 3072; dst = L_W + tb * 3072;
    } else {
#pragma unroll
        for (int i = 0; i < 4; ++i) vo[i] = (7 * i + (lane >> 3)) * 1024 + (((lane & 7) ^ ((4 * i + (lane >> 4)) & 7)) << 4);
        src = p.x + (qb * 128 + tb * 32) * 1024; step = 128; dst = tb * 4096;
    }
    auto issue = [&](int c) {
        if (ROLE == 0) dma3(vo[0], vo[1], src + c * step, (c % 3) * CHUNK + dst);
        else dma4(vo[0], vo[1], vo[2], vo[3], src + c * step, (c % 3) * CHUNK + dst);
    };
    issue(0); issue(1); issue(2);
    asm volatile("s_waitcnt lgkmcnt(0)" : "+s"(dtw), "+s"(dsw), "+s"(nw));
    nonce = nw;
    if (w == 0 && lane < 8) ST(u32, L_SYNC + lane * 4) = 0;
    const float dt = (float)(__bf16)expf(ch ? bfhi(dtw) : bflo(dtw));
    if (ROLE == 0) asm volatile("s_waitcnt vmcnt(9)" : "+v"(are), "+v"(aim), "+v"(bre), "+v"(bim), "+v"(cre), "+v"(cim));
    else asm volatile("s_waitcnt vmcnt(12)" : "+v"(are), "+v"(aim));
    STAMP(1);
    {
        const float zr2 = (dt * bflo(are)) * LOG2E, th = (dt * bflo(aim)) * 0.15915494309189535f;
        const u32 t0 = L_TAB + ch * TAB_CH + n * 4;
        if (ROLE == 0) {
            const f32x2 e1 = cexp_n(zr2, th, 1.f), g = cexp_n(zr2, th, 256.f);
            f32x2 a[8];
            a[0] = cmul((f32x2){bflo(cre), bflo(cim)}, (f32x2){dt * bflo(bre), dt * bflo(bim)});
#define PUT_A(row, v) ST(u32, t0 + (row) * TAB_ROW) = pk2((v).x, -(v).y)
            PUT_A(0, a[0]);
#pragma unroll
            for (int i = 1; i < 8; ++i) { a[i] = cmul(a[i - 1], e1); PUT_A(i, a[i]); }
            f32x2 c0 = cmul(a[0], g), c2 = cmul(a[2], g), c4 = cmul(a[4], g), c6 = cmul(a[6], g);
            PUT_A(8, c0); PUT_A(9, c2); PUT_A(10, c4); PUT_A(11, c6);
            c0 = cmul(c0, g); c4 = cmul(c4, g); PUT_A(12, c0); PUT_A(13, c4);
            c0 = cmul(c0, g); c4 = cmul(c4, g); PUT_A(14, c0); PUT_A(15, c4);
#pragma unroll
            for (int i = 16; i < 21; ++i) { c0 = cmul(c0, g); PUT_A(i, c0); }
#undef PUT_A
        } else {
            const f32x2 e8 = cexp_n(zr2, th, 8.f), e16 = cmul(e8, e8);
            f32x2 p0 = {1.f, 0.f}, p1 = e8;
#pragma unroll
            for (int i = 0; i < 32; i += 2) {
                ST(u32, t0 + TAB_A + i * TAB_ROW) = pk2(p0.x, p0.y);
                ST(u32, t0 + TAB_A + (i + 1) * TAB_ROW) = pk2(p1.x, p1.y);
                p0 = cmul(p0, e16); p1 = cmul(p1, e16);
            }
        }
    }
    STAMP(2);
    u32 xa[4];
#pragma unroll
    for (int ks = 0; ks < 4; ++ks) xa[ks] = (tb * 32 + r) * 128 + (((2 * ks + h) ^ ((r >> 1) & 7)) << 4);
    const int g1 = (lane >> 4) & 1;
    const u32 trb = L_W + (8 * h + ((lane >> 2) & 3)) * 64 + (lane & 3) * 8;
    const u32 wa = trb + g1 * 1024 + ((ROLE ^ g1) << 5), wv = trb + 2048 + (g1 << 5);
    f32x16 aqk = {}, av = {};
    float gacc = 0.f;
    u32 ug[4];
#pragma unroll
    for (int c = 0; c < 8; ++c) {
        if (c == 0) { if (ROLE) BAR(8); else BAR(6); }
        else if (c < 6) { if (ROLE) BAR(4); else BAR(3); }
        else if (c == 6) { if (ROLE) BAR(8); else BAR(7); }
        else BAR(4);
        if (c >= 1 && c <= 5) issue(c + 2);
        if (c == 5) {
            const u32 uo = (w * 256 + lane) * 1024;
            const char* ub = p.x + d0 * 2;
            asm volatile("global_load_dword %0, %4, %5\n\tglobal_load_dword %1, %4, %6\n\tglobal_load_dword %2, %4, %7\n\tglobal_load_dword %3, %4, %8"
                         : "=&v"(ug[0]), "=&v"(ug[1]), "=&v"(ug[2]), "=&v"(ug[3])
                         : "v"(uo), "s"(ub), "s"(ub + 65536), "s"(ub + 131072), "s"(ub + 196608));
        }
        if (c == 0) {
            STAMP(3);
            const int aq = (threadIdx.x >> 7) & 1, col = lane & 15, g = lane >> 4;
            const u32 ta = L_TAB + ch * TAB_CH + (aq * 16 + col) * TAB_ROW + g * 16;
            const u32 tbb = L_TAB + ch * TAB_CH + TAB_A + (ROLE * 16 + col) * TAB_ROW + g * 16;
            f32x4 kq = {};
#pragma unroll
            for (int ks = 0; ks < 8; ++ks) kq = mfma16(LD(u32x4, ta + ks * 64), LD(u32x4, tbb + ks * 64), kq);
            if (ROLE == 0 && aq == 0 && lane == 0) kq[0] += ch ? bfhi(dsw) : bflo(dsw);
            const int l0 = aq ? (g == 0 ? 1024 : g == 1 ? 2048 : 2052) : (g < 2 ? 4 * g : 256 * g - 256);
            const int s1 = aq ? (g == 0 ? 256 : 1) : (g < 2 ? 1 : 2 * g - 2), s2 = (!aq && g == 3) ? 256 : 2 * s1;
            const u32 k0 = L_KF + ch * KF_CH + (l0 + 8 * (ROLE * 16 + col)) * 2;
            const u32 e01 = pk2(kq[0], kq[1]), e23 = pk2(kq[2], kq[3]);
            ST(unsigned short, k0) = (unsigned short)e01; ST(unsigned short, k0 + 2 * s1) = (unsigned short)(e01 >> 16);
            ST(unsigned short, k0 + 2 * s2) = (unsigned short)e23; ST(unsigned short, k0 + 2 * (s1 + s2)) = (unsigned short)(e23 >> 16);
        }
        if (c == 1) {
            const int i8 = (w & 3) * 64 + lane, blk = i8 >> 2, pc = i8 & 3;
            u32x4 kv;
#pragma unroll
            for (int j = 0; j < 4; ++j) {
                const float t2 = (float)(2 * (32 * blk + 31 - 8 * pc - 2 * j));
                const u32 rr = pk2(t2, t2 - 2.f);
                const u16x2 e = {LD(unsigned short, L_KF + ROLE * KF_CH + (u32)bflo(rr)), LD(unsigned short, L_KF + ROLE * KF_CH + (u32)bfhi(rr))};
                kv[j] = __builtin_bit_cast(u32, e);
            }
            ST(u32x4, L_KB + ROLE * KB_CH + (blk + 32) * 80 + pc * 16) = kv;
            if (i8 < 128) ST(u32x4, L_KB + ROLE * KB_CH + blk * 80 + pc * 16) = (u32x4){0, 0, 0, 0};
            else if (i8 < 128 + 36) ST(u32x2, L_UC + ROLE * UC_CH + ((i8 - 128) / 9) * UC_COPY + ((i8 - 128) % 9) * 8) = (u32x2){0, 0};
        }
        const u32 cb = (c % 3) * CHUNK;
#pragma unroll
        for (int ks = 0; ks < 4; ++ks) {
            const u32x4 xf = LD(u32x4, cb + xa[ks]);
            aqk = mfma32(trd2(cb + wa + ks * 3072, cb + wa + ks * 3072 + 256), xf, aqk);
            if (ROLE == 0) av = mfma32(xf, trd2(cb + wv + ks * 3072, cb + wv + ks * 3072 + 256), av);
            else {
                const u32x4 gw = LD(u32x4, L_GW + (c * 4 + ks) * 32 + h * 16);
#pragma unroll
                for (int j = 0; j < 4; ++j)
                    gacc = dot2(xf[j], gw[j], gacc);
                asm volatile("" : "+v"(gacc));
            }
        }
    }
    STAMP(4);
    const u32 tile = (qb * 4 + tb) * 2048;
    {
        const u32x4 kf = {pk2(aqk[8], aqk[9]), pk2(aqk[10], aqk[11]), pk2(aqk[12], aqk[13]), pk2(aqk[14], aqk[15])};
        const int pr = (r & 19) | ((r & 4) << 1) | ((r & 8) >> 1);
        __builtin_amdgcn_raw_buffer_store_b128(kf, wsr, WS_KV + (hp * 2 + ROLE) * 131072 + tile + (h * 32 + pr) * 16, 0, SC1);
    }
    if (ROLE == 0) {
#pragma unroll
        for (int s = 0; s < 2; ++s) {
            const u32x4 vf = {pk2(av[8 * s], av[8 * s + 1]), pk2(av[8 * s + 2], av[8 * s + 3]), pk2(av[8 * s + 4], av[8 * s + 5]), pk2(av[8 * s + 6], av[8 * s + 7])};
            __builtin_amdgcn_raw_buffer_store_b128(vf, wsr, WS_KV + (hp * 2 + (r >> 4)) * 131072 + tile + 1024 + s * 512 + h * 256 + (r & 15) * 16, 0, SC1);
        }
    }
    float qn = 0.f, kn = 0.f;
#pragma unroll
    for (int i = 0; i < 8; ++i) { aqk[i] *= QSCALE; qn = fmaf(aqk[i], aqk[i], qn); kn = fmaf(aqk[8 + i], aqk[8 + i], kn); }
    ST(u32x4, L_QX + (ROLE * 4 + tb) * 1024 + lane * 16) = (u32x4){pk2(aqk[0], aqk[1]), pk2(aqk[2], aqk[3]), pk2(aqk[4], aqk[5]), pk2(aqk[6], aqk[7])};
    qn = halves_sum(qn); kn = halves_sum(kn);
    kn = dpp_max<0x111>(kn); kn = dpp_max<0x112>(kn); kn = dpp_max<0x114>(kn); kn = dpp_max<0x118>(kn); kn = dpp_max<0x142>(kn);
    if (h == 0) ST(float, L_QN + (ROLE * 128 + tb * 32 + r) * 4) = qn;
    if (lane == 31) ST(float, L_KN + (ROLE * 4 + tb) * 4) = kn;
    if (ROLE == 1) {
        const float s = halves_sum(gacc);
        if (h == 0) ST(float, L_G + (tb * 32 + r) * 4) = 1.f / (1.f + __expf(-s));
    }
    asm volatile("s_waitcnt vmcnt(%4)" : "+v"(ug[0]), "+v"(ug[1]), "+v"(ug[2]), "+v"(ug[3]) : "i"(ROLE ? 1 : 3));
#pragma unroll
    for (int i = 0; i < 4; ++i) {
        const u32 ua = L_UC + (w * 256 + i * 64 + lane + 36) * 2;
#pragma unroll
        for (int c = 0; c < 4; ++c) {
            ST(unsigned short, ua + c * (UC_COPY - 2)) = (unsigned short)ug[i];
            ST(unsigned short, ua + c * (UC_COPY - 2) + UC_CH) = (unsigned short)(ug[i] >> 16);
        }
    }
}

__device__ __forceinline__ void conv_wave(__amdgpu_buffer_rsrc_t wsr, char* ws, int lane, int tb, int d0, u64 tag) {
    const int ch = tb & 1, mh = tb >> 1, r = lane & 31, h = lane >> 5, c = (r + 1) & 3;
    u32 ua = L_UC + ch * UC_CH + c * UC_COPY + (mh * 512 + (r + 1 - c) + 8 * h + 4) * 2;
    u32 ka = L_KB + ch * KB_CH + (32 + r - mh * 16) * 80 + h * 16;
    f32x16 y0 = {}, y1 = {};
#pragma unroll 2
    for (int m = 0; m < 16; ++m) {
#pragma unroll
        for (int ks = 0; ks < 2; ++ks) {
            const u32x2 a0 = LD(u32x2, ua + ks * 32), a1 = LD(u32x2, ua + ks * 32 + 8);
            const u32x2 c0 = LD(u32x2, ua + ks * 32 + 2048), c1 = LD(u32x2, ua + ks * 32 + 2056);
            const u32x4 b0 = LD(u32x4, ka + ks * 32), b1 = LD(u32x4, ka + ks * 32 + 2560);
            const u32x4 lo = {a0.x, a0.y, a1.x, a1.y}, hi = {c0.x, c0.y, c1.x, c1.y};
            y0 = mfma32(lo, b0, y0);
            y1 = mfma32(lo, b1, y1);
            y1 = mfma32(hi, b0, y1);
        }
        ua += 64; ka -= 80;
    }
    const f32x16 give = mh ? y0 : y1;
    f32x16 y = mh ? y1 : y0;
#pragma unroll
    for (int i = 0; i < 4; ++i) ST(f32x4, L_CX + tb * 4096 + i * 1024 + lane * 16) = (f32x4){give[4 * i], give[4 * i + 1], give[4 * i + 2], give[4 * i + 3]};
    asm volatile("s_waitcnt lgkmcnt(0)" ::: "memory");
    if (lane == 0) __hip_atomic_store((LAS u32*)(L_SYNC + tb * 4), 1u, __ATOMIC_RELAXED, __HIP_MEMORY_SCOPE_WORKGROUP);
    while (__hip_atomic_load((LAS u32*)(L_SYNC + (tb ^ 2) * 4), __ATOMIC_RELAXED, __HIP_MEMORY_SCOPE_WORKGROUP) == 0) __builtin_amdgcn_s_sleep(1);
    asm volatile("" ::: "memory");
#pragma unroll
    for (int i = 0; i < 4; ++i) {
        const f32x4 o = LD(f32x4, L_CX + (tb ^ 2) * 4096 + i * 1024 + lane * 16);
#pragma unroll
        for (int j = 0; j < 4; ++j) y[4 * i + j] += o[j];
    }
    asm volatile("s_waitcnt lgkmcnt(0)" : "+v"(y) :: "memory");
    if (lane == 0) __hip_atomic_fetch_add((LAS u32*)(L_SYNC + 16), 1u, __ATOMIC_RELAXED, __HIP_MEMORY_SCOPE_WORKGROUP);
    const u32 yo = WS_YT + ((d0 + ch) * 2048 + mh * 1024 + r * 32 + h * 8) * 2;
#pragma unroll
    for (int i = 0; i < 2; ++i)
        __builtin_amdgcn_raw_buffer_store_b128(widen(pk2(y[8 * i], y[8 * i + 1]), pk2(y[8 * i + 2], y[8 * i + 3]), pk2(y[8 * i + 4], y[8 * i + 5]), pk2(y[8 * i + 6], y[8 * i + 7])),
                                               wsr, yo + i * 32, 0, SC1);
    asm volatile("s_waitcnt vmcnt(0)" ::: "memory");
    if (lane == 0) __hip_atomic_store((GAS u64*)(ws + WS_YFLAG + ((d0 + ch) * 2 + mh) * 64), tag << 16, RLX_AGENT);
}

template <int PH>
__global__ __launch_bounds__(512, 2) void s4_mha_mix(Params p) {
    __shared__ __attribute__((aligned(16))) char smem[LDS_BYTES];
    constexpr bool FUSED = PH == 7;
    asm volatile("" :: "s"((u32)(uintptr_t)smem));
    const int lane = threadIdx.x & 63, w = __builtin_amdgcn_readfirstlane(threadIdx.x >> 6);
    const int b = blockIdx.x, xg = b & 7, jb = b >> 3;
    const int hp = 8 * (xg & 1) + (jb & 7), qb = 4 * (xg >> 1) + (jb >> 3), d0 = 64 * xg + 2 * jb;
    const int tb = w & 3, a = w >> 2, r = lane & 31, h = lane >> 5;
    char* ws = p.ws;
    const __amdgpu_buffer_rsrc_t wsr = __builtin_amdgcn_make_buffer_rsrc(ws, 0, WS_END, 0x00020000);
    STAMP(0);
    u64 nonce = 0;
    const int cb64 = hp & 7, rb64 = 2 * qb + (hp >> 3);

    if (PH & 1) {
        if (a == 0) phase1<0>(p, wsr, ws, lane, w, hp, qb, d0, nonce STAMP_PASS); else phase1<1>(p, wsr, ws, lane, w, hp, qb, d0, nonce STAMP_PASS);
        BAR(0);
        STAMP(5);
    }
    const u64 tag = (nonce + 1) & 0xffffffffffffull;
    if (PH & 1) {
        if (threadIdx.x < 2) {
            const f32x4 k4 = LD(f32x4, L_KN + lane * 16);
            const u32 kb = (__float_as_uint(fmaxf(fmaxf(k4.x, k4.y), fmaxf(k4.z, k4.w))) >> 16) + 1;
            __hip_atomic_store((GAS u64*)(ws + WS_KVFLAG + (hp * 16 + qb) * 128 + lane * 8), (tag << 16) | min(kb, 0x7fc0u), RLX_AGENT);
        }
        if (!FUSED) {
            for (int i = threadIdx.x; i < (8192 + 2560) / 16; i += 512)
                *(GAS u32x4*)(ws + WS_QF + b * 12288 + i * 16) = LD(u32x4, L_QX + i * 16);
        }
    }
    if (!FUSED && !(PH & 1)) {
        for (int i = threadIdx.x; i < (8192 + 2560) / 16; i += 512)
            ST(u32x4, L_QX + i * 16) = *(const GAS u32x4*)(ws + WS_QF + b * 12288 + i * 16);
        if (threadIdx.x == 0) ST(u32, L_SYNC + 16) = 4;
        __syncthreads();
    }
    if (FUSED || PH == 4) {
        const u32 vo = (lane >> 3) * 1024 + (((lane & 7) ^ ((lane >> 2) & 4)) << 4);
#pragma unroll
        for (int i = 0; i < 2; ++i) dma4(vo, vo + 7168, vo + 14336, vo + 21504, p.wo + ((w * 64 + i * 32) * 1024 + cb64 * 128), L_WO + (w * 64 + i * 32) * 128);
    }
    if ((PH & 1) && a == 1) { conv_wave(wsr, ws, lane, tb, d0, tag); STAMP(6); }

    if (PH & 2) {
        u32x4 q0 = LD(u32x4, L_QX + (a * 4 + tb) * 1024 + lane * 16), q1 = LD(u32x4, L_QX + (a * 4 + ((tb + 1) & 3)) * 1024 + lane * 16);
        u32x4 q2 = LD(u32x4, L_QX + (a * 4 + ((tb + 2) & 3)) * 1024 + lane * 16), q3 = LD(u32x4, L_QX + (a * 4 + ((tb + 3) & 3)) * 1024 + lane * 16);
        const f32x2 qn2 = LD(f32x2, L_QN + a * 512 + lane * 8);
        f32x16 o0 = {}, o1 = {}, o2 = {}, o3 = {};
        float m0 = 0.f, m1 = 0.f, m2 = 0.f, m3 = 0.f;
        const u32 kvo = WS_KV + (hp * 2 + a) * 131072 + tb * 32768;
        const u64 fl = poll_tag<FUSED>(ws + WS_KVFLAG + (hp * 16 + tb * 4 + (lane & 3)) * 128 + a * 8, lane < 4, tag);
        STAMP(7);
        u32 kb = lane < 4 ? (u32)fl & 0xffffu : 0u;
        kb = max(max(__builtin_amdgcn_readlane(kb, 0), __builtin_amdgcn_readlane(kb, 1)), max(__builtin_amdgcn_readlane(kb, 2), __builtin_amdgcn_readlane(kb, 3)));
        const bool safe = __all(fmaxf(qn2.x, qn2.y) * __uint_as_float(kb << 16) <= SAFE2);
        if (__builtin_expect(safe, 1)) {
            const u32 one = 0x3f803f80u;
            u32x4 ones = {one, one, one, one};
            u32x8 sb_hi = {};
            u64 base = (u64)(uintptr_t)(ws + kvo);
            asm volatile(
                "v_mov_b32 v116, v156\nv_mov_b32 v117, v156\nv_mov_b32 v118, v156\nv_mov_b32 v119, v156\n"
                "v_mov_b32 v120, v156\nv_mov_b32 v121, v156\nv_mov_b32 v122, v156\nv_mov_b32 v123, v156\n"
                "v_mov_b32 v128, v156\nv_mov_b32 v129, v156\nv_mov_b32 v130, v156\nv_mov_b32 v131, v156\n"
                "v_mov_b32 v132, v156\nv_mov_b32 v133, v156\nv_mov_b32 v134, v156\nv_mov_b32 v135, v156\n"
                "v_mov_b32 v140, v156\nv_mov_b32 v141, v156\nv_mov_b32 v142, v156\nv_mov_b32 v143, v156\n"
                "v_mov_b32 v144, v156\nv_mov_b32 v145, v156\nv_mov_b32 v146, v156\nv_mov_b32 v147, v156\n"
                "v_mov_b32 v152, v156\nv_mov_b32 v153, v156\nv_mov_b32 v154, v156\nv_mov_b32 v155, v156\n"
    "s_add_u32 s40, s40, 4096\n"
    "s_addc_u32 s41, s41, 0\n"
    "global_load_dwordx4 v[112:115], v161, s[40:41] offset:-4096 sc1\n"
    "s_mov_b64 exec, s[42:43]\n"
    "global_load_dwordx4 v[116:119], v162, s[40:41] offset:-3072 sc1\n"
    "global_load_dwordx4 v[120:123], v162, s[40:41] offset:-2560 sc1\n"
    "s_mov_b64 exec, -1\n"
    "global_load_dwordx4 v[124:127], v161, s[40:41] offset:-2048 sc1\n"
    "s_mov_b64 exec, s[42:43]\n"
    "global_load_dwordx4 v[128:131], v162, s[40:41] offset:-1024 sc1\n"
    "global_load_dwordx4 v[132:135], v162, s[40:41] offset:-512 sc1\n"
    "s_mov_b64 exec, -1\n"
    "global_load_dwordx4 v[136:139], v161, s[40:41] offset:0 sc1\n"
    "s_mov_b64 exec, s[42:43]\n"
    "global_load_dwordx4 v[140:143], v162, s[40:41] offset:1024 sc1\n"
    "global_load_dwordx4 v[144:147], v162, s[40:41] offset:1536 sc1\n"
    "s_mov_b64 exec, -1\n"
    "s_add_u32 s40, s40, 6144\n"
    "s_addc_u32 s41, s41, 0\n"
    "s_mov_b32 s44, 3\n"
    "s_waitcnt vmcnt(6)\n"
    "v_mfma_f32_32x32x16_bf16 v[80:95], v[112:115], v[64:67], 0\n"
    "s_nop 11\n"
    ".p2align 3\n"
    "1:\n"
    "v_exp_f32 v80, v80\n"
    "v_exp_f32 v81, v81\n"
    "v_smfmac_f32_32x32x32_bf16 v[48:63], v[156:159], v[104:111], v160\n"
    "global_load_dwordx4 v[148:151], v161, s[40:41] offset:-4096 sc1\n"
    "s_mov_b64 exec, s[42:43]\n"
    "global_load_dwordx4 v[152:155], v162, s[40:41] offset:-3072 sc1\n"
    "global_load_dwordx4 v[156:159], v162, s[40:41] offset:-2560 sc1\n"
    "s_mov_b64 exec, -1\n"
    "v_exp_f32 v82, v82\n"
    "v_exp_f32 v83, v83\n"
    "v_exp_f32 v84, v84\n"
    "v_mfma_f32_32x32x16_bf16 v[96:111], v[112:115], v[68:71], 0\n"
    "v_exp_f32 v85, v85\n"
    "v_exp_f32 v86, v86\n"
    "v_exp_f32 v87, v87\n"
    "v_exp_f32 v88, v88\n"
    "v_exp_f32 v89, v89\n"
    "v_smfmac_f32_32x32x32_bf16 v[0:15], v[116:119], v[80:87], v160\n"
    "v_exp_f32 v90, v90\n"
    "v_exp_f32 v91, v91\n"
    "v_exp_f32 v92, v92\n"
    "v_exp_f32 v93, v93\n"
    "v_exp_f32 v94, v94\n"
    "v_exp_f32 v95, v95\n"
    "v_exp_f32 v96, v96\n"
    "v_exp_f32 v97, v97\n"
    "v_smfmac_f32_32x32x32_bf16 v[0:15], v[120:123], v[88:95], v160\n"
    "v_exp_f32 v98, v98\n"
    "v_exp_f32 v99, v99\n"
    "v_exp_f32 v100, v100\n"
    "v_mfma_f32_32x32x16_bf16 v[80:95], v[112:115], v[72:75], 0\n"
    "v_exp_f32 v101, v101\n"
    "v_exp_f32 v102, v102\n"
    "v_exp_f32 v103, v103\n"
    "v_exp_f32 v104, v104\n"
    "v_exp_f32 v105, v105\n"
    "v_smfmac_f32_32x32x32_bf16 v[16:31], v[116:119], v[96:103], v160\n"
    "v_exp_f32 v106, v106\n"
    "v_exp_f32 v107, v107\n"
    "v_exp_f32 v108, v108\n"
    "v_exp_f32 v109, v109\n"
    "v_exp_f32 v110, v110\n"
    "v_exp_f32 v111, v111\n"
    "v_exp_f32 v80, v80\n"
    "v_exp_f32 v81, v81\n"
    "v_smfmac_f32_32x32x32_bf16 v[16:31], v[120:123], v[104:111], v160\n"
    "v_exp_f32 v82, v82\n"
    "v_exp_f32 v83, v83\n"
    "v_exp_f32 v84, v84\n"
    "v_mfma_f32_32x32x16_bf16 v[96:111], v[112:115], v[76:79], 0\n"
    "v_exp_f32 v85, v85\n"
    "v_exp_f32 v86, v86\n"
    "v_exp_f32 v87, v87\n"
    "v_exp_f32 v88, v88\n"
    "v_exp_f32 v89, v89\n"
    "v_smfmac_f32_32x32x32_bf16 v[32:47], v[116:119], v[80:87], v160\n"
    "v_exp_f32 v90, v90\n"
    "v_exp_f32 v91, v91\n"
    "v_exp_f32 v92, v92\n"
    "v_exp_f32 v93, v93\n"
    "v_exp_f32 v94, v94\n"
    "v_exp_f32 v95, v95\n"
    "v_exp_f32 v96, v96\n"
    "v_exp_f32 v97, v97\n"
    "v_smfmac_f32_32x32x32_bf16 v[32:47], v[120:123], v[88:95], v160\n"
    "v_exp_f32 v98, v98\n"
    "v_exp_f32 v99, v99\n"
    "v_exp_f32 v100, v100\n"
    "s_waitcnt vmcnt(6)\n"
    "v_mfma_f32_32x32x16_bf16 v[80:95], v[124:127], v[64:67], 0\n"
    "v_exp_f32 v101, v101\n"
    "v_exp_f32 v102, v102\n"
    "v_exp_f32 v103, v103\n"
    "v_exp_f32 v104, v104\n"
    "v_exp_f32 v105, v105\n"
    "v_smfmac_f32_32x32x32_bf16 v[48:63], v[116:119], v[96:103], v160\n"
    "v_exp_f32 v106, v106\n"
    "v_exp_f32 v107, v107\n"
    "v_exp_f32 v108, v108\n"
    "v_exp_f32 v109, v109\n"
    "v_exp_f32 v110, v110\n"
    "v_exp_f32 v111, v111\n"
    "v_exp_f32 v80, v80\n"
    "v_exp_f32 v81, v81\n"
    "v_smfmac_f32_32x32x32_bf16 v[48:63], v[120:123], v[104:111], v160\n"
    "global_load_dwordx4 v[112:115], v161, s[40:41] offset:-2048 sc1\n"
    "s_mov_b64 exec, s[42:43]\n"
    "global_load_dwordx4 v[116:119], v162, s[40:41] offset:-1024 sc1\n"
    "global_load_dwordx4 v[120:123], v162, s[40:41] offset:-512 sc1\n"
    "s_mov_b64 exec, -1\n"
    "v_exp_f32 v82, v82\n"
    "v_exp_f32 v83, v83\n"
    "v_exp_f32 v84, v84\n"
    "v_mfma_f32_32x32x16_bf16 v[96:111], v[124:127], v[68:71], 0\n"
    "v_exp_f32 v85, v85\n"
    "v_exp_f32 v86, v86\n"
    "v_exp_f32 v87, v87\n"
    "v_exp_f32 v88, v88\n"
    "v_exp_f32 v89, v89\n"
    "v_smfmac_f32_32x32x32_bf16 v[0:15], v[128:131], v[80:87], v160\n"
    "v_exp_f32 v90, v90\n"
    "v_exp_f32 v91, v91\n"
    "v_exp_f32 v92, v92\n"
    "v_exp_f32 v93, v93\n"
    "v_exp_f32 v94, v94\n"
    "v_exp_f32 v95, v95\n"
    "v_exp_f32 v96, v96\n"
    "v_exp_f32 v97, v97\n"
    "v_smfmac_f32_32x32x32_bf16 v[0:15], v[132:135], v[88:95], v160\n"
    "v_exp_f32 v98, v98\n"
    "v_exp_f32 v99, v99\n"
    "v_exp_f32 v100, v100\n"
    "v_mfma_f32_32x32x16_bf16 v[80:95], v[124:127], v[72:75], 0\n"
    "v_exp_f32 v101, v101\n"
    "v_exp_f32 v102, v102\n"
    "v_exp_f32 v103, v103\n"
    "v_exp_f32 v104, v104\n"
    "v_exp_f32 v105, v105\n"
    "v_smfmac_f32_32x32x32_bf16 v[16:31], v[128:131], v[96:103], v160\n"
    "v_exp_f32 v106, v106\n"
    "v_exp_f32 v107, v107\n"
    "v_exp_f32 v108, v108\n"
    "v_exp_f32 v109, v109\n"
    "v_exp_f32 v110, v110\n"
    "v_exp_f32 v111, v111\n"
    "v_exp_f32 v80, v80\n"
    "v_exp_f32 v81, v81\n"
    "v_smfmac_f32_32x32x32_bf16 v[16:31], v[132:135], v[104:111], v160\n"
    "v_exp_f32 v82, v82\n"
    "v_exp_f32 v83, v83\n"
    "v_exp_f32 v84, v84\n"
    "v_mfma_f32_32x32x16_bf16 v[96:111], v[124:127], v[76:79], 0\n"
    "v_exp_f32 v85, v85\n"
    "v_exp_f32 v86, v86\n"
    "v_exp_f32 v87, v87\n"
    "v_exp_f32 v88, v88\n"
    "v_exp_f32 v89, v89\n"
    "v_smfmac_f32_32x32x32_bf16 v[32:47], v[128:131], v[80:87], v160\n"
    "v_exp_f32 v90, v90\n"
    "v_exp_f32 v91, v91\n"
    "v_exp_f32 v92, v92\n"
    "v_exp_f32 v93, v93\n"
    "v_exp_f32 v94, v94\n"
    "v_exp_f32 v95, v95\n"
    "v_exp_f32 v96, v96\n"
    "v_exp_f32 v97, v97\n"
    "v_smfmac_f32_32x32x32_bf16 v[32:47], v[132:135], v[88:95], v160\n"
    "v_exp_f32 v98, v98\n"
    "v_exp_f32 v99, v99\n"
    "v_exp_f32 v100, v100\n"
    "s_waitcnt vmcnt(6)\n"
    "v_mfma_f32_32x32x16_bf16 v[80:95], v[136:139], v[64:67], 0\n"
    "v_exp_f32 v101, v101\n"
    "v_exp_f32 v102, v102\n"
    "v_exp_f32 v103, v103\n"
    "v_exp_f32 v104, v104\n"
    "v_exp_f32 v105, v105\n"
    "v_smfmac_f32_32x32x32_bf16 v[48:63], v[128:131], v[96:103], v160\n"
    "v_exp_f32 v106, v106\n"
    "v_exp_f32 v107, v107\n"
    "v_exp_f32 v108, v108\n"
    "v_exp_f32 v109, v109\n"
    "v_exp_f32 v110, v110\n"
    "v_exp_f32 v111, v111\n"
    "v_exp_f32 v80, v80\n"
    "v_exp_f32 v81, v81\n"
    "v_smfmac_f32_32x32x32_bf16 v[48:63], v[132:135], v[104:111], v160\n"
    "global_load_dwordx4 v[124:127], v161, s[40:41] offset:0 sc1\n"
    "s_mov_b64 exec, s[42:43]\n"
    "global_load_dwordx4 v[128:131], v162, s[40:41] offset:1024 sc1\n"
    "global_load_dwordx4 v[132:135], v162, s[40:41] offset:1536 sc1\n"
    "s_mov_b64 exec, -1\n"
    "v_exp_f32 v82, v82\n"
    "v_exp_f32 v83, v83\n"
    "v_exp_f32 v84, v84\n"
    "v_mfma_f32_32x32x16_bf16 v[96:111], v[136:139], v[68:71], 0\n"
    "v_exp_f32 v85, v85\n"
    "v_exp_f32 v86, v86\n"
    "v_exp_f32 v87, v87\n"
    "v_exp_f32 v88, v88\n"
    "v_exp_f32 v89, v89\n"
    "v_smfmac_f32_32x32x32_bf16 v[0:15], v[140:143], v[80:87], v160\n"
    "v_exp_f32 v90, v90\n"
    "v_exp_f32 v91, v91\n"
    "v_exp_f32 v92, v92\n"
    "v_exp_f32 v93, v93\n"
    "v_exp_f32 v94, v94\n"
    "v_exp_f32 v95, v95\n"
    "v_exp_f32 v96, v96\n"
    "v_exp_f32 v97, v97\n"
    "v_smfmac_f32_32x32x32_bf16 v[0:15], v[144:147], v[88:95], v160\n"
    "v_exp_f32 v98, v98\n"
    "v_exp_f32 v99, v99\n"
    "v_exp_f32 v100, v100\n"
    "v_mfma_f32_32x32x16_bf16 v[80:95], v[136:139], v[72:75], 0\n"
    "v_exp_f32 v101, v101\n"
    "v_exp_f32 v102, v102\n"
    "v_exp_f32 v103, v103\n"
    "v_exp_f32 v104, v104\n"
    "v_exp_f32 v105, v105\n"
    "v_smfmac_f32_32x32x32_bf16 v[16:31], v[140:143], v[96:103], v160\n"
    "v_exp_f32 v106, v106\n"
    "v_exp_f32 v107, v107\n"
    "v_exp_f32 v108, v108\n"
    "v_exp_f32 v109, v109\n"
    "v_exp_f32 v110, v110\n"
    "v_exp_f32 v111, v111\n"
    "v_exp_f32 v80, v80\n"
    "v_exp_f32 v81, v81\n"
    "v_smfmac_f32_32x32x32_bf16 v[16:31], v[144:147], v[104:111], v160\n"
    "v_exp_f32 v82, v82\n"
    "v_exp_f32 v83, v83\n"
    "v_exp_f32 v84, v84\n"
    "v_mfma_f32_32x32x16_bf16 v[96:111], v[136:139], v[76:79], 0\n"
    "v_exp_f32 v85, v85\n"
    "v_exp_f32 v86, v86\n"
    "v_exp_f32 v87, v87\n"
    "v_exp_f32 v88, v88\n"
    "v_exp_f32 v89, v89\n"
    "v_smfmac_f32_32x32x32_bf16 v[32:47], v[140:143], v[80:87], v160\n"
    "v_exp_f32 v90, v90\n"
    "v_exp_f32 v91, v91\n"
    "v_exp_f32 v92, v92\n"
    "v_exp_f32 v93, v93\n"
    "v_exp_f32 v94, v94\n"
    "v_exp_f32 v95, v95\n"
    "v_exp_f32 v96, v96\n"
    "v_exp_f32 v97, v97\n"
    "v_smfmac_f32_32x32x32_bf16 v[32:47], v[144:147], v[88:95], v160\n"
    "v_exp_f32 v98, v98\n"
    "v_exp_f32 v99, v99\n"
    "v_exp_f32 v100, v100\n"
    "s_waitcnt vmcnt(6)\n"
    "v_mfma_f32_32x32x16_bf16 v[80:95], v[148:151], v[64:67], 0\n"
    "v_exp_f32 v101, v101\n"
    "v_exp_f32 v102, v102\n"
    "v_exp_f32 v103, v103\n"
    "v_exp_f32 v104, v104\n"
    "v_exp_f32 v105, v105\n"
    "v_smfmac_f32_32x32x32_bf16 v[48:63], v[140:143], v[96:103], v160\n"
    "v_exp_f32 v106, v106\n"
    "v_exp_f32 v107, v107\n"
    "v_exp_f32 v108, v108\n"
    "v_exp_f32 v109, v109\n"
    "v_exp_f32 v110, v110\n"
    "v_exp_f32 v111, v111\n"
    "v_exp_f32 v80, v80\n"
    "v_exp_f32 v81, v81\n"
    "v_smfmac_f32_32x32x32_bf16 v[48:63], v[144:147], v[104:111], v160\n"
    "global_load_dwordx4 v[136:139], v161, s[40:41] offset:2048 sc1\n"
    "s_mov_b64 exec, s[42:43]\n"
    "global_load_dwordx4 v[140:143], v162, s[40:41] offset:3072 sc1\n"
    "global_load_dwordx4 v[144:147], v162, s[40:41] offset:3584 sc1\n"
    "s_mov_b64 exec, -1\n"
    "v_exp_f32 v82, v82\n"
    "v_exp_f32 v83, v83\n"
    "v_exp_f32 v84, v84\n"
    "v_mfma_f32_32x32x16_bf16 v[96:111], v[148:151], v[68:71], 0\n"
    "v_exp_f32 v85, v85\n"
    "v_exp_f32 v86, v86\n"
    "v_exp_f32 v87, v87\n"
    "v_exp_f32 v88, v88\n"
    "v_exp_f32 v89, v89\n"
    "v_smfmac_f32_32x32x32_bf16 v[0:15], v[152:155], v[80:87], v160\n"
    "v_exp_f32 v90, v90\n"
    "v_exp_f32 v91, v91\n"
    "v_exp_f32 v92, v92\n"
    "v_exp_f32 v93, v93\n"
    "v_exp_f32 v94, v94\n"
    "v_exp_f32 v95, v95\n"
    "v_exp_f32 v96, v96\n"
    "v_exp_f32 v97, v97\n"
    "v_smfmac_f32_32x32x32_bf16 v[0:15], v[156:159], v[88:95], v160\n"
    "v_exp_f32 v98, v98\n"
    "v_exp_f32 v99, v99\n"
    "v_exp_f32 v100, v100\n"
    "v_mfma_f32_32x32x16_bf16 v[80:95], v[148:151], v[72:75], 0\n"
    "v_exp_f32 v101, v101\n"
    "v_exp_f32 v102, v102\n"
    "v_exp_f32 v103, v103\n"
    "v_exp_f32 v104, v104\n"
    "v_exp_f32 v105, v105\n"
    "v_smfmac_f32_32x32x32_bf16 v[16:31], v[152:155], v[96:103], v160\n"
    "v_exp_f32 v106, v106\n"
    "v_exp_f32 v107, v107\n"
    "v_exp_f32 v108, v108\n"
    "v_exp_f32 v109, v109\n"
    "v_exp_f32 v110, v110\n"
    "v_exp_f32 v111, v111\n"
    "v_exp_f32 v80, v80\n"
    "v_exp_f32 v81, v81\n"
    "v_smfmac_f32_32x32x32_bf16 v[16:31], v[156:159], v[104:111], v160\n"
    "v_exp_f32 v82, v82\n"
    "v_exp_f32 v83, v83\n"
    "v_exp_f32 v84, v84\n"
    "v_mfma_f32_32x32x16_bf16 v[96:111], v[148:151], v[76:79], 0\n"
    "v_exp_f32 v85, v85\n"
    "v_exp_f32 v86, v86\n"
    "v_exp_f32 v87, v87\n"
    "v_exp_f32 v88, v88\n"
    "v_exp_f32 v89, v89\n"
    "v_smfmac_f32_32x32x32_bf16 v[32:47], v[152:155], v[80:87], v160\n"
    "v_exp_f32 v90, v90\n"
    "v_exp_f32 v91, v91\n"
    "v_exp_f32 v92, v92\n"
    "v_exp_f32 v93, v93\n"
    "v_exp_f32 v94, v94\n"
    "v_exp_f32 v95, v95\n"
    "v_exp_f32 v96, v96\n"
    "v_exp_f32 v97, v97\n"
    "v_smfmac_f32_32x32x32_bf16 v[32:47], v[156:159], v[88:95], v160\n"
    "v_exp_f32 v98, v98\n"
    "v_exp_f32 v99, v99\n"
    "v_exp_f32 v100, v100\n"
    "s_waitcnt vmcnt(6)\n"
    "v_mfma_f32_32x32x16_bf16 v[80:95], v[112:115], v[64:67], 0\n"
    "v_exp_f32 v101, v101\n"
    "v_exp_f32 v102, v102\n"
    "v_exp_f32 v103, v103\n"
    "v_exp_f32 v104, v104\n"
    "v_exp_f32 v105, v105\n"
    "v_smfmac_f32_32x32x32_bf16 v[48:63], v[152:155], v[96:103], v160\n"
    "v_exp_f32 v106, v106\n"
    "v_exp_f32 v107, v107\n"
    "v_exp_f32 v108, v108\n"
    "v_exp_f32 v109, v109\n"
    "v_exp_f32 v110, v110\n"
    "v_exp_f32 v111, v111\n"
    "s_add_u32 s40, s40, 8192\n"
    "s_addc_u32 s41, s41, 0\n"
    "s_sub_u32 s44, s44, 1\n"
    "s_cmp_lg_u32 s44, 0\n"
    "s_cbranch_scc1 1b\n"
    "v_exp_f32 v80, v80\n"
    "v_exp_f32 v81, v81\n"
    "v_smfmac_f32_32x32x32_bf16 v[48:63], v[156:159], v[104:111], v160\n"
    "global_load_dwordx4 v[148:151], v161, s[40:41] offset:-4096 sc1\n"
    "s_mov_b64 exec, s[42:43]\n"
    "global_load_dwordx4 v[152:155], v162, s[40:41] offset:-3072 sc1\n"
    "global_load_dwordx4 v[156:159], v162, s[40:41] offset:-2560 sc1\n"
    "s_mov_b64 exec, -1\n"
    "v_exp_f32 v82, v82\n"
    "v_exp_f32 v83, v83\n"
    "v_exp_f32 v84, v84\n"
    "v_mfma_f32_32x32x16_bf16 v[96:111], v[112:115], v[68:71], 0\n"
    "v_exp_f32 v85, v85\n"
    "v_exp_f32 v86, v86\n"
    "v_exp_f32 v87, v87\n"
    "v_exp_f32 v88, v88\n"
    "v_exp_f32 v89, v89\n"
    "v_smfmac_f32_32x32x32_bf16 v[0:15], v[116:119], v[80:87], v160\n"
    "v_exp_f32 v90, v90\n"
    "v_exp_f32 v91, v91\n"
    "v_exp_f32 v92, v92\n"
    "v_exp_f32 v93, v93\n"
    "v_exp_f32 v94, v94\n"
    "v_exp_f32 v95, v95\n"
    "v_exp_f32 v96, v96\n"
    "v_exp_f32 v97, v97\n"
    "v_smfmac_f32_32x32x32_bf16 v[0:15], v[120:123], v[88:95], v160\n"
    "v_exp_f32 v98, v98\n"
    "v_exp_f32 v99, v99\n"
    "v_exp_f32 v100, v100\n"
    "v_mfma_f32_32x32x16_bf16 v[80:95], v[112:115], v[72:75], 0\n"
    "v_exp_f32 v101, v101\n"
    "v_exp_f32 v102, v102\n"
    "v_exp_f32 v103, v103\n"
    "v_exp_f32 v104, v104\n"
    "v_exp_f32 v105, v105\n"
    "v_smfmac_f32_32x32x32_bf16 v[16:31], v[116:119], v[96:103], v160\n"
    "v_exp_f32 v106, v106\n"
    "v_exp_f32 v107, v107\n"
    "v_exp_f32 v108, v108\n"
    "v_exp_f32 v109, v109\n"
    "v_exp_f32 v110, v110\n"
    "v_exp_f32 v111, v111\n"
    "v_exp_f32 v80, v80\n"
    "v_exp_f32 v81, v81\n"
    "v_smfmac_f32_32x32x32_bf16 v[16:31], v[120:123], v[104:111], v160\n"
    "v_exp_f32 v82, v82\n"
    "v_exp_f32 v83, v83\n"
    "v_exp_f32 v84, v84\n"
    "v_mfma_f32_32x32x16_bf16 v[96:111], v[112:115], v[76:79], 0\n"
    "v_exp_f32 v85, v85\n"
    "v_exp_f32 v86, v86\n"
    "v_exp_f32 v87, v87\n"
    "v_exp_f32 v88, v88\n"
    "v_exp_f32 v89, v89\n"
    "v_smfmac_f32_32x32x32_bf16 v[32:47], v[116:119], v[80:87], v160\n"
    "v_exp_f32 v90, v90\n"
    "v_exp_f32 v91, v91\n"
    "v_exp_f32 v92, v92\n"
    "v_exp_f32 v93, v93\n"
    "v_exp_f32 v94, v94\n"
    "v_exp_f32 v95, v95\n"
    "v_exp_f32 v96, v96\n"
    "v_exp_f32 v97, v97\n"
    "v_smfmac_f32_32x32x32_bf16 v[32:47], v[120:123], v[88:95], v160\n"
    "v_exp_f32 v98, v98\n"
    "v_exp_f32 v99, v99\n"
    "v_exp_f32 v100, v100\n"
    "s_waitcnt vmcnt(6)\n"
    "v_mfma_f32_32x32x16_bf16 v[80:95], v[124:127], v[64:67], 0\n"
    "v_exp_f32 v101, v101\n"
    "v_exp_f32 v102, v102\n"
    "v_exp_f32 v103, v103\n"
    "v_exp_f32 v104, v104\n"
    "v_exp_f32 v105, v105\n"
    "v_smfmac_f32_32x32x32_bf16 v[48:63], v[116:119], v[96:103], v160\n"
    "v_exp_f32 v106, v106\n"
    "v_exp_f32 v107, v107\n"
    "v_exp_f32 v108, v108\n"
    "v_exp_f32 v109, v109\n"
    "v_exp_f32 v110, v110\n"
    "v_exp_f32 v111, v111\n"
    "v_exp_f32 v80, v80\n"
    "v_exp_f32 v81, v81\n"
    "v_smfmac_f32_32x32x32_bf16 v[48:63], v[120:123], v[104:111], v160\n"
    "v_exp_f32 v82, v82\n"
    "v_exp_f32 v83, v83\n"
    "v_exp_f32 v84, v84\n"
    "v_mfma_f32_32x32x16_bf16 v[96:111], v[124:127], v[68:71], 0\n"
    "v_exp_f32 v85, v85\n"
    "v_exp_f32 v86, v86\n"
    "v_exp_f32 v87, v87\n"
    "v_exp_f32 v88, v88\n"
    "v_exp_f32 v89, v89\n"
    "v_smfmac_f32_32x32x32_bf16 v[0:15], v[128:131], v[80:87], v160\n"
    "v_exp_f32 v90, v90\n"
    "v_exp_f32 v91, v91\n"
    "v_exp_f32 v92, v92\n"
    "v_exp_f32 v93, v93\n"
    "v_exp_f32 v94, v94\n"
    "v_exp_f32 v95, v95\n"
    "v_exp_f32 v96, v96\n"
    "v_exp_f32 v97, v97\n"
    "v_smfmac_f32_32x32x32_bf16 v[0:15], v[132:135], v[88:95], v160\n"
    "v_exp_f32 v98, v98\n"
    "v_exp_f32 v99, v99\n"
    "v_exp_f32 v100, v100\n"
    "v_mfma_f32_32x32x16_bf16 v[80:95], v[124:127], v[72:75], 0\n"
    "v_exp_f32 v101, v101\n"
    "v_exp_f32 v102, v102\n"
    "v_exp_f32 v103, v103\n"
    "v_exp_f32 v104, v104\n"
    "v_exp_f32 v105, v105\n"
    "v_smfmac_f32_32x32x32_bf16 v[16:31], v[128:131], v[96:103], v160\n"
    "v_exp_f32 v106, v106\n"
    "v_exp_f32 v107, v107\n"
    "v_exp_f32 v108, v108\n"
    "v_exp_f32 v109, v109\n"
    "v_exp_f32 v110, v110\n"
    "v_exp_f32 v111, v111\n"
    "v_exp_f32 v80, v80\n"
    "v_exp_f32 v81, v81\n"
    "v_smfmac_f32_32x32x32_bf16 v[16:31], v[132:135], v[104:111], v160\n"
    "v_exp_f32 v82, v82\n"
    "v_exp_f32 v83, v83\n"
    "v_exp_f32 v84, v84\n"
    "v_mfma_f32_32x32x16_bf16 v[96:111], v[124:127], v[76:79], 0\n"
    "v_exp_f32 v85, v85\n"
    "v_exp_f32 v86, v86\n"
    "v_exp_f32 v87, v87\n"
    "v_exp_f32 v88, v88\n"
    "v_exp_f32 v89, v89\n"
    "v_smfmac_f32_32x32x32_bf16 v[32:47], v[128:131], v[80:87], v160\n"
    "v_exp_f32 v90, v90\n"
    "v_exp_f32 v91, v91\n"
    "v_exp_f32 v92, v92\n"
    "v_exp_f32 v93, v93\n"
    "v_exp_f32 v94, v94\n"
    "v_exp_f32 v95, v95\n"
    "v_exp_f32 v96, v96\n"
    "v_exp_f32 v97, v97\n"
    "v_smfmac_f32_32x32x32_bf16 v[32:47], v[132:135], v[88:95], v160\n"
    "v_exp_f32 v98, v98\n"
    "v_exp_f32 v99, v99\n"
    "v_exp_f32 v100, v100\n"
    "s_waitcnt vmcnt(3)\n"
    "v_mfma_f32_32x32x16_bf16 v[80:95], v[136:139], v[64:67], 0\n"
    "v_exp_f32 v101, v101\n"
    "v_exp_f32 v102, v102\n"
    "v_exp_f32 v103, v103\n"
    "v_exp_f32 v104, v104\n"
    "v_exp_f32 v105, v105\n"
    "v_smfmac_f32_32x32x32_bf16 v[48:63], v[128:131], v[96:103], v160\n"
    "v_exp_f32 v106, v106\n"
    "v_exp_f32 v107, v107\n"
    "v_exp_f32 v108, v108\n"
    "v_exp_f32 v109, v109\n"
    "v_exp_f32 v110, v110\n"
    "v_exp_f32 v111, v111\n"
    "v_exp_f32 v80, v80\n"
    "v_exp_f32 v81, v81\n"
    "v_smfmac_f32_32x32x32_bf16 v[48:63], v[132:135], v[104:111], v160\n"
    "v_exp_f32 v82, v82\n"
    "v_exp_f32 v83, v83\n"
    "v_exp_f32 v84, v84\n"
    "v_mfma_f32_32x32x16_bf16 v[96:111], v[136:139], v[68:71], 0\n"
    "v_exp_f32 v85, v85\n"
    "v_exp_f32 v86, v86\n"
    "v_exp_f32 v87, v87\n"
    "v_exp_f32 v88, v88\n"
    "v_exp_f32 v89, v89\n"
    "v_smfmac_f32_32x32x32_bf16 v[0:15], v[140:143], v[80:87], v160\n"
    "v_exp_f32 v90, v90\n"
    "v_exp_f32 v91, v91\n"
    "v_exp_f32 v92, v92\n"
    "v_exp_f32 v93, v93\n"
    "v_exp_f32 v94, v94\n"
    "v_exp_f32 v95, v95\n"
    "v_exp_f32 v96, v96\n"
    "v_exp_f32 v97, v97\n"
    "v_smfmac_f32_32x32x32_bf16 v[0:15], v[144:147], v[88:95], v160\n"
    "v_exp_f32 v98, v98\n"
    "v_exp_f32 v99, v99\n"
    "v_exp_f32 v100, v100\n"
    "v_mfma_f32_32x32x16_bf16 v[80:95], v[136:139], v[72:75], 0\n"
    "v_exp_f32 v101, v101\n"
    "v_exp_f32 v102, v102\n"
    "v_exp_f32 v103, v103\n"
    "v_exp_f32 v104, v104\n"
    "v_exp_f32 v105, v105\n"
    "v_smfmac_f32_32x32x32_bf16 v[16:31], v[140:143], v[96:103], v160\n"
    "v_exp_f32 v106, v106\n"
    "v_exp_f32 v107, v107\n"
    "v_exp_f32 v108, v108\n"
    "v_exp_f32 v109, v109\n"
    "v_exp_f32 v110, v110\n"
    "v_exp_f32 v111, v111\n"
    "v_exp_f32 v80, v80\n"
    "v_exp_f32 v81, v81\n"
    "v_smfmac_f32_32x32x32_bf16 v[16:31], v[144:147], v[104:111], v160\n"
    "v_exp_f32 v82, v82\n"
    "v_exp_f32 v83, v83\n"
    "v_exp_f32 v84, v84\n"
    "v_mfma_f32_32x32x16_bf16 v[96:111], v[136:139], v[76:79], 0\n"
    "v_exp_f32 v85, v85\n"
    "v_exp_f32 v86, v86\n"
    "v_exp_f32 v87, v87\n"
    "v_exp_f32 v88, v88\n"
    "v_exp_f32 v89, v89\n"
    "v_smfmac_f32_32x32x32_bf16 v[32:47], v[140:143], v[80:87], v160\n"
    "v_exp_f32 v90, v90\n"
    "v_exp_f32 v91, v91\n"
    "v_exp_f32 v92, v92\n"
    "v_exp_f32 v93, v93\n"
    "v_exp_f32 v94, v94\n"
    "v_exp_f32 v95, v95\n"
    "v_exp_f32 v96, v96\n"
    "v_exp_f32 v97, v97\n"
    "v_smfmac_f32_32x32x32_bf16 v[32:47], v[144:147], v[88:95], v160\n"
    "v_exp_f32 v98, v98\n"
    "v_exp_f32 v99, v99\n"
    "v_exp_f32 v100, v100\n"
    "s_waitcnt vmcnt(0)\n"
    "v_mfma_f32_32x32x16_bf16 v[80:95], v[148:151], v[64:67], 0\n"
    "v_exp_f32 v101, v101\n"
    "v_exp_f32 v102, v102\n"
    "v_exp_f32 v103, v103\n"
    "v_exp_f32 v104, v104\n"
    "v_exp_f32 v105, v105\n"
    "v_smfmac_f32_32x32x32_bf16 v[48:63], v[140:143], v[96:103], v160\n"
    "v_exp_f32 v106, v106\n"
    "v_exp_f32 v107, v107\n"
    "v_exp_f32 v108, v108\n"
    "v_exp_f32 v109, v109\n"
    "v_exp_f32 v110, v110\n"
    "v_exp_f32 v111, v111\n"
    "v_exp_f32 v80, v80\n"
    "v_exp_f32 v81, v81\n"
    "v_smfmac_f32_32x32x32_bf16 v[48:63], v[144:147], v[104:111], v160\n"
    "v_exp_f32 v82, v82\n"
    "v_exp_f32 v83, v83\n"
    "v_exp_f32 v84, v84\n"
    "v_mfma_f32_32x32x16_bf16 v[96:111], v[148:151], v[68:71], 0\n"
    "v_exp_f32 v85, v85\n"
    "v_exp_f32 v86, v86\n"
    "v_exp_f32 v87, v87\n"
    "v_exp_f32 v88, v88\n"
    "v_exp_f32 v89, v89\n"
    "v_smfmac_f32_32x32x32_bf16 v[0:15], v[152:155], v[80:87], v160\n"
    "v_exp_f32 v90, v90\n"
    "v_exp_f32 v91, v91\n"
    "v_exp_f32 v92, v92\n"
    "v_exp_f32 v93, v93\n"
    "v_exp_f32 v94, v94\n"
    "v_exp_f32 v95, v95\n"
    "v_exp_f32 v96, v96\n"
    "v_exp_f32 v97, v97\n"
    "v_smfmac_f32_32x32x32_bf16 v[0:15], v[156:159], v[88:95], v160\n"
    "v_exp_f32 v98, v98\n"
    "v_exp_f32 v99, v99\n"
    "v_exp_f32 v100, v100\n"
    "v_mfma_f32_32x32x16_bf16 v[80:95], v[148:151], v[72:75], 0\n"
    "v_exp_f32 v101, v101\n"
    "v_exp_f32 v102, v102\n"
    "v_exp_f32 v103, v103\n"
    "v_exp_f32 v104, v104\n"
    "v_exp_f32 v105, v105\n"
    "v_smfmac_f32_32x32x32_bf16 v[16:31], v[152:155], v[96:103], v160\n"
    "v_exp_f32 v106, v106\n"
    "v_exp_f32 v107, v107\n"
    "v_exp_f32 v108, v108\n"
    "v_exp_f32 v109, v109\n"
    "v_exp_f32 v110, v110\n"
    "v_exp_f32 v111, v111\n"
    "v_exp_f32 v80, v80\n"
    "v_exp_f32 v81, v81\n"
    "v_smfmac_f32_32x32x32_bf16 v[16:31], v[156:159], v[104:111], v160\n"
    "v_exp_f32 v82, v82\n"
    "v_exp_f32 v83, v83\n"
    "v_exp_f32 v84, v84\n"
    "v_mfma_f32_32x32x16_bf16 v[96:111], v[148:151], v[76:79], 0\n"
    "v_exp_f32 v85, v85\n"
    "v_exp_f32 v86, v86\n"
    "v_exp_f32 v87, v87\n"
    "v_exp_f32 v88, v88\n"
    "v_exp_f32 v89, v89\n"
    "v_smfmac_f32_32x32x32_bf16 v[32:47], v[152:155], v[80:87], v160\n"
    "v_exp_f32 v90, v90\n"
    "v_exp_f32 v91, v91\n"
    "v_exp_f32 v92, v92\n"
    "v_exp_f32 v93, v93\n"
    "v_exp_f32 v94, v94\n"
    "v_exp_f32 v95, v95\n"
    "v_exp_f32 v96, v96\n"
    "v_exp_f32 v97, v97\n"
    "v_smfmac_f32_32x32x32_bf16 v[32:47], v[156:159], v[88:95], v160\n"
    "v_exp_f32 v98, v98\n"
    "v_exp_f32 v99, v99\n"
    "v_exp_f32 v100, v100\n"
    "v_exp_f32 v101, v101\n"
    "v_exp_f32 v102, v102\n"
    "v_exp_f32 v103, v103\n"
    "v_exp_f32 v104, v104\n"
    "v_exp_f32 v105, v105\n"
    "v_smfmac_f32_32x32x32_bf16 v[48:63], v[152:155], v[96:103], v160\n"
    "v_exp_f32 v106, v106\n"
    "v_exp_f32 v107, v107\n"
    "v_exp_f32 v108, v108\n"
    "v_exp_f32 v109, v109\n"
    "v_exp_f32 v110, v110\n"
    "v_exp_f32 v111, v111\n"
    "s_nop 1\n"
    "v_smfmac_f32_32x32x32_bf16 v[48:63], v[156:159], v[104:111], v160\n"
    "s_nop 11\n"
                : "+{v[0:15]}"(o0), "+{v[16:31]}"(o1), "+{v[32:47]}"(o2), "+{v[48:63]}"(o3), "+{v[104:111]}"(sb_hi), "+{v[156:159]}"(ones), "+{s[40:41]}"(base)
                : "{v[64:67]}"(q0), "{v[68:71]}"(q1), "{v[72:75]}"(q2), "{v[76:79]}"(q3), "{v160}"(0xddddu), "{v161}"(lane * 16), "{v162}"(h * 256 + (r & 15) * 16),
                  "{s[42:43]}"(0x0000ffff0000ffffull)
                : "memory", "scc", "s44",
                  "v80", "v81", "v82", "v83", "v84", "v85", "v86", "v87", "v88", "v89", "v90", "v91", "v92", "v93", "v94", "v95",
                  "v96", "v97", "v98", "v99", "v100", "v101", "v102", "v103",
                  "v112", "v113", "v114", "v115", "v116", "v117", "v118", "v119", "v120", "v121", "v122", "v123", "v124", "v125", "v126", "v127",
                  "v128", "v129", "v130", "v131", "v132", "v133", "v134", "v135", "v136", "v137", "v138", "v139", "v140", "v141", "v142", "v143",
                  "v144", "v145", "v146", "v147", "v148", "v149", "v150", "v151", "v152", "v153", "v154", "v155");
        } else {
            m0 = m1 = m2 = m3 = -INFINITY;
            const u32x4 ones = {0x3f803f80u, 0x3f803f80u, 0x3f803f80u, 0x3f803f80u};
#pragma unroll 1
            for (int t = 0; t < 16; ++t) {
                const u32x4 kf = __builtin_amdgcn_raw_buffer_load_b128(wsr, kvo + t * 2048 + lane * 16, 0, SC1);
                u32x4 v0 = __builtin_amdgcn_raw_buffer_load_b128(wsr, kvo + t * 2048 + 1024 + h * 256 + (r & 15) * 16, 0, SC1);
                u32x4 v1 = __builtin_amdgcn_raw_buffer_load_b128(wsr, kvo + t * 2048 + 1536 + h * 256 + (r & 15) * 16, 0, SC1);
                if (r >= 16) { v0 = ones; v1 = ones; }
                auto step = [&](const u32x4& q, f32x16& o, float& m) {
                    f32x16 s = mfma32(kf, q, (f32x16){});
                    float mx = s[0];
#pragma unroll
                    for (int i = 1; i < 16; ++i) mx = fmaxf(mx, s[i]);
                    const float mn = fmaxf(m, halves_max(mx)), al = __builtin_amdgcn_exp2f(m - mn);
                    m = mn;
#pragma unroll
                    for (int i = 0; i < 16; ++i) { s[i] = __builtin_amdgcn_exp2f(s[i] - mn); o[i] *= al; }
                    const f32x8 lo = __builtin_shufflevector(s, s, 0, 1, 2, 3, 4, 5, 6, 7), hi = __builtin_shufflevector(s, s, 8, 9, 10, 11, 12, 13, 14, 15);
                    o = __builtin_amdgcn_smfmac_f32_32x32x32_bf16(__builtin_bit_cast(bf16x8, v0), __builtin_bit_cast(bf16x16, lo), o, 0xdddd, 0, 0);
                    o = __builtin_amdgcn_smfmac_f32_32x32x32_bf16(__builtin_bit_cast(bf16x8, v1), __builtin_bit_cast(bf16x16, hi), o, 0xdddd, 0, 0);
                };
                step(q0, o0, m0); step(q1, o1, m1); step(q2, o2, m2); step(q3, o3, m3);
            }
        }
        STAMP(8);
        while (__hip_atomic_load((LAS u32*)(L_SYNC + 16), __ATOMIC_RELAXED, __HIP_MEMORY_SCOPE_WORKGROUP) != 4) __builtin_amdgcn_s_sleep(1);
        asm volatile("" ::: "memory");
        auto put = [&](int i, const f32x16& o, float m) {
            const u32 rec = L_PART + ((a * 4 + ((tb + i) & 3)) * 3 + i - 1) * 2560;
            ST(f32x4, rec + lane * 16) = (f32x4){o[0], o[1], o[2], o[3]};
            ST(f32x4, rec + 1024 + lane * 16) = (f32x4){o[4], o[5], o[6], o[7]};
            ST(f32x2, rec + 2048 + lane * 8) = (f32x2){o[8], m};
        };
        put(1, o1, m1); put(2, o2, m2); put(3, o3, m3);
        BAR(0);
        {
            f32x4 pa[3], pb[3]; f32x2 lm[3];
#pragma unroll
            for (int i = 0; i < 3; ++i) {
                const u32 rec = L_PART + ((a * 4 + tb) * 3 + i) * 2560;
                pa[i] = LD(f32x4, rec + lane * 16); pb[i] = LD(f32x4, rec + 1024 + lane * 16); lm[i] = LD(f32x2, rec + 2048 + lane * 8);
            }
            const float mm = fmaxf(fmaxf(m0, lm[0].y), fmaxf(lm[1].y, lm[2].y));
            const float s0 = __builtin_amdgcn_exp2f(m0 - mm);
            float l = o0[8] * s0, acc[8];
#pragma unroll
            for (int j = 0; j < 8; ++j) acc[j] = o0[j] * s0;
#pragma unroll
            for (int i = 0; i < 3; ++i) {
                const float s = __builtin_amdgcn_exp2f(lm[i].y - mm);
                l = fmaf(lm[i].x, s, l);
#pragma unroll
                for (int j = 0; j < 4; ++j) { acc[j] = fmaf(pa[i][j], s, acc[j]); acc[4 + j] = fmaf(pb[i][j], s, acc[4 + j]); }
            }
            const float rl = __builtin_amdgcn_rcpf(l);
            const u32x4 of = {pk2(acc[0] * rl, acc[1] * rl), pk2(acc[2] * rl, acc[3] * rl), pk2(acc[4] * rl, acc[5] * rl), pk2(acc[6] * rl, acc[7] * rl)};
            __builtin_amdgcn_raw_buffer_store_b128(of, wsr, WS_OF + ((qb * 4 + tb) * 32 + hp * 2 + a) * 1024 + lane * 16, 0, SC1);
        }
        BAR(0);
        if (FUSED && threadIdx.x == 0) __hip_atomic_store((GAS u64*)(ws + WS_OFLAG + (qb * 16 + hp) * 128), tag << 16, RLX_AGENT);
        STAMP(9);
    }

    if (PH & 4) {
        const int tk = w & 1, k4 = w >> 1, g1 = (lane >> 4) & 1, q4 = (lane >> 2) & 3;
        if (!FUSED) BAR(0);
        poll_tag<FUSED>(ws + WS_YFLAG + ((cb64 * 64 + w * 8 + (lane & 7)) * 2 + (rb64 >> 4)) * 64, lane < 8, tag);
        const u32x4 yv = __builtin_amdgcn_raw_buffer_load_b128(wsr, WS_YT + ((cb64 * 64 + w * 8 + (lane >> 3)) * 2048 + rb64 * 64) * 2 + (lane & 7) * 16, 0, SC1);
        u32x4 wf[2][8];
#pragma unroll
        for (int c = 0; c < 2; ++c) {
            const u32 wa = L_WO + (k4 * 128 + 4 * h + q4) * 128 + (((c ^ (k4 >> 1) ^ (q4 >> 1)) & 1) << 6) + ((g1 ^ (k4 & 1)) << 5) + (lane & 3) * 8;
#pragma unroll
            for (int i = 0; i < 8; ++i) wf[c][i] = trd2(wa + i * 2048, wa + i * 2048 + 1024);
        }
        const float g = LD(float, L_G + ((hp >> 3) * 64 + tk * 32 + r) * 4);
        ST(u32x4, L_YT + (w * 64 + lane) * 16) = yv;
        poll_tag<FUSED>(ws + WS_OFLAG + (qb * 16 + k4 * 4 + (lane & 3)) * 128, lane < 4, tag);
        STAMP(10);
        u32x4 of[8];
#pragma unroll
        for (int i = 0; i < 8; ++i) of[i] = __builtin_amdgcn_raw_buffer_load_b128(wsr, WS_OF + ((rb64 * 2 + tk) * 32 + k4 * 8 + i) * 1024 + lane * 16, 0, SC1);
        __builtin_amdgcn_sched_barrier(0);
        f32x16 c0 = {}, c1 = {};
#pragma unroll
        for (int i = 0; i < 8; ++i) { c0 = mfma32(wf[0][i], of[i], c0); c1 = mfma32(wf[1][i], of[i], c1); }
        STAMP(11);
        auto put = [&](int i, const f32x16& c, int o) {
            const u32 rec = L_RED + ((tk * 4 + (i ^ k4)) * 3 + i - 1) * 2048 + lane * 16;
            ST(f32x4, rec) = (f32x4){c[o], c[o + 1], c[o + 2], c[o + 3]};
            ST(f32x4, rec + 1024) = (f32x4){c[o + 4], c[o + 5], c[o + 6], c[o + 7]};
        };
        put(1, c0, 8); put(2, c1, 0); put(3, c1, 8);
        BAR_LDS();
        if (FUSED && b == 0 && threadIdx.x == 0) *(GAS u64*)(ws + WS_NONCE) = nonce + 1;
        float acc[8];
#pragma unroll
        for (int j = 0; j < 8; ++j) acc[j] = c0[j];
#pragma unroll
        for (int i = 0; i < 3; ++i) {
            const u32 rec = L_RED + ((tk * 4 + k4) * 3 + i) * 2048 + lane * 16;
            const f32x4 u = LD(f32x4, rec), v = LD(f32x4, rec + 1024);
#pragma unroll
            for (int j = 0; j < 4; ++j) { acc[j] += u[j]; acc[4 + j] += v[j]; }
        }
        const u32 ya = L_YT + (k4 * 16 + 4 * h + q4) * 128 + (tk * 32 + g1 * 16 + (lane & 3) * 4) * 2;
        const u32x4 yf = trd2(ya, ya + 1024);
        u32 pk[4];
#pragma unroll
        for (int j = 0; j < 4; ++j)
            pk[j] = pk2(fmaf(g, bflo(yf[j]) - acc[2 * j], acc[2 * j]), fmaf(g, bfhi(yf[j]) - acc[2 * j + 1], acc[2 * j + 1]));
        *(GAS u32x4*)(p.out + ((rb64 * 64 + tk * 32 + r) * 512 + cb64 * 64 + k4 * 16 + h * 8) * 2) = widen(pk[0], pk[1], pk[2], pk[3]);
        STAMP(12);
    }
}

extern "C" void kernel_launch(void* const* d_in, const int* in_sizes, int n_in, void* d_out, int out_size, void* d_ws, size_t ws_size,
                              hipStream_t stream) {
    Params p;
    p.x = (const char*)d_in[0]; p.a_re = (const char*)d_in[1]; p.a_im = (const char*)d_in[2]; p.b_re = (const char*)d_in[3];
    p.b_im = (const char*)d_in[4]; p.c_re = (const char*)d_in[5]; p.c_im = (const char*)d_in[6]; p.log_dt = (const char*)d_in[7];
    p.dskip = (const char*)d_in[8]; p.wqkv = (const char*)d_in[10]; p.wo = (const char*)d_in[11]; p.gate_w = (const char*)d_in[12];
    p.out = (char*)d_out; p.ws = (char*)d_ws;
    s4_mha_mix<7><<<256, 512, 0, stream>>>(p);
}
```

```cpp
#define p0 PROBE_I 0 PROBE_J 0 PROBE_B 100 PROBE_W 0
#include <hip/hip_runtime.h>
#include <hip/hip_bf16.h>
#include <stdint.h>
#pragma clang diagnostic ignored "-Wint-to-pointer-cast"

#define NLAUNCH 1

#define LAS __attribute__((address_space(3)))
#define GAS __attribute__((address_space(1)))
typedef unsigned u32;
typedef unsigned long long u64;
typedef __attribute__((ext_vector_type(2))) u32 u32x2;
typedef __attribute__((ext_vector_type(4))) u32 u32x4;
typedef __attribute__((ext_vector_type(8))) u32 u32x8;
typedef __attribute__((ext_vector_type(2))) float f32x2;
typedef __attribute__((ext_vector_type(4))) float f32x4;
typedef __attribute__((ext_vector_type(8))) float f32x8;
typedef __attribute__((ext_vector_type(16))) float f32x16;
typedef __attribute__((ext_vector_type(2))) __bf16 bf16x2;
typedef __attribute__((ext_vector_type(8))) __bf16 bf16x8;
typedef __attribute__((ext_vector_type(16))) __bf16 bf16x16;
typedef __attribute__((ext_vector_type(4))) short s16x4;
typedef __attribute__((ext_vector_type(2))) unsigned short u16x2;

constexpr u32 WS_KV = 0;
constexpr u32 WS_OF = 4u << 20;
constexpr u32 WS_YT = 6u << 20;
constexpr u32 WS_KVFLAG = 8u << 20;
constexpr u32 WS_OFLAG = WS_KVFLAG + 32768;
constexpr u32 WS_YFLAG = WS_OFLAG + 32768;
constexpr u32 WS_NONCE = WS_YFLAG + 65536;
constexpr u32 WS_QF = 9u << 20;
constexpr u32 WS_END = 12u << 20;
constexpr u32 WS_STAMP = 16u << 20;

constexpr u32 CHUNK = 28672, L_W = 16384;
constexpr u32 L_TAB = 86016, TAB_ROW = 528, TAB_A = 21 * TAB_ROW, TAB_CH = 53 * TAB_ROW;
constexpr u32 L_KF = L_TAB + 2 * TAB_CH, KF_CH = 4608;
constexpr u32 L_QX = L_KF + 2 * KF_CH;
constexpr u32 L_MISC = L_QX + 8192;
constexpr u32 L_GW = L_MISC;
constexpr u32 L_QN = L_GW + 1024;
constexpr u32 L_G = L_QN + 1024;
constexpr u32 L_KN = L_G + 512;
constexpr u32 L_SYNC = L_KN + 32;
constexpr u32 L_UC = L_TAB, UC_COPY = 4160, UC_CH = 4 * UC_COPY;
constexpr u32 L_KB = L_UC + 2 * UC_CH + 64, KB_CH = 96 * 80;
constexpr u32 L_WO = 0;
constexpr u32 L_CX = 65536;
constexpr u32 L_PART = 65536;
constexpr u32 L_RED = 65536;
constexpr u32 L_YT = L_QX;
constexpr u32 LDS_BYTES = 163840;
static_assert(L_SYNC + 32 <= LDS_BYTES && L_KB + 2 * KB_CH <= L_KF, "lds map");

constexpr float LOG2E = 1.4426950408889634f;
constexpr float QSCALE = 0.25f * LOG2E;
constexpr float SAFE2 = 64.f * 64.f;
constexpr int CODE_BYTES = 22 * 1024;
constexpr u32 SPIN_CAP = 1u << 17;

#define LD(T, off) (*(const LAS T*)(u32)(off))
#define ST(T, off) (*(LAS T*)(u32)(off))
#define RLX_AGENT __ATOMIC_RELAXED, __HIP_MEMORY_SCOPE_AGENT
#define SC1 16
#define LGKM0() do { asm volatile("" ::: "memory"); __builtin_amdgcn_s_waitcnt(0xc07f); } while (0)
#define BAR(N) do { LGKM0(); asm volatile("s_waitcnt vmcnt(" #N ") lgkmcnt(0)\n\ts_barrier" ::: "memory"); } while (0)
#define BAR_LDS() do { LGKM0(); asm volatile("s_waitcnt lgkmcnt(0)\n\ts_barrier" ::: "memory"); } while (0)

#define STAMP(i) do { } while (0)
#define STAMP_ARG
#define STAMP_PASS

__device__ __forceinline__ u32 pk2(float a, float b) { bf16x2 v = {(__bf16)a, (__bf16)b}; return __builtin_bit_cast(u32, v); }
__device__ __forceinline__ float dot2(u32 a, u32 b, float c) {
    return __builtin_amdgcn_fdot2_f32_bf16(__builtin_bit_cast(bf16x2, a), __builtin_bit_cast(bf16x2, b), c, false); }
__device__ __forceinline__ float bflo(u32 v) { return __uint_as_float(v << 16); }
__device__ __forceinline__ float bfhi(u32 v) { return __uint_as_float(v & 0xffff0000u); }
__device__ __forceinline__ f32x16 mfma32(u32x4 a, u32x4 b, f32x16 c) {
    return __builtin_amdgcn_mfma_f32_32x32x16_bf16(__builtin_bit_cast(bf16x8, a), __builtin_bit_cast(bf16x8, b), c, 0, 0, 0); }
__device__ __forceinline__ f32x4 mfma16(u32x4 a, u32x4 b, f32x4 c) {
    return __builtin_amdgcn_mfma_f32_16x16x32_bf16(__builtin_bit_cast(bf16x8, a), __builtin_bit_cast(bf16x8, b), c, 0, 0, 0); }
__device__ __forceinline__ u32x2 trd(u32 off) {
    return __builtin_bit_cast(u32x2, __builtin_amdgcn_ds_read_tr16_b64_v4i16((LAS s16x4*)off)); }
__device__ __forceinline__ u32x4 trd2(u32 o0, u32 o1) { const u32x2 a = trd(o0), b = trd(o1); return (u32x4){a.x, a.y, b.x, b.y}; }
__device__ __forceinline__ void dma1(u32 v0, const void* sbase, u32 dst) {
    asm volatile("s_mov_b32 m0, %2\n\ts_nop 0\n\tglobal_load_lds_dwordx4 %0, %1" :: "v"(v0), "s"(sbase), "s"(dst) : "memory"); }
__device__ __forceinline__ void dma3(u32 v0, u32 v1, const void* sbase, u32 dst) {
    asm volatile("s_mov_b32 m0, %3\n\ts_nop 0\n\tglobal_load_lds_dwordx4 %0, %2\n\tglobal_load_lds_dwordx4 %1, %2 offset:1024\n\t"
                 "global_load_lds_dwordx4 %0, %2 offset:2048" :: "v"(v0), "v"(v1), "s"(sbase), "s"(dst) : "memory"); }
__device__ __forceinline__ void dma4(u32 v0, u32 v1, u32 v2, u32 v3, const void* sbase, u32 dst) {
    asm volatile("s_mov_b32 m0, %5\n\ts_nop 0\n\tglobal_load_lds_dwordx4 %0, %4\n\tglobal_load_lds_dwordx4 %1, %4 offset:1024\n\t"
                 "global_load_lds_dwordx4 %2, %4 offset:2048\n\tglobal_load_lds_dwordx4 %3, %4 offset:3072"
                 :: "v"(v0), "v"(v1), "v"(v2), "v"(v3), "s"(sbase), "s"(dst) : "memory"); }
__device__ __forceinline__ float halves_sum(float v) {
    auto r = __builtin_amdgcn_permlane32_swap(__float_as_uint(v), __float_as_uint(v), false, false);
    return __uint_as_float(r[0]) + __uint_as_float(r[1]); }
__device__ __forceinline__ float halves_max(float v) {
    auto r = __builtin_amdgcn_permlane32_swap(__float_as_uint(v), __float_as_uint(v), false, false);
    return fmaxf(__uint_as_float(r[0]), __uint_as_float(r[1])); }
template <int CTRL> __device__ __forceinline__ float dpp_max(float v) {
    return fmaxf(v, __uint_as_float(__builtin_amdgcn_update_dpp(__float_as_uint(v), __float_as_uint(v), CTRL, 0xf, 0xf, false))); }
__device__ __forceinline__ u32x4 widen(u32 a0, u32 a1, u32 b0, u32 b1) {
    auto r0 = __builtin_amdgcn_permlane32_swap(a0, b0, false, false);
    auto r1 = __builtin_amdgcn_permlane32_swap(a1, b1, false, false);
    return (u32x4){r0[0], r1[0], r0[1], r1[1]}; }
__device__ __forceinline__ f32x2 cmul(f32x2 a, f32x2 b) { return (f32x2){a.x * b.x - a.y * b.y, a.x * b.y + a.y * b.x}; }
__device__ __forceinline__ f32x2 cexp_n(float zr2, float th, float n) {
    const float m = __builtin_amdgcn_exp2f(zr2 * n), a = __builtin_amdgcn_fractf(th * n);
    return (f32x2){m * __builtin_amdgcn_cosf(a), m * __builtin_amdgcn_sinf(a)}; }
template <bool WAIT> __device__ __forceinline__ u64 poll_tag(const char* p, bool active, u64 tag) {
    u64 v = tag << 16;
#pragma unroll 1
    for (u32 spins = 0; spins < SPIN_CAP; ++spins) {
        if (active) v = __hip_atomic_load((const GAS u64*)p, RLX_AGENT);
        if (!WAIT || __all((v >> 16) == tag)) break;
        __builtin_amdgcn_s_sleep(1);
    }
    __builtin_amdgcn_fence(__ATOMIC_ACQUIRE, "wavefront");
    return v; }

struct Params {
    const char *x, *a_re, *a_im, *b_re, *b_im, *c_re, *c_im, *log_dt, *dskip, *wqkv, *wo, *gate_w;
    char *out, *ws;
};

template <int ROLE>
__device__ __forceinline__ void phase1(const Params& p, __amdgpu_buffer_rsrc_t wsr, char* ws, int lane, int w, int hp, int qb, int d0, u64& nonce, u64 pc0 STAMP_ARG) {
    const int tb = w & 3, r = lane & 31, h = lane >> 5;
    const int ch = w & 1, n = ((w >> 1) & 1) * 64 + lane;
    const u32 po = ((d0 + ch) * 128 + n) * 2;
    u32 pf = 0;
    if (ROLE == 1 && tb >= 2) asm volatile("global_load_dword %0, %1, %2" : "=&v"(pf) : "v"(min((((hp & 3) * 2 + (tb & 1)) * 64 + lane) * 64, CODE_BYTES)), "s"(pc0));
    u32 are, aim, bre = 0, bim = 0, cre = 0, cim = 0;
    asm volatile("global_load_ushort %0, %2, %3\n\tglobal_load_ushort %1, %2, %4" : "=&v"(are), "=&v"(aim) : "v"(po), "s"(p.a_re), "s"(p.a_im));
    if (ROLE == 0)
        asm volatile("global_load_ushort %0, %4, %5\n\tglobal_load_ushort %1, %4, %6\n\tglobal_load_ushort %2, %4, %7\n\tglobal_load_ushort %3, %4, %8"
                     : "=&v"(bre), "=&v"(bim), "=&v"(cre), "=&v"(cim) : "v"(po), "s"(p.b_re), "s"(p.b_im), "s"(p.c_re), "s"(p.c_im));
    else if (tb == 0) dma1(lane * 16, p.gate_w, L_GW);
    u32 dtw, dsw;
    u64 nw;
    asm volatile("s_load_dword %0, %3, 0x0\n\ts_load_dword %1, %4, 0x0\n\ts_load_dwordx2 %2, %5, 0x0"
                 : "=&s"(dtw), "=&s"(dsw), "=&s"(nw) : "s"(p.log_dt + d0 * 2), "s"(p.dskip + d0 * 2), "s"(ws + WS_NONCE));
    u32 vo[4];
    const char* src; u32 step, dst;
    if (ROLE == 0) {
        vo[0] = (lane >> 2) * 3072 + ((lane & 3) << 4); vo[1] = vo[0] ^ 32;
        src = p.wqkv + (tb * 16 * 3072 + hp * 64); step = 64 * 3072; dst = L_W + tb * 3072;
    } else {
#pragma unroll
        for (int i = 0; i < 4; ++i) vo[i] = (7 * i + (lane >> 3)) * 1024 + (((lane & 7) ^ ((4 * i + (lane >> 4)) & 7)) << 4);
        src = p.x + (qb * 128 + tb * 32) * 1024; step = 128; dst = tb * 4096;
    }
    auto issue = [&](int c) {
        if (ROLE == 0) dma3(vo[0], vo[1], src + c * step, (c % 3) * CHUNK + dst);
        else dma4(vo[0], vo[1], vo[2], vo[3], src + c * step, (c % 3) * CHUNK + dst);
    };
    issue(0); issue(1); issue(2);
    u32 ug[4];
    {
        const char* ub = p.x + d0 * 2;
        asm volatile("global_load_dword %0, %4, %5\n\tglobal_load_dword %1, %4, %6\n\tglobal_load_dword %2, %4, %7\n\tglobal_load_dword %3, %4, %8"
                     : "=&v"(ug[0]), "=&v"(ug[1]), "=&v"(ug[2]), "=&v"(ug[3])
#define UB(o) (ub + (o))
                     : "v"((w * 256 + lane) * 1024),
                       "s"(UB(0)), "s"(UB(65536)), "s"(UB(131072)), "s"(UB(196608)));
    }
    asm volatile("s_waitcnt lgkmcnt(0)" : "+s"(dtw), "+s"(dsw), "+s"(nw));
    nonce = nw;
    if (w == 0 && lane < 8) ST(u32, L_SYNC + lane * 4) = 0;
    const float dt = (float)(__bf16)expf(ch ? bfhi(dtw) : bflo(dtw));
    if (ROLE == 0) asm volatile("s_waitcnt vmcnt(13)" : "+v"(are), "+v"(aim), "+v"(bre), "+v"(bim), "+v"(cre), "+v"(cim), "+v"(pf));
    else asm volatile("s_waitcnt vmcnt(16)" : "+v"(are), "+v"(aim), "+v"(pf));
    STAMP(1);
    {
        const float zr2 = (dt * bflo(are)) * LOG2E, th = (dt * bflo(aim)) * 0.15915494309189535f;
        const u32 t0 = L_TAB + ch * TAB_CH + n * 4;
        if (ROLE == 0) {
            const f32x2 e1 = cexp_n(zr2, th, 1.f), g = cexp_n(zr2, th, 256.f);
            f32x2 a[8];
            a[0] = cmul((f32x2){bflo(cre), bflo(cim)}, (f32x2){dt * bflo(bre), dt * bflo(bim)});
#define PUT_A(row, v) ST(u32, t0 + (row) * TAB_ROW) = pk2((v).x, -(v).y)
            PUT_A(0, a[0]);
#pragma unroll
            for (int i = 1; i < 8; ++i) { a[i] = cmul(a[i - 1], e1); PUT_A(i, a[i]); }
            f32x2 c0 = cmul(a[0], g), c2 = cmul(a[2], g), c4 = cmul(a[4], g), c6 = cmul(a[6], g);
            PUT_A(8, c0); PUT_A(9, c2); PUT_A(10, c4); PUT_A(11, c6);
            c0 = cmul(c0, g); c4 = cmul(c4, g); PUT_A(12, c0); PUT_A(13, c4);
            c0 = cmul(c0, g); c4 = cmul(c4, g); PUT_A(14, c0); PUT_A(15, c4);
#pragma unroll
            for (int i = 16; i < 21; ++i) { c0 = cmul(c0, g); PUT_A(i, c0); }
#undef PUT_A
        } else {
            const f32x2 e8 = cexp_n(zr2, th, 8.f), e16 = cmul(e8, e8);
            f32x2 p0 = {1.f, 0.f}, p1 = e8;
#pragma unroll
            for (int i = 0; i < 32; i += 2) {
                ST(u32, t0 + TAB_A + i * TAB_ROW) = pk2(p0.x, p0.y);
                ST(u32, t0 + TAB_A + (i + 1) * TAB_ROW) = pk2(p1.x, p1.y);
                p0 = cmul(p0, e16); p1 = cmul(p1, e16);
            }
        }
    }
    STAMP(2);
    u32 xa[4];
#pragma unroll
    for (int ks = 0; ks < 4; ++ks) xa[ks] = (tb * 32 + r) * 128 + (((2 * ks + h) ^ ((r >> 1) & 7)) << 4);
    const int g1 = (lane >> 4) & 1;
    const u32 trb = L_W + (8 * h + ((lane >> 2) & 3)) * 64 + (lane & 3) * 8;
    const u32 wa = trb + g1 * 1024 + ((ROLE ^ g1) << 5), wv = trb + 2048 + (g1 << 5);
    f32x16 aqk = {}, av = {};
    float gacc = 0.f;
    u32x4 fx[2][4], fa[2][4], fb[2][4];
    const bool gate_here = ROLE == 1 && (tb >> 1) == (hp >> 3);
    auto mul = [&](int c) {
#pragma unroll
        for (int ks = 0; ks < 4; ++ks) {
            aqk = mfma32(fa[c & 1][ks], fx[c & 1][ks], aqk);
            if (ROLE == 0) av = mfma32(fx[c & 1][ks], fb[c & 1][ks], av);
        }
        if (gate_here) {
#pragma unroll
            for (int ks = 0; ks < 4; ++ks)
#pragma unroll
                for (int j = 0; j < 4; ++j) gacc = dot2(fx[c & 1][ks][j], fb[c & 1][ks][j], gacc);
        }
    };
#pragma unroll
    for (int c = 0; c < 8; ++c) {
        if (c == 0) { if (ROLE) BAR(12); else BAR(10); }
        else if (c < 3) { if (ROLE) BAR(8); else BAR(7); }
        else if (c < 7) { if (ROLE) BAR(4); else BAR(3); }
        else BAR(0);
        if (c >= 1 && c <= 5) issue(c + 2);
        const u32 cb = (c % 3) * CHUNK;
#pragma unroll
        for (int ks = 0; ks < 4; ++ks) {
            fx[c & 1][ks] = LD(u32x4, cb + xa[ks]);
            fa[c & 1][ks] = trd2(cb + wa + ks * 3072, cb + wa + ks * 3072 + 256);
            if (ROLE == 0) fb[c & 1][ks] = trd2(cb + wv + ks * 3072, cb + wv + ks * 3072 + 256);
        }
        if (gate_here) {
#pragma unroll
            for (int ks = 0; ks < 4; ++ks) fb[c & 1][ks] = LD(u32x4, L_GW + (c * 4 + ks) * 32 + h * 16);
        }
        __builtin_amdgcn_sched_barrier(0);
        if (c >= 1) mul(c - 1);
        __builtin_amdgcn_sched_barrier(0);
        if (c == 0) {
            STAMP(3);
            const int aq = (threadIdx.x >> 7) & 1, col = lane & 15, g = lane >> 4;
            const u32 ta = L_TAB + ch * TAB_CH + (aq * 16 + col) * TAB_ROW + g * 16;
            const u32 tbb = L_TAB + ch * TAB_CH + TAB_A + (ROLE * 16 + col) * TAB_ROW + g * 16;
            f32x4 kq = {};
#pragma unroll
            for (int ks = 0; ks < 8; ++ks) kq = mfma16(LD(u32x4, ta + ks * 64), LD(u32x4, tbb + ks * 64), kq);
            if (ROLE == 0 && aq == 0 && lane == 0) kq[0] += ch ? bfhi(dsw) : bflo(dsw);
            const int l0 = aq ? (g == 0 ? 1024 : g == 1 ? 2048 : 2052) : (g < 2 ? 4 * g : 256 * g - 256);
            const int s1 = aq ? (g == 0 ? 256 : 1) : (g < 2 ? 1 : 2 * g - 2), s2 = (!aq && g == 3) ? 256 : 2 * s1;
            const u32 k0 = L_KF + ch * KF_CH + (l0 + 8 * (ROLE * 16 + col)) * 2;
            const u32 e01 = pk2(kq[0], kq[1]), e23 = pk2(kq[2], kq[3]);
            ST(unsigned short, k0) = (unsigned short)e01; ST(unsigned short, k0 + 2 * s1) = (unsigned short)(e01 >> 16);
            ST(unsigned short, k0 + 2 * s2) = (unsigned short)e23; ST(unsigned short, k0 + 2 * (s1 + s2)) = (unsigned short)(e23 >> 16);
        }
        if (c == 1) {
            const int i8 = (w & 3) * 64 + lane, blk = i8 >> 2, pc = i8 & 3;
            u32x4 kv;
#pragma unroll
            for (int j = 0; j < 4; ++j) {
                const float t2 = (float)(2 * (32 * blk + 31 - 8 * pc - 2 * j));
                const u32 rr = pk2(t2, t2 - 2.f);
                const u16x2 e = {LD(unsigned short, L_KF + ROLE * KF_CH + (u32)bflo(rr)), LD(unsigned short, L_KF + ROLE * KF_CH + (u32)bfhi(rr))};
                kv[j] = __builtin_bit_cast(u32, e);
            }
            ST(u32x4, L_KB + ROLE * KB_CH + (blk + 32) * 80 + pc * 16) = kv;
            if (i8 < 128) ST(u32x4, L_KB + ROLE * KB_CH + blk * 80 + pc * 16) = (u32x4){0, 0, 0, 0};
            else if (i8 < 128 + 36) ST(u32x2, L_UC + ROLE * UC_CH + ((i8 - 128) / 9) * UC_COPY + ((i8 - 128) % 9) * 8) = (u32x2){0, 0};
        }
    }
    mul(7);
    STAMP(4);
    const u32 tile = (qb * 4 + tb) * 2048;
    {
        const u32x4 kf = {pk2(aqk[8], aqk[9]), pk2(aqk[10], aqk[11]), pk2(aqk[12], aqk[13]), pk2(aqk[14], aqk[15])};
        const int pr = (r & 19) | ((r & 4) << 1) | ((r & 8) >> 1);
        __builtin_amdgcn_raw_buffer_store_b128(kf, wsr, WS_KV + (hp * 2 + ROLE) * 131072 + tile + (h * 32 + pr) * 16, 0, SC1);
    }
    if (ROLE == 0) {
#pragma unroll
        for (int s = 0; s < 2; ++s) {
            const u32x4 vf = {pk2(av[8 * s], av[8 * s + 1]), pk2(av[8 * s + 2], av[8 * s + 3]), pk2(av[8 * s + 4], av[8 * s + 5]), pk2(av[8 * s + 6], av[8 * s + 7])};
            __builtin_amdgcn_raw_buffer_store_b128(vf, wsr, WS_KV + (hp * 2 + (r >> 4)) * 131072 + tile + 1024 + s * 512 + h * 256 + (r & 15) * 16, 0, SC1);
        }
    }
    float qn = 0.f, kn = 0.f;
#pragma unroll
    for (int i = 0; i < 8; ++i) { aqk[i] *= QSCALE; qn = fmaf(aqk[i], aqk[i], qn); kn = fmaf(aqk[8 + i], aqk[8 + i], kn); }
    ST(u32x4, L_QX + (ROLE * 4 + tb) * 1024 + lane * 16) = (u32x4){pk2(aqk[0], aqk[1]), pk2(aqk[2], aqk[3]), pk2(aqk[4], aqk[5]), pk2(aqk[6], aqk[7])};
    qn = halves_sum(qn); kn = halves_sum(kn);
    kn = dpp_max<0x111>(kn); kn = dpp_max<0x112>(kn); kn = dpp_max<0x114>(kn); kn = dpp_max<0x118>(kn); kn = dpp_max<0x142>(kn);
    if (h == 0) ST(float, L_QN + (ROLE * 128 + tb * 32 + r) * 4) = qn;
    if (lane == 31) ST(float, L_KN + (ROLE * 4 + tb) * 4) = kn;
    if (ROLE == 1) {
        const float s = halves_sum(gacc);
        if (h == 0) ST(float, L_G + (tb * 32 + r) * 4) = 1.f / (1.f + __expf(-s));
    }
    asm volatile("" : "+v"(ug[0]), "+v"(ug[1]), "+v"(ug[2]), "+v"(ug[3]));
#pragma unroll
    for (int i = 0; i < 4; ++i) {
        const u32 ua = L_UC + (w * 256 + i * 64 + lane + 36) * 2;
#pragma unroll
        for (int c = 0; c < 4; ++c) {
            ST(unsigned short, ua + c * (UC_COPY - 2)) = (unsigned short)ug[i];
            ST(unsigned short, ua + c * (UC_COPY - 2) + UC_CH) = (unsigned short)(ug[i] >> 16);
        }
    }
    STAMP(15);
}

__device__ __forceinline__ void conv_wave(__amdgpu_buffer_rsrc_t wsr, char* ws, int lane, int tb, int d0, u64 tag) {
    const int ch = tb & 1, mh = tb >> 1, r = lane & 31, h = lane >> 5, c = (r + 1) & 3;
    u32 ua = L_UC + ch * UC_CH + c * UC_COPY + (mh * 512 + (r + 1 - c) + 8 * h + 4) * 2;
    u32 ka = L_KB + ch * KB_CH + (30 + r - mh * 16) * 80 + h * 16;
    f32x16 y0, y1;
    __builtin_amdgcn_s_setprio(2);
    asm volatile(
        "ds_read_b64 v[32:33], v96 offset:0\nds_read_b64 v[34:35], v96 offset:8\nds_read_b128 v[36:39], v97 offset:160\n"
        "ds_read_b128 v[40:43], v97 offset:2720\nds_read_b64 v[44:45], v96 offset:2048\nds_read_b64 v[46:47], v96 offset:2056\n"
        "ds_read_b64 v[48:49], v96 offset:32\nds_read_b64 v[50:51], v96 offset:40\nds_read_b128 v[52:55], v97 offset:192\n"
        "ds_read_b128 v[56:59], v97 offset:2752\nds_read_b64 v[60:61], v96 offset:2080\nds_read_b64 v[62:63], v96 offset:2088\nv_mov_b32 v0, 0\n"
        "v_mov_b32 v1, 0\nv_mov_b32 v2, 0\nv_mov_b32 v3, 0\nv_mov_b32 v4, 0\nv_mov_b32 v5, 0\nv_mov_b32 v6, 0\nv_mov_b32 v7, 0\nv_mov_b32 v8, 0\n"
        "v_mov_b32 v9, 0\nv_mov_b32 v10, 0\nv_mov_b32 v11, 0\nv_mov_b32 v12, 0\nv_mov_b32 v13, 0\nv_mov_b32 v14, 0\nv_mov_b32 v15, 0\nv_mov_b32 v16, 0\n"
        "v_mov_b32 v17, 0\nv_mov_b32 v18, 0\nv_mov_b32 v19, 0\nv_mov_b32 v20, 0\nv_mov_b32 v21, 0\nv_mov_b32 v22, 0\nv_mov_b32 v23, 0\nv_mov_b32 v24, 0\n"
        "v_mov_b32 v25, 0\nv_mov_b32 v26, 0\nv_mov_b32 v27, 0\nv_mov_b32 v28, 0\nv_mov_b32 v29, 0\nv_mov_b32 v30, 0\nv_mov_b32 v31, 0\ns_mov_b32 s44, 8\n"
        ".p2align 3\n1:\ns_waitcnt lgkmcnt(9)\nv_mfma_f32_32x32x16_bf16 v[0:15], v[64:67], v[68:71], v[0:15]\nds_read_b64 v[64:65], v96 offset:64\n"
        "ds_read_b64 v[66:67], v96 offset:72\ns_waitcnt lgkmcnt(10)\nv_mfma_f32_32x32x16_bf16 v[16:31], v[64:67], v[72:75], v[16:31]\n"
        "ds_read_b128 v[68:71], v97 offset:80\nds_read_b128 v[72:75], v97 offset:2640\ns_waitcnt lgkmcnt(10)\n"
        "v_mfma_f32_32x32x16_bf16 v[16:31], v[76:79], v[68:71], v[16:31]\nds_read_b64 v[76:77], v96 offset:2112\nds_read_b64 v[78:79], v96 offset:2120\n"
        "s_waitcnt lgkmcnt(9)\nv_mfma_f32_32x32x16_bf16 v[0:15], v[80:83], v[84:87], v[0:15]\nds_read_b64 v[80:81], v96 offset:96\n"
        "ds_read_b64 v[82:83], v96 offset:104\ns_waitcnt lgkmcnt(10)\nv_mfma_f32_32x32x16_bf16 v[16:31], v[80:83], v[88:91], v[16:31]\n"
        "ds_read_b128 v[84:87], v97 offset:112\nds_read_b128 v[88:91], v97 offset:2672\ns_waitcnt lgkmcnt(10)\n"
        "v_mfma_f32_32x32x16_bf16 v[16:31], v[92:95], v[84:87], v[16:31]\nds_read_b64 v[92:93], v96 offset:2144\nds_read_b64 v[94:95], v96 offset:2152\n"
        "s_waitcnt lgkmcnt(9)\nv_mfma_f32_32x32x16_bf16 v[0:15], v[32:35], v[36:39], v[0:15]\nds_read_b64 v[32:33], v96 offset:128\n"
        "ds_read_b64 v[34:35], v96 offset:136\ns_waitcnt lgkmcnt(10)\nv_mfma_f32_32x32x16_bf16 v[16:31], v[32:35], v[40:43], v[16:31]\n"
        "ds_read_b128 v[36:39], v97 offset:0\nds_read_b128 v[40:43], v97 offset:2560\ns_waitcnt lgkmcnt(10)\n"
        "v_mfma_f32_32x32x16_bf16 v[16:31], v[44:47], v[36:39], v[16:31]\nds_read_b64 v[44:45], v96 offset:2176\nds_read_b64 v[46:47], v96 offset:2184\n"
        "s_waitcnt lgkmcnt(9)\nv_mfma_f32_32x32x16_bf16 v[0:15], v[48:51], v[52:55], v[0:15]\nds_read_b64 v[48:49], v96 offset:160\n"
        "ds_read_b64 v[50:51], v96 offset:168\ns_waitcnt lgkmcnt(10)\nv_mfma_f32_32x32x16_bf16 v[16:31], v[48:51], v[56:59], v[16:31]\n"
        "ds_read_b128 v[52:55], v97 offset:32\nds_read_b128 v[56:59], v97 offset:2592\ns_waitcnt lgkmcnt(10)\n"
        "v_mfma_f32_32x32x16_bf16 v[16:31], v[60:63], v[52:55], v[16:31]\nds_read_b64 v[60:61], v96 offset:2208\nds_read_b64 v[62:63], v96 offset:2216\n"
        "v_add_u32 v96, 0x80, v96\nv_subrev_u32 v97, 0xa0, v97\ns_sub_u32 s44, s44, 1\ns_cmp_lg_u32 s44, 0\ns_cbranch_scc1 1b\ns_waitcnt lgkmcnt(0)\ns_nop 7\n"
        : "=&{v[0:15]}"(y0), "=&{v[16:31]}"(y1), "+{v96}"(ua), "+{v97}"(ka) :
        : "memory", "scc", "s44", "v32", "v33", "v34", "v35", "v36", "v37", "v38", "v39", "v40", "v41", "v42", "v43", "v44", "v45", "v46", "v47", "v48",
          "v49", "v50", "v51", "v52", "v53", "v54", "v55", "v56", "v57", "v58", "v59", "v60", "v61", "v62", "v63", "v64", "v65", "v66", "v67", "v68", "v69",
          "v70", "v71", "v72", "v73", "v74", "v75", "v76", "v77", "v78", "v79", "v80", "v81", "v82", "v83", "v84", "v85", "v86", "v87", "v88", "v89", "v90",
          "v91", "v92", "v93", "v94", "v95");
    const f32x16 give = mh ? y0 : y1;
    f32x16 y = mh ? y1 : y0;
#pragma unroll
    for (int i = 0; i < 4; ++i) ST(f32x4, L_CX + tb * 4096 + i * 1024 + lane * 16) = (f32x4){give[4 * i], give[4 * i + 1], give[4 * i + 2], give[4 * i + 3]};
    asm volatile("s_waitcnt lgkmcnt(0)" ::: "memory");
    if (lane == 0) __hip_atomic_store((LAS u32*)(L_SYNC + tb * 4), 1u, __ATOMIC_RELAXED, __HIP_MEMORY_SCOPE_WORKGROUP);
    while (__hip_atomic_load((LAS u32*)(L_SYNC + (tb ^ 2) * 4), __ATOMIC_RELAXED, __HIP_MEMORY_SCOPE_WORKGROUP) == 0) __builtin_amdgcn_s_sleep(1);
    asm volatile("" ::: "memory");
#pragma unroll
    for (int i = 0; i < 4; ++i) {
        const f32x4 o = LD(f32x4, L_CX + (tb ^ 2) * 4096 + i * 1024 + lane * 16);
#pragma unroll
        for (int j = 0; j < 4; ++j) y[4 * i + j] += o[j];
    }
    asm volatile("s_waitcnt lgkmcnt(0)" : "+v"(y) :: "memory");
    if (lane == 0) __hip_atomic_fetch_add((LAS u32*)(L_SYNC + 16), 1u, __ATOMIC_RELAXED, __HIP_MEMORY_SCOPE_WORKGROUP);
    const u32 yo = WS_YT + ((d0 + ch) * 2048 + mh * 1024 + r * 32 + h * 8) * 2;
#pragma unroll
    for (int i = 0; i < 2; ++i)
        __builtin_amdgcn_raw_buffer_store_b128(widen(pk2(y[8 * i], y[8 * i + 1]), pk2(y[8 * i + 2], y[8 * i + 3]), pk2(y[8 * i + 4], y[8 * i + 5]), pk2(y[8 * i + 6], y[8 * i + 7])),
                                               wsr, yo + i * 32, 0, SC1);
    asm volatile("s_waitcnt vmcnt(0)" ::: "memory");
    __builtin_amdgcn_s_setprio(0);
    if (lane == 0) __hip_atomic_store((GAS u64*)(ws + WS_YFLAG + ((d0 + ch) * 2 + mh) * 64), tag << 16, RLX_AGENT);
}

template <int PH>
__global__ __launch_bounds__(512, 2) void s4_mha_mix(Params p) {
    __shared__ __attribute__((aligned(16))) char smem[LDS_BYTES];
    constexpr bool FUSED = PH == 7;
    asm volatile("" :: "s"((u32)(uintptr_t)smem));
    const int lane = threadIdx.x & 63, w = __builtin_amdgcn_readfirstlane(threadIdx.x >> 6);
    const int b = blockIdx.x, xg = b & 7, jb = b >> 3;
    const int hp = 8 * (xg & 1) + (jb & 7), qb = 4 * (xg >> 1) + (jb >> 3), d0 = 64 * xg + 2 * jb;
    const int tb = w & 3, a = w >> 2, r = lane & 31, h = lane >> 5;
    char* ws = p.ws;
    const __amdgpu_buffer_rsrc_t wsr = __builtin_amdgcn_make_buffer_rsrc(ws, 0, WS_END, 0x00020000);
    STAMP(0);
    u64 nonce = 0, pc0;
    asm volatile("s_getpc_b64 %0" : "=s"(pc0));
    const int cb64 = hp & 7, rb64 = 2 * qb + (hp >> 3);

    if (PH & 1) {
        if (a == 0) phase1<0>(p, wsr, ws, lane, w, hp, qb, d0, nonce, pc0 STAMP_PASS); else phase1<1>(p, wsr, ws, lane, w, hp, qb, d0, nonce, pc0 STAMP_PASS);
        BAR(0);
        STAMP(5);
    }
    const u64 tag = (nonce + 1) & 0xffffffffffffull;
    if (PH & 1) {
        if (threadIdx.x < 2) {
            const f32x4 k4 = LD(f32x4, L_KN + lane * 16);
            const u32 kb = (__float_as_uint(fmaxf(fmaxf(k4.x, k4.y), fmaxf(k4.z, k4.w))) >> 16) + 1;
            __hip_atomic_store((GAS u64*)(ws + WS_KVFLAG + (hp * 16 + qb) * 128 + lane * 8), (tag << 16) | min(kb, 0x7fc0u), RLX_AGENT);
        }
        if (!FUSED) {
            for (int i = threadIdx.x; i < (8192 + 2560) / 16; i += 512)
                *(GAS u32x4*)(ws + WS_QF + b * 12288 + i * 16) = LD(u32x4, L_QX + i * 16);
        }
    }
    if (!FUSED && !(PH & 1)) {
        for (int i = threadIdx.x; i < (8192 + 2560) / 16; i += 512)
            ST(u32x4, L_QX + i * 16) = *(const GAS u32x4*)(ws + WS_QF + b * 12288 + i * 16);
        if (threadIdx.x == 0) ST(u32, L_SYNC + 16) = 4;
        __syncthreads();
    }
    if (FUSED || PH == 4) {
        const u32 vo = (lane >> 3) * 1024 + (((lane & 7) ^ ((lane >> 2) & 4)) << 4);
#pragma unroll
        for (int i = 0; i < 2; ++i) dma4(vo, vo + 7168, vo + 14336, vo + 21504, p.wo + ((w * 64 + i * 32) * 1024 + cb64 * 128), L_WO + (w * 64 + i * 32) * 128);
    }
    if ((PH & 1) && a == 1) { conv_wave(wsr, ws, lane, tb, d0, tag); STAMP(6); }

    if (PH & 2) {
        u32x4 q0 = LD(u32x4, L_QX + (a * 4 + tb) * 1024 + lane * 16), q1 = LD(u32x4, L_QX + (a * 4 + ((tb + 1) & 3)) * 1024 + lane * 16);
        u32x4 q2 = LD(u32x4, L_QX + (a * 4 + ((tb + 2) & 3)) * 1024 + lane * 16), q3 = LD(u32x4, L_QX + (a * 4 + ((tb + 3) & 3)) * 1024 + lane * 16);
        const f32x2 qn2 = LD(f32x2, L_QN + a * 512 + lane * 8);
        f32x16 o0 = {}, o1 = {}, o2 = {}, o3 = {};
        float m0 = 0.f, m1 = 0.f, m2 = 0.f, m3 = 0.f;
        const u32 kvo = WS_KV + (hp * 2 + a) * 131072 + tb * 32768;
        const u64 fl = poll_tag<FUSED>(ws + WS_KVFLAG + (hp * 16 + tb * 4 + (lane & 3)) * 128 + a * 8, lane < 4, tag);
        STAMP(7);
        u32 kb = lane < 4 ? (u32)fl & 0xffffu : 0u;
        kb = max(max(__builtin_amdgcn_readlane(kb, 0), __builtin_amdgcn_readlane(kb, 1)), max(__builtin_amdgcn_readlane(kb, 2), __builtin_amdgcn_readlane(kb, 3)));
        const bool safe = __all(fmaxf(qn2.x, qn2.y) * __uint_as_float(kb << 16) <= SAFE2);
        if (__builtin_expect(safe, 1)) {
            const u32 one = (r & 27) == 16 ? 0x3f803f80u : 0u;
            u64 base = (u64)(uintptr_t)(ws + kvo);
            asm volatile(
                "v_mov_b32 v116, v163\nv_mov_b32 v117, v163\nv_mov_b32 v118, v163\nv_mov_b32 v119, v163\nv_mov_b32 v120, v163\nv_mov_b32 v121, v163\n"
                "v_mov_b32 v122, v163\nv_mov_b32 v123, v163\nv_mov_b32 v128, v163\nv_mov_b32 v129, v163\nv_mov_b32 v130, v163\nv_mov_b32 v131, v163\n"
                "v_mov_b32 v132, v163\nv_mov_b32 v133, v163\nv_mov_b32 v134, v163\nv_mov_b32 v135, v163\nv_mov_b32 v140, v163\nv_mov_b32 v141, v163\n"
                "v_mov_b32 v142, v163\nv_mov_b32 v143, v163\nv_mov_b32 v144, v163\nv_mov_b32 v145, v163\nv_mov_b32 v146, v163\nv_mov_b32 v147, v163\n"
                "v_mov_b32 v152, v163\nv_mov_b32 v153, v163\nv_mov_b32 v154, v163\nv_mov_b32 v155, v163\nv_mov_b32 v156, v163\nv_mov_b32 v157, v163\n"
                "v_mov_b32 v158, v163\nv_mov_b32 v159, v163\ns_add_u32 s40, s40, 4096\ns_addc_u32 s41, s41, 0\n"
                "global_load_dwordx4 v[112:115], v161, s[40:41] offset:-4096 sc1\ns_mov_b64 exec, s[42:43]\n"
                "global_load_dwordx4 v[116:119], v162, s[40:41] offset:-3072 sc1\nglobal_load_dwordx4 v[120:123], v162, s[40:41] offset:-2560 sc1\ns_mov_b64 exec, -1\n"
                "global_load_dwordx4 v[124:127], v161, s[40:41] offset:-2048 sc1\ns_mov_b64 exec, s[42:43]\n"
                "global_load_dwordx4 v[128:131], v162, s[40:41] offset:-1024 sc1\nglobal_load_dwordx4 v[132:135], v162, s[40:41] offset:-512 sc1\ns_mov_b64 exec, -1\n"
                "global_load_dwordx4 v[136:139], v161, s[40:41] offset:0 sc1\ns_mov_b64 exec, s[42:43]\n"
                "global_load_dwordx4 v[140:143], v162, s[40:41] offset:1024 sc1\nglobal_load_dwordx4 v[144:147], v162, s[40:41] offset:1536 sc1\ns_mov_b64 exec, -1\n"
                "s_add_u32 s40, s40, 6144\ns_addc_u32 s41, s41, 0\ns_mov_b32 s44, 3\nv_mov_b32 v104, 0\nv_mov_b32 v105, 0\nv_mov_b32 v106, 0\nv_mov_b32 v107, 0\n"
                "v_mov_b32 v108, 0\nv_mov_b32 v109, 0\nv_mov_b32 v110, 0\nv_mov_b32 v111, 0\nv_mov_b32 v0, 0\nv_mov_b32 v1, 0\nv_mov_b32 v2, 0\nv_mov_b32 v3, 0\n"
                "v_mov_b32 v4, 0\nv_mov_b32 v5, 0\nv_mov_b32 v6, 0\nv_mov_b32 v7, 0\nv_mov_b32 v8, 0\nv_mov_b32 v9, 0\nv_mov_b32 v10, 0\nv_mov_b32 v11, 0\n"
                "v_mov_b32 v12, 0\nv_mov_b32 v13, 0\nv_mov_b32 v14, 0\nv_mov_b32 v15, 0\nv_mov_b32 v16, 0\nv_mov_b32 v17, 0\nv_mov_b32 v18, 0\nv_mov_b32 v19, 0\n"
                "v_mov_b32 v20, 0\nv_mov_b32 v21, 0\nv_mov_b32 v22, 0\nv_mov_b32 v23, 0\nv_mov_b32 v24, 0\nv_mov_b32 v25, 0\nv_mov_b32 v26, 0\nv_mov_b32 v27, 0\n"
                "v_mov_b32 v28, 0\nv_mov_b32 v29, 0\nv_mov_b32 v30, 0\nv_mov_b32 v31, 0\nv_mov_b32 v32, 0\nv_mov_b32 v33, 0\nv_mov_b32 v34, 0\nv_mov_b32 v35, 0\n"
                "v_mov_b32 v36, 0\nv_mov_b32 v37, 0\nv_mov_b32 v38, 0\nv_mov_b32 v39, 0\nv_mov_b32 v40, 0\nv_mov_b32 v41, 0\nv_mov_b32 v42, 0\nv_mov_b32 v43, 0\n"
                "v_mov_b32 v44, 0\nv_mov_b32 v45, 0\nv_mov_b32 v46, 0\nv_mov_b32 v47, 0\nv_mov_b32 v48, 0\nv_mov_b32 v49, 0\nv_mov_b32 v50, 0\nv_mov_b32 v51, 0\n"
                "v_mov_b32 v52, 0\nv_mov_b32 v53, 0\nv_mov_b32 v54, 0\nv_mov_b32 v55, 0\nv_mov_b32 v56, 0\nv_mov_b32 v57, 0\nv_mov_b32 v58, 0\nv_mov_b32 v59, 0\n"
                "v_mov_b32 v60, 0\nv_mov_b32 v61, 0\nv_mov_b32 v62, 0\nv_mov_b32 v63, 0\ns_waitcnt vmcnt(6)\n"
                "v_mfma_f32_32x32x16_bf16 v[80:95], v[112:115], v[64:67], 0\ns_nop 11\n.p2align 3\n1:\nv_exp_f32 v80, v80\nv_exp_f32 v81, v81\n"
                "v_smfmac_f32_32x32x32_bf16 v[48:63], v[156:159], v[104:111], v160\nglobal_load_dwordx4 v[148:151], v161, s[40:41] offset:-4096 sc1\n"
                "s_mov_b64 exec, s[42:43]\nglobal_load_dwordx4 v[152:155], v162, s[40:41] offset:-3072 sc1\n"
                "global_load_dwordx4 v[156:159], v162, s[40:41] offset:-2560 sc1\ns_mov_b64 exec, -1\nv_exp_f32 v82, v82\nv_exp_f32 v83, v83\nv_exp_f32 v84, v84\n"
                "v_mfma_f32_32x32x16_bf16 v[96:111], v[112:115], v[68:71], 0\nv_exp_f32 v85, v85\nv_exp_f32 v86, v86\nv_exp_f32 v87, v87\nv_exp_f32 v88, v88\n"
                "v_exp_f32 v89, v89\nv_smfmac_f32_32x32x32_bf16 v[0:15], v[116:119], v[80:87], v160\nv_exp_f32 v90, v90\nv_exp_f32 v91, v91\nv_exp_f32 v92, v92\n"
                "v_exp_f32 v93, v93\nv_exp_f32 v94, v94\nv_exp_f32 v95, v95\nv_exp_f32 v96, v96\nv_exp_f32 v97, v97\n"
                "v_smfmac_f32_32x32x32_bf16 v[0:15], v[120:123], v[88:95], v160\nv_exp_f32 v98, v98\nv_exp_f32 v99, v99\nv_exp_f32 v100, v100\n"
                "v_mfma_f32_32x32x16_bf16 v[80:95], v[112:115], v[72:75], 0\nv_exp_f32 v101, v101\nv_exp_f32 v102, v102\nv_exp_f32 v103, v103\nv_exp_f32 v104, v104\n"
                "v_exp_f32 v105, v105\nv_smfmac_f32_32x32x32_bf16 v[16:31], v[116:119], v[96:103], v160\nv_exp_f32 v106, v106\nv_exp_f32 v107, v107\n"
                "v_exp_f32 v108, v108\nv_exp_f32 v109, v109\nv_exp_f32 v110, v110\nv_exp_f32 v111, v111\nv_exp_f32 v80, v80\nv_exp_f32 v81, v81\n"
                "v_smfmac_f32_32x32x32_bf16 v[16:31], v[120:123], v[104:111], v160\nv_exp_f32 v82, v82\nv_exp_f32 v83, v83\nv_exp_f32 v84, v84\n"
                "v_mfma_f32_32x32x16_bf16 v[96:111], v[112:115], v[76:79], 0\nv_exp_f32 v85, v85\nv_exp_f32 v86, v86\nv_exp_f32 v87, v87\nv_exp_f32 v88, v88\n"
                "v_exp_f32 v89, v89\nv_smfmac_f32_32x32x32_bf16 v[32:47], v[116:119], v[80:87], v160\nv_exp_f32 v90, v90\nv_exp_f32 v91, v91\nv_exp_f32 v92, v92\n"
                "v_exp_f32 v93, v93\nv_exp_f32 v94, v94\nv_exp_f32 v95, v95\nv_exp_f32 v96, v96\nv_exp_f32 v97, v97\n"
                "v_smfmac_f32_32x32x32_bf16 v[32:47], v[120:123], v[88:95], v160\nv_exp_f32 v98, v98\nv_exp_f32 v99, v99\nv_exp_f32 v100, v100\ns_waitcnt vmcnt(6)\n"
                "v_mfma_f32_32x32x16_bf16 v[80:95], v[124:127], v[64:67], 0\nv_exp_f32 v101, v101\nv_exp_f32 v102, v102\nv_exp_f32 v103, v103\nv_exp_f32 v104, v104\n"
                "v_exp_f32 v105, v105\nv_smfmac_f32_32x32x32_bf16 v[48:63], v[116:119], v[96:103], v160\nv_exp_f32 v106, v106\nv_exp_f32 v107, v107\n"
                "v_exp_f32 v108, v108\nv_exp_f32 v109, v109\nv_exp_f32 v110, v110\nv_exp_f32 v111, v111\nv_exp_f32 v80, v80\nv_exp_f32 v81, v81\n"
                "v_smfmac_f32_32x32x32_bf16 v[48:63], v[120:123], v[104:111], v160\nglobal_load_dwordx4 v[112:115], v161, s[40:41] offset:-2048 sc1\n"
                "s_mov_b64 exec, s[42:43]\nglobal_load_dwordx4 v[116:119], v162, s[40:41] offset:-1024 sc1\n"
                "global_load_dwordx4 v[120:123], v162, s[40:41] offset:-512 sc1\ns_mov_b64 exec, -1\nv_exp_f32 v82, v82\nv_exp_f32 v83, v83\nv_exp_f32 v84, v84\n"
                "v_mfma_f32_32x32x16_bf16 v[96:111], v[124:127], v[68:71], 0\nv_exp_f32 v85, v85\nv_exp_f32 v86, v86\nv_exp_f32 v87, v87\nv_exp_f32 v88, v88\n"
                "v_exp_f32 v89, v89\nv_smfmac_f32_32x32x32_bf16 v[0:15], v[128:131], v[80:87], v160\nv_exp_f32 v90, v90\nv_exp_f32 v91, v91\nv_exp_f32 v92, v92\n"
                "v_exp_f32 v93, v93\nv_exp_f32 v94, v94\nv_exp_f32 v95, v95\nv_exp_f32 v96, v96\nv_exp_f32 v97, v97\n"
                "v_smfmac_f32_32x32x32_bf16 v[0:15], v[132:135], v[88:95], v160\nv_exp_f32 v98, v98\nv_exp_f32 v99, v99\nv_exp_f32 v100, v100\n"
                "v_mfma_f32_32x32x16_bf16 v[80:95], v[124:127], v[72:75], 0\nv_exp_f32 v101, v101\nv_exp_f32 v102, v102\nv_exp_f32 v103, v103\nv_exp_f32 v104, v104\n"
                "v_exp_f32 v105, v105\nv_smfmac_f32_32x32x32_bf16 v[16:31], v[128:131], v[96:103], v160\nv_exp_f32 v106, v106\nv_exp_f32 v107, v107\n"
                "v_exp_f32 v108, v108\nv_exp_f32 v109, v109\nv_exp_f32 v110, v110\nv_exp_f32 v111, v111\nv_exp_f32 v80, v80\nv_exp_f32 v81, v81\n"
                "v_smfmac_f32_32x32x32_bf16 v[16:31], v[132:135], v[104:111], v160\nv_exp_f32 v82, v82\nv_exp_f32 v83, v83\nv_exp_f32 v84, v84\n"
                "v_mfma_f32_32x32x16_bf16 v[96:111], v[124:127], v[76:79], 0\nv_exp_f32 v85, v85\nv_exp_f32 v86, v86\nv_exp_f32 v87, v87\nv_exp_f32 v88, v88\n"
                "v_exp_f32 v89, v89\nv_smfmac_f32_32x32x32_bf16 v[32:47], v[128:131], v[80:87], v160\nv_exp_f32 v90, v90\nv_exp_f32 v91, v91\nv_exp_f32 v92, v92\n"
                "v_exp_f32 v93, v93\nv_exp_f32 v94, v94\nv_exp_f32 v95, v95\nv_exp_f32 v96, v96\nv_exp_f32 v97, v97\n"
                "v_smfmac_f32_32x32x32_bf16 v[32:47], v[132:135], v[88:95], v160\nv_exp_f32 v98, v98\nv_exp_f32 v99, v99\nv_exp_f32 v100, v100\ns_waitcnt vmcnt(6)\n"
                "v_mfma_f32_32x32x16_bf16 v[80:95], v[136:139], v[64:67], 0\nv_exp_f32 v101, v101\nv_exp_f32 v102, v102\nv_exp_f32 v103, v103\nv_exp_f32 v104, v104\n"
                "v_exp_f32 v105, v105\nv_smfmac_f32_32x32x32_bf16 v[48:63], v[128:131], v[96:103], v160\nv_exp_f32 v106, v106\nv_exp_f32 v107, v107\n"
                "v_exp_f32 v108, v108\nv_exp_f32 v109, v109\nv_exp_f32 v110, v110\nv_exp_f32 v111, v111\nv_exp_f32 v80, v80\nv_exp_f32 v81, v81\n"
                "v_smfmac_f32_32x32x32_bf16 v[48:63], v[132:135], v[104:111], v160\nglobal_load_dwordx4 v[124:127], v161, s[40:41] offset:0 sc1\n"
                "s_mov_b64 exec, s[42:43]\nglobal_load_dwordx4 v[128:131], v162, s[40:41] offset:1024 sc1\n"
                "global_load_dwordx4 v[132:135], v162, s[40:41] offset:1536 sc1\ns_mov_b64 exec, -1\nv_exp_f32 v82, v82\nv_exp_f32 v83, v83\nv_exp_f32 v84, v84\n"
                "v_mfma_f32_32x32x16_bf16 v[96:111], v[136:139], v[68:71], 0\nv_exp_f32 v85, v85\nv_exp_f32 v86, v86\nv_exp_f32 v87, v87\nv_exp_f32 v88, v88\n"
                "v_exp_f32 v89, v89\nv_smfmac_f32_32x32x32_bf16 v[0:15], v[140:143], v[80:87], v160\nv_exp_f32 v90, v90\nv_exp_f32 v91, v91\nv_exp_f32 v92, v92\n"
                "v_exp_f32 v93, v93\nv_exp_f32 v94, v94\nv_exp_f32 v95, v95\nv_exp_f32 v96, v96\nv_exp_f32 v97, v97\n"
                "v_smfmac_f32_32x32x32_bf16 v[0:15], v[144:147], v[88:95], v160\nv_exp_f32 v98, v98\nv_exp_f32 v99, v99\nv_exp_f32 v100, v100\n"
                "v_mfma_f32_32x32x16_bf16 v[80:95], v[136:139], v[72:75], 0\nv_exp_f32 v101, v101\nv_exp_f32 v102, v102\nv_exp_f32 v103, v103\nv_exp_f32 v104, v104\n"
                "v_exp_f32 v105, v105\nv_smfmac_f32_32x32x32_bf16 v[16:31], v[140:143], v[96:103], v160\nv_exp_f32 v106, v106\nv_exp_f32 v107, v107\n"
                "v_exp_f32 v108, v108\nv_exp_f32 v109, v109\nv_exp_f32 v110, v110\nv_exp_f32 v111, v111\nv_exp_f32 v80, v80\nv_exp_f32 v81, v81\n"
                "v_smfmac_f32_32x32x32_bf16 v[16:31], v[144:147], v[104:111], v160\nv_exp_f32 v82, v82\nv_exp_f32 v83, v83\nv_exp_f32 v84, v84\n"
                "v_mfma_f32_32x32x16_bf16 v[96:111], v[136:139], v[76:79], 0\nv_exp_f32 v85, v85\nv_exp_f32 v86, v86\nv_exp_f32 v87, v87\nv_exp_f32 v88, v88\n"
                "v_exp_f32 v89, v89\nv_smfmac_f32_32x32x32_bf16 v[32:47], v[140:143], v[80:87], v160\nv_exp_f32 v90, v90\nv_exp_f32 v91, v91\nv_exp_f32 v92, v92\n"
                "v_exp_f32 v93, v93\nv_exp_f32 v94, v94\nv_exp_f32 v95, v95\nv_exp_f32 v96, v96\nv_exp_f32 v97, v97\n"
                "v_smfmac_f32_32x32x32_bf16 v[32:47], v[144:147], v[88:95], v160\nv_exp_f32 v98, v98\nv_exp_f32 v99, v99\nv_exp_f32 v100, v100\ns_waitcnt vmcnt(6)\n"
                "v_mfma_f32_32x32x16_bf16 v[80:95], v[148:151], v[64:67], 0\nv_exp_f32 v101, v101\nv_exp_f32 v102, v102\nv_exp_f32 v103, v103\nv_exp_f32 v104, v104\n"
                "v_exp_f32 v105, v105\nv_smfmac_f32_32x32x32_bf16 v[48:63], v[140:143], v[96:103], v160\nv_exp_f32 v106, v106\nv_exp_f32 v107, v107\n"
                "v_exp_f32 v108, v108\nv_exp_f32 v109, v109\nv_exp_f32 v110, v110\nv_exp_f32 v111, v111\nv_exp_f32 v80, v80\nv_exp_f32 v81, v81\n"
                "v_smfmac_f32_32x32x32_bf16 v[48:63], v[144:147], v[104:111], v160\nglobal_load_dwordx4 v[136:139], v161, s[40:41] offset:2048 sc1\n"
                "s_mov_b64 exec, s[42:43]\nglobal_load_dwordx4 v[140:143], v162, s[40:41] offset:3072 sc1\n"
                "global_load_dwordx4 v[144:147], v162, s[40:41] offset:3584 sc1\ns_mov_b64 exec, -1\nv_exp_f32 v82, v82\nv_exp_f32 v83, v83\nv_exp_f32 v84, v84\n"
                "v_mfma_f32_32x32x16_bf16 v[96:111], v[148:151], v[68:71], 0\nv_exp_f32 v85, v85\nv_exp_f32 v86, v86\nv_exp_f32 v87, v87\nv_exp_f32 v88, v88\n"
                "v_exp_f32 v89, v89\nv_smfmac_f32_32x32x32_bf16 v[0:15], v[152:155], v[80:87], v160\nv_exp_f32 v90, v90\nv_exp_f32 v91, v91\nv_exp_f32 v92, v92\n"
                "v_exp_f32 v93, v93\nv_exp_f32 v94, v94\nv_exp_f32 v95, v95\nv_exp_f32 v96, v96\nv_exp_f32 v97, v97\n"
                "v_smfmac_f32_32x32x32_bf16 v[0:15], v[156:159], v[88:95], v160\nv_exp_f32 v98, v98\nv_exp_f32 v99, v99\nv_exp_f32 v100, v100\n"
                "v_mfma_f32_32x32x16_bf16 v[80:95], v[148:151], v[72:75], 0\nv_exp_f32 v101, v101\nv_exp_f32 v102, v102\nv_exp_f32 v103, v103\nv_exp_f32 v104, v104\n"
                "v_exp_f32 v105, v105\nv_smfmac_f32_32x32x32_bf16 v[16:31], v[152:155], v[96:103], v160\nv_exp_f32 v106, v106\nv_exp_f32 v107, v107\n"
                "v_exp_f32 v108, v108\nv_exp_f32 v109, v109\nv_exp_f32 v110, v110\nv_exp_f32 v111, v111\nv_exp_f32 v80, v80\nv_exp_f32 v81, v81\n"
                "v_smfmac_f32_32x32x32_bf16 v[16:31], v[156:159], v[104:111], v160\nv_exp_f32 v82, v82\nv_exp_f32 v83, v83\nv_exp_f32 v84, v84\n"
                "v_mfma_f32_32x32x16_bf16 v[96:111], v[148:151], v[76:79], 0\nv_exp_f32 v85, v85\nv_exp_f32 v86, v86\nv_exp_f32 v87, v87\nv_exp_f32 v88, v88\n"
                "v_exp_f32 v89, v89\nv_smfmac_f32_32x32x32_bf16 v[32:47], v[152:155], v[80:87], v160\nv_exp_f32 v90, v90\nv_exp_f32 v91, v91\nv_exp_f32 v92, v92\n"
                "v_exp_f32 v93, v93\nv_exp_f32 v94, v94\nv_exp_f32 v95, v95\nv_exp_f32 v96, v96\nv_exp_f32 v97, v97\n"
                "v_smfmac_f32_32x32x32_bf16 v[32:47], v[156:159], v[88:95], v160\nv_exp_f32 v98, v98\nv_exp_f32 v99, v99\nv_exp_f32 v100, v100\ns_waitcnt vmcnt(6)\n"
                "v_mfma_f32_32x32x16_bf16 v[80:95], v[112:115], v[64:67], 0\nv_exp_f32 v101, v101\nv_exp_f32 v102, v102\nv_exp_f32 v103, v103\nv_exp_f32 v104, v104\n"
                "v_exp_f32 v105, v105\nv_smfmac_f32_32x32x32_bf16 v[48:63], v[152:155], v[96:103], v160\nv_exp_f32 v106, v106\nv_exp_f32 v107, v107\n"
                "v_exp_f32 v108, v108\nv_exp_f32 v109, v109\nv_exp_f32 v110, v110\nv_exp_f32 v111, v111\ns_add_u32 s40, s40, 8192\ns_addc_u32 s41, s41, 0\n"
                "s_sub_u32 s44, s44, 1\ns_cmp_lg_u32 s44, 0\ns_cbranch_scc1 1b\nv_exp_f32 v80, v80\nv_exp_f32 v81, v81\n"
                "v_smfmac_f32_32x32x32_bf16 v[48:63], v[156:159], v[104:111], v160\nglobal_load_dwordx4 v[148:151], v161, s[40:41] offset:-4096 sc1\n"
                "s_mov_b64 exec, s[42:43]\nglobal_load_dwordx4 v[152:155], v162, s[40:41] offset:-3072 sc1\n"
                "global_load_dwordx4 v[156:159], v162, s[40:41] offset:-2560 sc1\ns_mov_b64 exec, -1\nv_exp_f32 v82, v82\nv_exp_f32 v83, v83\nv_exp_f32 v84, v84\n"
                "v_mfma_f32_32x32x16_bf16 v[96:111], v[112:115], v[68:71], 0\nv_exp_f32 v85, v85\nv_exp_f32 v86, v86\nv_exp_f32 v87, v87\nv_exp_f32 v88, v88\n"
                "v_exp_f32 v89, v89\nv_smfmac_f32_32x32x32_bf16 v[0:15], v[116:119], v[80:87], v160\nv_exp_f32 v90, v90\nv_exp_f32 v91, v91\nv_exp_f32 v92, v92\n"
                "v_exp_f32 v93, v93\nv_exp_f32 v94, v94\nv_exp_f32 v95, v95\nv_exp_f32 v96, v96\nv_exp_f32 v97, v97\n"
                "v_smfmac_f32_32x32x32_bf16 v[0:15], v[120:123], v[88:95], v160\nv_exp_f32 v98, v98\nv_exp_f32 v99, v99\nv_exp_f32 v100, v100\n"
                "v_mfma_f32_32x32x16_bf16 v[80:95], v[112:115], v[72:75], 0\nv_exp_f32 v101, v101\nv_exp_f32 v102, v102\nv_exp_f32 v103, v103\nv_exp_f32 v104, v104\n"
                "v_exp_f32 v105, v105\nv_smfmac_f32_32x32x32_bf16 v[16:31], v[116:119], v[96:103], v160\nv_exp_f32 v106, v106\nv_exp_f32 v107, v107\n"
                "v_exp_f32 v108, v108\nv_exp_f32 v109, v109\nv_exp_f32 v110, v110\nv_exp_f32 v111, v111\nv_exp_f32 v80, v80\nv_exp_f32 v81, v81\n"
                "v_smfmac_f32_32x32x32_bf16 v[16:31], v[120:123], v[104:111], v160\nv_exp_f32 v82, v82\nv_exp_f32 v83, v83\nv_exp_f32 v84, v84\n"
                "v_mfma_f32_32x32x16_bf16 v[96:111], v[112:115], v[76:79], 0\nv_exp_f32 v85, v85\nv_exp_f32 v86, v86\nv_exp_f32 v87, v87\nv_exp_f32 v88, v88\n"
                "v_exp_f32 v89, v89\nv_smfmac_f32_32x32x32_bf16 v[32:47], v[116:119], v[80:87], v160\nv_exp_f32 v90, v90\nv_exp_f32 v91, v91\nv_exp_f32 v92, v92\n"
                "v_exp_f32 v93, v93\nv_exp_f32 v94, v94\nv_exp_f32 v95, v95\nv_exp_f32 v96, v96\nv_exp_f32 v97, v97\n"
                "v_smfmac_f32_32x32x32_bf16 v[32:47], v[120:123], v[88:95], v160\nv_exp_f32 v98, v98\nv_exp_f32 v99, v99\nv_exp_f32 v100, v100\ns_waitcnt vmcnt(6)\n"
                "v_mfma_f32_32x32x16_bf16 v[80:95], v[124:127], v[64:67], 0\nv_exp_f32 v101, v101\nv_exp_f32 v102, v102\nv_exp_f32 v103, v103\nv_exp_f32 v104, v104\n"
                "v_exp_f32 v105, v105\nv_smfmac_f32_32x32x32_bf16 v[48:63], v[116:119], v[96:103], v160\nv_exp_f32 v106, v106\nv_exp_f32 v107, v107\n"
                "v_exp_f32 v108, v108\nv_exp_f32 v109, v109\nv_exp_f32 v110, v110\nv_exp_f32 v111, v111\nv_exp_f32 v80, v80\nv_exp_f32 v81, v81\n"
                "v_smfmac_f32_32x32x32_bf16 v[48:63], v[120:123], v[104:111], v160\nv_exp_f32 v82, v82\nv_exp_f32 v83, v83\nv_exp_f32 v84, v84\n"
                "v_mfma_f32_32x32x16_bf16 v[96:111], v[124:127], v[68:71], 0\nv_exp_f32 v85, v85\nv_exp_f32 v86, v86\nv_exp_f32 v87, v87\nv_exp_f32 v88, v88\n"
                "v_exp_f32 v89, v89\nv_smfmac_f32_32x32x32_bf16 v[0:15], v[128:131], v[80:87], v160\nv_exp_f32 v90, v90\nv_exp_f32 v91, v91\nv_exp_f32 v92, v92\n"
                "v_exp_f32 v93, v93\nv_exp_f32 v94, v94\nv_exp_f32 v95, v95\nv_exp_f32 v96, v96\nv_exp_f32 v97, v97\n"
                "v_smfmac_f32_32x32x32_bf16 v[0:15], v[132:135], v[88:95], v160\nv_exp_f32 v98, v98\nv_exp_f32 v99, v99\nv_exp_f32 v100, v100\n"
                "v_mfma_f32_32x32x16_bf16 v[80:95], v[124:127], v[72:75], 0\nv_exp_f32 v101, v101\nv_exp_f32 v102, v102\nv_exp_f32 v103, v103\nv_exp_f32 v104, v104\n"
                "v_exp_f32 v105, v105\nv_smfmac_f32_32x32x32_bf16 v[16:31], v[128:131], v[96:103], v160\nv_exp_f32 v106, v106\nv_exp_f32 v107, v107\n"
                "v_exp_f32 v108, v108\nv_exp_f32 v109, v109\nv_exp_f32 v110, v110\nv_exp_f32 v111, v111\nv_exp_f32 v80, v80\nv_exp_f32 v81, v81\n"
                "v_smfmac_f32_32x32x32_bf16 v[16:31], v[132:135], v[104:111], v160\nv_exp_f32 v82, v82\nv_exp_f32 v83, v83\nv_exp_f32 v84, v84\n"
                "v_mfma_f32_32x32x16_bf16 v[96:111], v[124:127], v[76:79], 0\nv_exp_f32 v85, v85\nv_exp_f32 v86, v86\nv_exp_f32 v87, v87\nv_exp_f32 v88, v88\n"
                "v_exp_f32 v89, v89\nv_smfmac_f32_32x32x32_bf16 v[32:47], v[128:131], v[80:87], v160\nv_exp_f32 v90, v90\nv_exp_f32 v91, v91\nv_exp_f32 v92, v92\n"
                "v_exp_f32 v93, v93\nv_exp_f32 v94, v94\nv_exp_f32 v95, v95\nv_exp_f32 v96, v96\nv_exp_f32 v97, v97\n"
                "v_smfmac_f32_32x32x32_bf16 v[32:47], v[132:135], v[88:95], v160\nv_exp_f32 v98, v98\nv_exp_f32 v99, v99\nv_exp_f32 v100, v100\ns_waitcnt vmcnt(3)\n"
                "v_mfma_f32_32x32x16_bf16 v[80:95], v[136:139], v[64:67], 0\nv_exp_f32 v101, v101\nv_exp_f32 v102, v102\nv_exp_f32 v103, v103\nv_exp_f32 v104, v104\n"
                "v_exp_f32 v105, v105\nv_smfmac_f32_32x32x32_bf16 v[48:63], v[128:131], v[96:103], v160\nv_exp_f32 v106, v106\nv_exp_f32 v107, v107\n"
                "v_exp_f32 v108, v108\nv_exp_f32 v109, v109\nv_exp_f32 v110, v110\nv_exp_f32 v111, v111\nv_exp_f32 v80, v80\nv_exp_f32 v81, v81\n"
                "v_smfmac_f32_32x32x32_bf16 v[48:63], v[132:135], v[104:111], v160\nv_exp_f32 v82, v82\nv_exp_f32 v83, v83\nv_exp_f32 v84, v84\n"
                "v_mfma_f32_32x32x16_bf16 v[96:111], v[136:139], v[68:71], 0\nv_exp_f32 v85, v85\nv_exp_f32 v86, v86\nv_exp_f32 v87, v87\nv_exp_f32 v88, v88\n"
                "v_exp_f32 v89, v89\nv_smfmac_f32_32x32x32_bf16 v[0:15], v[140:143], v[80:87], v160\nv_exp_f32 v90, v90\nv_exp_f32 v91, v91\nv_exp_f32 v92, v92\n"
                "v_exp_f32 v93, v93\nv_exp_f32 v94, v94\nv_exp_f32 v95, v95\nv_exp_f32 v96, v96\nv_exp_f32 v97, v97\n"
                "v_smfmac_f32_32x32x32_bf16 v[0:15], v[144:147], v[88:95], v160\nv_exp_f32 v98, v98\nv_exp_f32 v99, v99\nv_exp_f32 v100, v100\n"
                "v_mfma_f32_32x32x16_bf16 v[80:95], v[136:139], v[72:75], 0\nv_exp_f32 v101, v101\nv_exp_f32 v102, v102\nv_exp_f32 v103, v103\nv_exp_f32 v104, v104\n"
                "v_exp_f32 v105, v105\nv_smfmac_f32_32x32x32_bf16 v[16:31], v[140:143], v[96:103], v160\nv_exp_f32 v106, v106\nv_exp_f32 v107, v107\n"
                "v_exp_f32 v108, v108\nv_exp_f32 v109, v109\nv_exp_f32 v110, v110\nv_exp_f32 v111, v111\nv_exp_f32 v80, v80\nv_exp_f32 v81, v81\n"
                "v_smfmac_f32_32x32x32_bf16 v[16:31], v[144:147], v[104:111], v160\nv_exp_f32 v82, v82\nv_exp_f32 v83, v83\nv_exp_f32 v84, v84\n"
                "v_mfma_f32_32x32x16_bf16 v[96:111], v[136:139], v[76:79], 0\nv_exp_f32 v85, v85\nv_exp_f32 v86, v86\nv_exp_f32 v87, v87\nv_exp_f32 v88, v88\n"
                "v_exp_f32 v89, v89\nv_smfmac_f32_32x32x32_bf16 v[32:47], v[140:143], v[80:87], v160\nv_exp_f32 v90, v90\nv_exp_f32 v91, v91\nv_exp_f32 v92, v92\n"
                "v_exp_f32 v93, v93\nv_exp_f32 v94, v94\nv_exp_f32 v95, v95\nv_exp_f32 v96, v96\nv_exp_f32 v97, v97\n"
                "v_smfmac_f32_32x32x32_bf16 v[32:47], v[144:147], v[88:95], v160\nv_exp_f32 v98, v98\nv_exp_f32 v99, v99\nv_exp_f32 v100, v100\ns_waitcnt vmcnt(0)\n"
                "v_mfma_f32_32x32x16_bf16 v[80:95], v[148:151], v[64:67], 0\nv_exp_f32 v101, v101\nv_exp_f32 v102, v102\nv_exp_f32 v103, v103\nv_exp_f32 v104, v104\n"
                "v_exp_f32 v105, v105\nv_smfmac_f32_32x32x32_bf16 v[48:63], v[140:143], v[96:103], v160\nv_exp_f32 v106, v106\nv_exp_f32 v107, v107\n"
                "v_exp_f32 v108, v108\nv_exp_f32 v109, v109\nv_exp_f32 v110, v110\nv_exp_f32 v111, v111\nv_exp_f32 v80, v80\nv_exp_f32 v81, v81\n"
                "v_smfmac_f32_32x32x32_bf16 v[48:63], v[144:147], v[104:111], v160\nv_exp_f32 v82, v82\nv_exp_f32 v83, v83\nv_exp_f32 v84, v84\n"
                "v_mfma_f32_32x32x16_bf16 v[96:111], v[148:151], v[68:71], 0\nv_exp_f32 v85, v85\nv_exp_f32 v86, v86\nv_exp_f32 v87, v87\nv_exp_f32 v88, v88\n"
                "v_exp_f32 v89, v89\nv_smfmac_f32_32x32x32_bf16 v[0:15], v[152:155], v[80:87], v160\nv_exp_f32 v90, v90\nv_exp_f32 v91, v91\nv_exp_f32 v92, v92\n"
                "v_exp_f32 v93, v93\nv_exp_f32 v94, v94\nv_exp_f32 v95, v95\nv_exp_f32 v96, v96\nv_exp_f32 v97, v97\n"
                "v_smfmac_f32_32x32x32_bf16 v[0:15], v[156:159], v[88:95], v160\nv_exp_f32 v98, v98\nv_exp_f32 v99, v99\nv_exp_f32 v100, v100\n"
                "v_mfma_f32_32x32x16_bf16 v[80:95], v[148:151], v[72:75], 0\nv_exp_f32 v101, v101\nv_exp_f32 v102, v102\nv_exp_f32 v103, v103\nv_exp_f32 v104, v104\n"
                "v_exp_f32 v105, v105\nv_smfmac_f32_32x32x32_bf16 v[16:31], v[152:155], v[96:103], v160\nv_exp_f32 v106, v106\nv_exp_f32 v107, v107\n"
                "v_exp_f32 v108, v108\nv_exp_f32 v109, v109\nv_exp_f32 v110, v110\nv_exp_f32 v111, v111\nv_exp_f32 v80, v80\nv_exp_f32 v81, v81\n"
                "v_smfmac_f32_32x32x32_bf16 v[16:31], v[156:159], v[104:111], v160\nv_exp_f32 v82, v82\nv_exp_f32 v83, v83\nv_exp_f32 v84, v84\n"
                "v_mfma_f32_32x32x16_bf16 v[96:111], v[148:151], v[76:79], 0\nv_exp_f32 v85, v85\nv_exp_f32 v86, v86\nv_exp_f32 v87, v87\nv_exp_f32 v88, v88\n"
                "v_exp_f32 v89, v89\nv_smfmac_f32_32x32x32_bf16 v[32:47], v[152:155], v[80:87], v160\nv_exp_f32 v90, v90\nv_exp_f32 v91, v91\nv_exp_f32 v92, v92\n"
                "v_exp_f32 v93, v93\nv_exp_f32 v94, v94\nv_exp_f32 v95, v95\nv_exp_f32 v96, v96\nv_exp_f32 v97, v97\n"
                "v_smfmac_f32_32x32x32_bf16 v[32:47], v[156:159], v[88:95], v160\nv_exp_f32 v98, v98\nv_exp_f32 v99, v99\nv_exp_f32 v100, v100\nv_exp_f32 v101, v101\n"
                "v_exp_f32 v102, v102\nv_exp_f32 v103, v103\nv_exp_f32 v104, v104\nv_exp_f32 v105, v105\n"
                "v_smfmac_f32_32x32x32_bf16 v[48:63], v[152:155], v[96:103], v160\nv_exp_f32 v106, v106\nv_exp_f32 v107, v107\nv_exp_f32 v108, v108\n"
                "v_exp_f32 v109, v109\nv_exp_f32 v110, v110\nv_exp_f32 v111, v111\ns_nop 1\nv_smfmac_f32_32x32x32_bf16 v[48:63], v[156:159], v[104:111], v160\n"
                "s_nop 11\n"
                : "=&{v[0:15]}"(o0), "=&{v[16:31]}"(o1), "=&{v[32:47]}"(o2), "=&{v[48:63]}"(o3), "+{s[40:41]}"(base)
                : "{v[64:67]}"(q0), "{v[68:71]}"(q1), "{v[72:75]}"(q2), "{v[76:79]}"(q3), "{v160}"(0xddddu), "{v161}"(lane * 16),
                  "{v162}"(h * 256 + (r & 15) * 16), "{v163}"(one), "{s[42:43]}"(0x0000ffff0000ffffull)
                : "memory", "scc", "s44", "v80", "v81", "v82", "v83", "v84", "v85", "v86", "v87", "v88", "v89", "v90", "v91", "v92", "v93", "v94", "v95",
                  "v96", "v97", "v98", "v99", "v100", "v101", "v102", "v103", "v104", "v105", "v106", "v107", "v108", "v109", "v110", "v111", "v112", "v113",
                  "v114", "v115", "v116", "v117", "v118", "v119", "v120", "v121", "v122", "v123", "v124", "v125", "v126", "v127", "v128", "v129", "v130",
                  "v131", "v132", "v133", "v134", "v135", "v136", "v137", "v138", "v139", "v140", "v141", "v142", "v143", "v144", "v145", "v146", "v147",
                  "v148", "v149", "v150", "v151", "v152", "v153", "v154", "v155", "v156", "v157", "v158", "v159");
        } else {
            m0 = m1 = m2 = m3 = -INFINITY;
            const u32x4 ones = {0x3f803f80u, 0x3f803f80u, 0x3f803f80u, 0x3f803f80u};
#pragma unroll 1
            for (int t = 0; t < 16; ++t) {
                const u32x4 kf = __builtin_amdgcn_raw_buffer_load_b128(wsr, kvo + t * 2048 + lane * 16, 0, SC1);
                u32x4 v0 = __builtin_amdgcn_raw_buffer_load_b128(wsr, kvo + t * 2048 + 1024 + h * 256 + (r & 15) * 16, 0, SC1);
                u32x4 v1 = __builtin_amdgcn_raw_buffer_load_b128(wsr, kvo + t * 2048 + 1536 + h * 256 + (r & 15) * 16, 0, SC1);
                if (r >= 16) { v0 = ones; v1 = ones; }
                auto step = [&](const u32x4& q, f32x16& o, float& m) {
                    f32x16 s = mfma32(kf, q, (f32x16){});
                    float mx = s[0];
#pragma unroll
                    for (int i = 1; i < 16; ++i) mx = fmaxf(mx, s[i]);
                    const float mn = fmaxf(m, halves_max(mx)), al = __builtin_amdgcn_exp2f(m - mn);
                    m = mn;
#pragma unroll
                    for (int i = 0; i < 16; ++i) { s[i] = __builtin_amdgcn_exp2f(s[i] - mn); o[i] *= al; }
                    const f32x8 lo = __builtin_shufflevector(s, s, 0, 1, 2, 3, 4, 5, 6, 7), hi = __builtin_shufflevector(s, s, 8, 9, 10, 11, 12, 13, 14, 15);
                    o = __builtin_amdgcn_smfmac_f32_32x32x32_bf16(__builtin_bit_cast(bf16x8, v0), __builtin_bit_cast(bf16x16, lo), o, 0xdddd, 0, 0);
                    o = __builtin_amdgcn_smfmac_f32_32x32x32_bf16(__builtin_bit_cast(bf16x8, v1), __builtin_bit_cast(bf16x16, hi), o, 0xdddd, 0, 0);
                };
                step(q0, o0, m0); step(q1, o1, m1); step(q2, o2, m2); step(q3, o3, m3);
            }
        }
        STAMP(8);
        while (__hip_atomic_load((LAS u32*)(L_SYNC + 16), __ATOMIC_RELAXED, __HIP_MEMORY_SCOPE_WORKGROUP) != 4) __builtin_amdgcn_s_sleep(1);
        asm volatile("" ::: "memory");
        auto put = [&](int i, const f32x16& o, float m) {
            const u32 rec = L_PART + ((a * 4 + ((tb + i) & 3)) * 3 + i - 1) * 2560;
            ST(f32x4, rec + lane * 16) = (f32x4){o[0], o[1], o[2], o[3]};
            ST(f32x4, rec + 1024 + lane * 16) = (f32x4){o[4], o[5], o[6], o[7]};
            ST(f32x2, rec + 2048 + lane * 8) = (f32x2){o[8], m};
        };
        put(1, o1, m1); put(2, o2, m2); put(3, o3, m3);
        BAR(0);
        {
            f32x4 pa[3], pb[3]; f32x2 lm[3];
#pragma unroll
            for (int i = 0; i < 3; ++i) {
                const u32 rec = L_PART + ((a * 4 + tb) * 3 + i) * 2560;
                pa[i] = LD(f32x4, rec + lane * 16); pb[i] = LD(f32x4, rec + 1024 + lane * 16); lm[i] = LD(f32x2, rec + 2048 + lane * 8);
            }
            const float mm = fmaxf(fmaxf(m0, lm[0].y), fmaxf(lm[1].y, lm[2].y));
            const float s0 = __builtin_amdgcn_exp2f(m0 - mm);
            float l = o0[8] * s0, acc[8];
#pragma unroll
            for (int j = 0; j < 8; ++j) acc[j] = o0[j] * s0;
#pragma unroll
            for (int i = 0; i < 3; ++i) {
                const float s = __builtin_amdgcn_exp2f(lm[i].y - mm);
                l = fmaf(lm[i].x, s, l);
#pragma unroll
                for (int j = 0; j < 4; ++j) { acc[j] = fmaf(pa[i][j], s, acc[j]); acc[4 + j] = fmaf(pb[i][j], s, acc[4 + j]); }
            }
            const float rl = __builtin_amdgcn_rcpf(l);
            const u32x4 of = {pk2(acc[0] * rl, acc[1] * rl), pk2(acc[2] * rl, acc[3] * rl), pk2(acc[4] * rl, acc[5] * rl), pk2(acc[6] * rl, acc[7] * rl)};
            __builtin_amdgcn_raw_buffer_store_b128(of, wsr, WS_OF + ((qb * 4 + tb) * 32 + hp * 2 + a) * 1024 + lane * 16, 0, SC1);
        }
        BAR(0);
        if (FUSED && threadIdx.x == 0) __hip_atomic_store((GAS u64*)(ws + WS_OFLAG + (qb * 16 + hp) * 128), tag << 16, RLX_AGENT);
        STAMP(9);
    }

    if (PH & 4) {
        const int tk = w & 1, k4 = w >> 1, g1 = (lane >> 4) & 1, q4 = (lane >> 2) & 3;
        if (!FUSED) BAR(0);
        poll_tag<FUSED>(ws + WS_YFLAG + ((cb64 * 64 + w * 8 + (lane & 7)) * 2 + (rb64 >> 4)) * 64, lane < 8, tag);
        const u32x4 yv = __builtin_amdgcn_raw_buffer_load_b128(wsr, WS_YT + ((cb64 * 64 + w * 8 + (lane >> 3)) * 2048 + rb64 * 64) * 2 + (lane & 7) * 16, 0, SC1);
        u32x4 wf[2][8];
#pragma unroll
        for (int c = 0; c < 2; ++c) {
            const u32 wa = L_WO + (k4 * 128 + 4 * h + q4) * 128 + (((c ^ (k4 >> 1) ^ (q4 >> 1)) & 1) << 6) + ((g1 ^ (k4 & 1)) << 5) + (lane & 3) * 8;
#pragma unroll
            for (int i = 0; i < 8; ++i) wf[c][i] = trd2(wa + i * 2048, wa + i * 2048 + 1024);
        }
        const float g = LD(float, L_G + ((hp >> 3) * 64 + tk * 32 + r) * 4);
        ST(u32x4, L_YT + (w * 64 + lane) * 16) = yv;
        poll_tag<FUSED>(ws + WS_OFLAG + (qb * 16 + k4 * 4 + (lane & 3)) * 128, lane < 4, tag);
        STAMP(10);
        u32x4 of[8];
#pragma unroll
        for (int i = 0; i < 8; ++i) of[i] = __builtin_amdgcn_raw_buffer_load_b128(wsr, WS_OF + ((rb64 * 2 + tk) * 32 + k4 * 8 + i) * 1024 + lane * 16, 0, SC1);
        __builtin_amdgcn_sched_barrier(0);
        f32x16 c0 = {}, c1 = {};
#pragma unroll
        for (int i = 0; i < 8; ++i) { c0 = mfma32(wf[0][i], of[i], c0); c1 = mfma32(wf[1][i], of[i], c1); }
        STAMP(11);
        auto put = [&](int i, const f32x16& c, int o) {
            const u32 rec = L_RED + ((tk * 4 + (i ^ k4)) * 3 + i - 1) * 2048 + lane * 16;
            ST(f32x4, rec) = (f32x4){c[o], c[o + 1], c[o + 2], c[o + 3]};
            ST(f32x4, rec + 1024) = (f32x4){c[o + 4], c[o + 5], c[o + 6], c[o + 7]};
        };
        put(1, c0, 8); put(2, c1, 0); put(3, c1, 8);
        BAR_LDS();
        if (FUSED && b == 0 && threadIdx.x == 0) *(GAS u64*)(ws + WS_NONCE) = nonce + 1;
        float acc[8];
#pragma unroll
        for (int j = 0; j < 8; ++j) acc[j] = c0[j];
#pragma unroll
        for (int i = 0; i < 3; ++i) {
            const u32 rec = L_RED + ((tk * 4 + k4) * 3 + i) * 2048 + lane * 16;
            const f32x4 u = LD(f32x4, rec), v = LD(f32x4, rec + 1024);
#pragma unroll
            for (int j = 0; j < 4; ++j) { acc[j] += u[j]; acc[4 + j] += v[j]; }
        }
        const u32 ya = L_YT + (k4 * 16 + 4 * h + q4) * 128 + (tk * 32 + g1 * 16 + (lane & 3) * 4) * 2;
        const u32x4 yf = trd2(ya, ya + 1024);
        u32 pk[4];
#pragma unroll
        for (int j = 0; j < 4; ++j)
            pk[j] = pk2(fmaf(g, bflo(yf[j]) - acc[2 * j], acc[2 * j]), fmaf(g, bfhi(yf[j]) - acc[2 * j + 1], acc[2 * j + 1]));
        *(GAS u32x4*)(p.out + ((rb64 * 64 + tk * 32 + r) * 512 + cb64 * 64 + k4 * 16 + h * 8) * 2) = widen(pk[0], pk[1], pk[2], pk[3]);
        STAMP(12);
    }
}

extern "C" void kernel_launch(void* const* d_in, const int* in_sizes, int n_in, void* d_out, int out_size, void* d_ws, size_t ws_size,
                              hipStream_t stream) {
    Params p;
    p.x = (const char*)d_in[0]; p.a_re = (const char*)d_in[1]; p.a_im = (const char*)d_in[2]; p.b_re = (const char*)d_in[3];
    p.b_im = (const char*)d_in[4]; p.c_re = (const char*)d_in[5]; p.c_im = (const char*)d_in[6]; p.log_dt = (const char*)d_in[7];
    p.dskip = (const char*)d_in[8]; p.wqkv = (const char*)d_in[10]; p.wo = (const char*)d_in[11]; p.gate_w = (const char*)d_in[12];
    p.out = (char*)d_out; p.ws = (char*)d_ws;
    s4_mha_mix<7><<<256, 512, 0, stream>>>(p);
}
```

```cpp
#define r2 OUT_AUX 2
#include <hip/hip_runtime.h>
#include <hip/hip_bf16.h>
#include <stdint.h>
#pragma clang diagnostic ignored "-Wint-to-pointer-cast"

#define NLAUNCH 1

#define LAS __attribute__((address_space(3)))
#define GAS __attribute__((address_space(1)))
typedef unsigned u32;
typedef unsigned long long u64;
typedef __attribute__((ext_vector_type(2))) u32 u32x2;
typedef __attribute__((ext_vector_type(4))) u32 u32x4;
typedef __attribute__((ext_vector_type(8))) u32 u32x8;
typedef __attribute__((ext_vector_type(2))) float f32x2;
typedef __attribute__((ext_vector_type(4))) float f32x4;
typedef __attribute__((ext_vector_type(8))) float f32x8;
typedef __attribute__((ext_vector_type(16))) float f32x16;
typedef __attribute__((ext_vector_type(2))) __bf16 bf16x2;
typedef __attribute__((ext_vector_type(8))) __bf16 bf16x8;
typedef __attribute__((ext_vector_type(16))) __bf16 bf16x16;
typedef __attribute__((ext_vector_type(4))) short s16x4;
typedef __attribute__((ext_vector_type(2))) unsigned short u16x2;

constexpr u32 WS_KV = 0;
constexpr u32 WS_OF = 4u << 20;
constexpr u32 WS_YT = 6u << 20;
constexpr u32 WS_KVFLAG = 8u << 20;
constexpr u32 WS_OFLAG = WS_KVFLAG + 32768;
constexpr u32 WS_YFLAG = WS_OFLAG + 32768;
constexpr u32 WS_NONCE = WS_YFLAG + 65536;
constexpr u32 WS_QF = 9u << 20;
constexpr u32 WS_END = 12u << 20;
constexpr u32 WS_STAMP = 16u << 20;

constexpr u32 CHUNK = 28672, L_W = 16384;
constexpr u32 L_TAB = 86016, TAB_ROW = 528, TAB_A = 21 * TAB_ROW, TAB_CH = 53 * TAB_ROW;
constexpr u32 L_KF = L_TAB + 2 * TAB_CH, KF_CH = 4608;
constexpr u32 L_QX = L_KF + 2 * KF_CH;
constexpr u32 L_MISC = L_QX + 8192;
constexpr u32 L_GW = L_MISC;
constexpr u32 L_QN = L_GW + 1024;
constexpr u32 L_G = L_QN + 1024;
constexpr u32 L_KN = L_G + 512;
constexpr u32 L_SYNC = L_KN + 32;
constexpr u32 L_UC = L_TAB, UC_COPY = 4160, UC_CH = 4 * UC_COPY;
constexpr u32 L_KB = L_UC + 2 * UC_CH + 64, KB_CH = 96 * 80;
constexpr u32 L_WO = 0;
constexpr u32 L_CX = 65536;
constexpr u32 L_PART = 65536;
constexpr u32 L_RED = 65536;
constexpr u32 L_YT = L_QX;
constexpr u32 LDS_BYTES = 163840;
static_assert(L_SYNC + 32 <= LDS_BYTES && L_KB + 2 * KB_CH <= L_KF, "lds map");

constexpr float LOG2E = 1.4426950408889634f;
constexpr float QSCALE = 0.25f * LOG2E;
constexpr float SAFE2 = 64.f * 64.f;
constexpr int CODE_BYTES = 22 * 1024;
constexpr u32 SPIN_CAP = 1u << 17;

#define LD(T, off) (*(const LAS T*)(u32)(off))
#define ST(T, off) (*(LAS T*)(u32)(off))
#define RLX_AGENT __ATOMIC_RELAXED, __HIP_MEMORY_SCOPE_AGENT
#define SC1 16
#define LGKM0() do { asm volatile("" ::: "memory"); __builtin_amdgcn_s_waitcnt(0xc07f); } while (0)
#define BAR(N) do { LGKM0(); asm volatile("s_waitcnt vmcnt(" #N ") lgkmcnt(0)\n\ts_barrier" ::: "memory"); } while (0)
#define BAR_LDS() do { LGKM0(); asm volatile("s_waitcnt lgkmcnt(0)\n\ts_barrier" ::: "memory"); } while (0)

#define STAMP(i) do { } while (0)
#define STAMP_ARG
#define STAMP_PASS

__device__ __forceinline__ u32 pk2(float a, float b) { bf16x2 v = {(__bf16)a, (__bf16)b}; return __builtin_bit_cast(u32, v); }
__device__ __forceinline__ float dot2(u32 a, u32 b, float c) {
    return __builtin_amdgcn_fdot2_f32_bf16(__builtin_bit_cast(bf16x2, a), __builtin_bit_cast(bf16x2, b), c, false); }
__device__ __forceinline__ float bflo(u32 v) { return __uint_as_float(v << 16); }
__device__ __forceinline__ float bfhi(u32 v) { return __uint_as_float(v & 0xffff0000u); }
__device__ __forceinline__ f32x16 mfma32(u32x4 a, u32x4 b, f32x16 c) {
    return __builtin_amdgcn_mfma_f32_32x32x16_bf16(__builtin_bit_cast(bf16x8, a), __builtin_bit_cast(bf16x8, b), c, 0, 0, 0); }
__device__ __forceinline__ f32x4 mfma16(u32x4 a, u32x4 b, f32x4 c) {
    return __builtin_amdgcn_mfma_f32_16x16x32_bf16(__builtin_bit_cast(bf16x8, a), __builtin_bit_cast(bf16x8, b), c, 0, 0, 0); }
__device__ __forceinline__ u32x2 trd(u32 off) {
    return __builtin_bit_cast(u32x2, __builtin_amdgcn_ds_read_tr16_b64_v4i16((LAS s16x4*)off)); }
__device__ __forceinline__ u32x4 trd2(u32 o0, u32 o1) { const u32x2 a = trd(o0), b = trd(o1); return (u32x4){a.x, a.y, b.x, b.y}; }
__device__ __forceinline__ void dma1(u32 v0, const void* sbase, u32 dst) {
    asm volatile("s_mov_b32 m0, %2\n\ts_nop 0\n\tglobal_load_lds_dwordx4 %0, %1" :: "v"(v0), "s"(sbase), "s"(dst) : "memory"); }
__device__ __forceinline__ void dma3(u32 v0, u32 v1, const void* sbase, u32 dst) {
    asm volatile("s_mov_b32 m0, %3\n\ts_nop 0\n\tglobal_load_lds_dwordx4 %0, %2\n\tglobal_load_lds_dwordx4 %1, %2 offset:1024\n\t"
                 "global_load_lds_dwordx4 %0, %2 offset:2048" :: "v"(v0), "v"(v1), "s"(sbase), "s"(dst) : "memory"); }
__device__ __forceinline__ void dma4(u32 v0, u32 v1, u32 v2, u32 v3, const void* sbase, u32 dst) {
    asm volatile("s_mov_b32 m0, %5\n\ts_nop 0\n\tglobal_load_lds_dwordx4 %0, %4\n\tglobal_load_lds_dwordx4 %1, %4 offset:1024\n\t"
                 "global_load_lds_dwordx4 %2, %4 offset:2048\n\tglobal_load_lds_dwordx4 %3, %4 offset:3072"
                 :: "v"(v0), "v"(v1), "v"(v2), "v"(v3), "s"(sbase), "s"(dst) : "memory"); }
__device__ __forceinline__ float halves_sum(float v) {
    auto r = __builtin_amdgcn_permlane32_swap(__float_as_uint(v), __float_as_uint(v), false, false);
    return __uint_as_float(r[0]) + __uint_as_float(r[1]); }
__device__ __forceinline__ float halves_max(float v) {
    auto r = __builtin_amdgcn_permlane32_swap(__float_as_uint(v), __float_as_uint(v), false, false);
    return fmaxf(__uint_as_float(r[0]), __uint_as_float(r[1])); }
template <int CTRL> __device__ __forceinline__ float dpp_max(float v) {
    return fmaxf(v, __uint_as_float(__builtin_amdgcn_update_dpp(__float_as_uint(v), __float_as_uint(v), CTRL, 0xf, 0xf, false))); }
__device__ __forceinline__ u32x4 widen(u32 a0, u32 a1, u32 b0, u32 b1) {
    auto r0 = __builtin_amdgcn_permlane32_swap(a0, b0, false, false);
    auto r1 = __builtin_amdgcn_permlane32_swap(a1, b1, false, false);
    return (u32x4){r0[0], r1[0], r0[1], r1[1]}; }
__device__ __forceinline__ f32x2 cmul(f32x2 a, f32x2 b) { return (f32x2){a.x * b.x - a.y * b.y, a.x * b.y + a.y * b.x}; }
__device__ __forceinline__ f32x2 cexp_n(float zr2, float th, float n) {
    const float m = __builtin_amdgcn_exp2f(zr2 * n), a = __builtin_amdgcn_fractf(th * n);
    return (f32x2){m * __builtin_amdgcn_cosf(a), m * __builtin_amdgcn_sinf(a)}; }
template <bool WAIT> __device__ __forceinline__ u64 poll_tag(const char* p, bool active, u64 tag) {
    u64 v = tag << 16;
#pragma unroll 1
    for (u32 spins = 0; spins < SPIN_CAP; ++spins) {
        if (active) v = __hip_atomic_load((const GAS u64*)p, RLX_AGENT);
        if (!WAIT || __all((v >> 16) == tag)) break;
        __builtin_amdgcn_s_sleep(1);
    }
    __builtin_amdgcn_fence(__ATOMIC_ACQUIRE, "wavefront");
    return v; }

struct Params {
    const char *x, *a_re, *a_im, *b_re, *b_im, *c_re, *c_im, *log_dt, *dskip, *wqkv, *wo, *gate_w;
    char *out, *ws;
};

template <int ROLE>
__device__ __forceinline__ void phase1(const Params& p, __amdgpu_buffer_rsrc_t wsr, char* ws, int lane, int w, int hp, int qb, int d0, u64& nonce, u64 pc0 STAMP_ARG) {
    const int tb = w & 3, r = lane & 31, h = lane >> 5;
    const int ch = w & 1, n = ((w >> 1) & 1) * 64 + lane;
    const u32 po = ((d0 + ch) * 128 + n) * 2;
    u32 pf = 0;
    if (ROLE == 1 && tb >= 2) asm volatile("global_load_dword %0, %1, %2" : "=&v"(pf) : "v"(min((((hp & 3) * 2 + (tb & 1)) * 64 + lane) * 64, CODE_BYTES)), "s"(pc0));
    u32 are, aim, bre = 0, bim = 0, cre = 0, cim = 0;
    asm volatile("global_load_ushort %0, %2, %3\n\tglobal_load_ushort %1, %2, %4" : "=&v"(are), "=&v"(aim) : "v"(po), "s"(p.a_re), "s"(p.a_im));
    if (ROLE == 0)
        asm volatile("global_load_ushort %0, %4, %5\n\tglobal_load_ushort %1, %4, %6\n\tglobal_load_ushort %2, %4, %7\n\tglobal_load_ushort %3, %4, %8"
                     : "=&v"(bre), "=&v"(bim), "=&v"(cre), "=&v"(cim) : "v"(po), "s"(p.b_re), "s"(p.b_im), "s"(p.c_re), "s"(p.c_im));
    else if (tb == 0) dma1(lane * 16, p.gate_w, L_GW);
    u32 dtw, dsw;
    u64 nw;
    asm volatile("s_load_dword %0, %3, 0x0\n\ts_load_dword %1, %4, 0x0\n\ts_load_dwordx2 %2, %5, 0x0"
                 : "=&s"(dtw), "=&s"(dsw), "=&s"(nw) : "s"(p.log_dt + d0 * 2), "s"(p.dskip + d0 * 2), "s"(ws + WS_NONCE));
    u32 vo[4];
    const char* src; u32 step, dst;
    if (ROLE == 0) {
        vo[0] = (lane >> 2) * 3072 + ((lane & 3) << 4); vo[1] = vo[0] ^ 32;
        src = p.wqkv + (tb * 16 * 3072 + hp * 64); step = 64 * 3072; dst = L_W + tb * 3072;
    } else {
#pragma unroll
        for (int i = 0; i < 4; ++i) vo[i] = (7 * i + (lane >> 3)) * 1024 + (((lane & 7) ^ ((4 * i + (lane >> 4)) & 7)) << 4);
        src = p.x + (qb * 128 + tb * 32) * 1024; step = 128; dst = tb * 4096;
    }
    auto issue = [&](int c) {
        if (ROLE == 0) dma3(vo[0], vo[1], src + c * step, (c % 3) * CHUNK + dst);
        else dma4(vo[0], vo[1], vo[2], vo[3], src + c * step, (c % 3) * CHUNK + dst);
    };
    issue(0); issue(1); issue(2);
    u32 ug[4];
    {
        const char* ub = p.x + d0 * 2;
        asm volatile("global_load_dword %0, %4, %5\n\tglobal_load_dword %1, %4, %6\n\tglobal_load_dword %2, %4, %7\n\tglobal_load_dword %3, %4, %8"
                     : "=&v"(ug[0]), "=&v"(ug[1]), "=&v"(ug[2]), "=&v"(ug[3])
#define UB(o) (ub + (o))
                     : "v"((w * 256 + lane) * 1024),
                       "s"(UB(0)), "s"(UB(65536)), "s"(UB(131072)), "s"(UB(196608)));
    }
    asm volatile("s_waitcnt lgkmcnt(0)" : "+s"(dtw), "+s"(dsw), "+s"(nw));
    nonce = nw;
    if (w == 0 && lane < 8) ST(u32, L_SYNC + lane * 4) = 0;
    const float dt = (float)(__bf16)expf(ch ? bfhi(dtw) : bflo(dtw));
    if (ROLE == 0) asm volatile("s_waitcnt vmcnt(13)" : "+v"(are), "+v"(aim), "+v"(bre), "+v"(bim), "+v"(cre), "+v"(cim), "+v"(pf));
    else asm volatile("s_waitcnt vmcnt(16)" : "+v"(are), "+v"(aim), "+v"(pf));
    STAMP(1);
    {
        const float zr2 = (dt * bflo(are)) * LOG2E, th = (dt * bflo(aim)) * 0.15915494309189535f;
        const u32 t0 = L_TAB + ch * TAB_CH + n * 4;
        if (ROLE == 0) {
            const f32x2 e1 = cexp_n(zr2, th, 1.f), g = cexp_n(zr2, th, 256.f);
            f32x2 a[8];
            a[0] = cmul((f32x2){bflo(cre), bflo(cim)}, (f32x2){dt * bflo(bre), dt * bflo(bim)});
#define PUT_A(row, v) ST(u32, t0 + (row) * TAB_ROW) = pk2((v).x, -(v).y)
            PUT_A(0, a[0]);
#pragma unroll
            for (int i = 1; i < 8; ++i) { a[i] = cmul(a[i - 1], e1); PUT_A(i, a[i]); }
            f32x2 c0 = cmul(a[0], g), c2 = cmul(a[2], g), c4 = cmul(a[4], g), c6 = cmul(a[6], g);
            PUT_A(8, c0); PUT_A(9, c2); PUT_A(10, c4); PUT_A(11, c6);
            c0 = cmul(c0, g); c4 = cmul(c4, g); PUT_A(12, c0); PUT_A(13, c4);
            c0 = cmul(c0, g); c4 = cmul(c4, g); PUT_A(14, c0); PUT_A(15, c4);
#pragma unroll
            for (int i = 16; i < 21; ++i) { c0 = cmul(c0, g); PUT_A(i, c0); }
#undef PUT_A
        } else {
            const f32x2 e8 = cexp_n(zr2, th, 8.f), e16 = cmul(e8, e8);
            f32x2 p0 = {1.f, 0.f}, p1 = e8;
#pragma unroll
            for (int i = 0; i < 32; i += 2) {
                ST(u32, t0 + TAB_A + i * TAB_ROW) = pk2(p0.x, p0.y);
                ST(u32, t0 + TAB_A + (i + 1) * TAB_ROW) = pk2(p1.x, p1.y);
                p0 = cmul(p0, e16); p1 = cmul(p1, e16);
            }
        }
    }
    STAMP(2);
    u32 xa[4];
#pragma unroll
    for (int ks = 0; ks < 4; ++ks) xa[ks] = (tb * 32 + r) * 128 + (((2 * ks + h) ^ ((r >> 1) & 7)) << 4);
    const int g1 = (lane >> 4) & 1;
    const u32 trb = L_W + (8 * h + ((lane >> 2) & 3)) * 64 + (lane & 3) * 8;
    const u32 wa = trb + g1 * 1024 + ((ROLE ^ g1) << 5), wv = trb + 2048 + (g1 << 5);
    f32x16 aqk = {}, av = {};
    float gacc = 0.f;
    u32x4 fx[2][4], fa[2][4], fb[2][4];
    const bool gate_here = ROLE == 1 && (tb >> 1) == (hp >> 3);
    auto mul = [&](int c) {
#pragma unroll
        for (int ks = 0; ks < 4; ++ks) {
            aqk = mfma32(fa[c & 1][ks], fx[c & 1][ks], aqk);
            if (ROLE == 0) av = mfma32(fx[c & 1][ks], fb[c & 1][ks], av);
        }
        if (gate_here) {
#pragma unroll
            for (int ks = 0; ks < 4; ++ks)
#pragma unroll
                for (int j = 0; j < 4; ++j) gacc = dot2(fx[c & 1][ks][j], fb[c & 1][ks][j], gacc);
        }
    };
#pragma unroll
    for (int c = 0; c < 8; ++c) {
        if (c == 0) { if (ROLE) BAR(12); else BAR(10); }
        else if (c < 3) { if (ROLE) BAR(8); else BAR(7); }
        else if (c < 7) { if (ROLE) BAR(4); else BAR(3); }
        else BAR(0);
        if (c >= 1 && c <= 5) issue(c + 2);
        const u32 cb = (c % 3) * CHUNK;
#pragma unroll
        for (int ks = 0; ks < 4; ++ks) {
            fx[c & 1][ks] = LD(u32x4, cb + xa[ks]);
            fa[c & 1][ks] = trd2(cb + wa + ks * 3072, cb + wa + ks * 3072 + 256);
            if (ROLE == 0) fb[c & 1][ks] = trd2(cb + wv + ks * 3072, cb + wv + ks * 3072 + 256);
        }
        if (gate_here) {
#pragma unroll
            for (int ks = 0; ks < 4; ++ks) fb[c & 1][ks] = LD(u32x4, L_GW + (c * 4 + ks) * 32 + h * 16);
        }
        __builtin_amdgcn_sched_barrier(0);
        if (c >= 1) mul(c - 1);
        __builtin_amdgcn_sched_barrier(0);
        if (c == 0) {
            STAMP(3);
            const int aq = (threadIdx.x >> 7) & 1, col = lane & 15, g = lane >> 4;
            const u32 ta = L_TAB + ch * TAB_CH + (aq * 16 + col) * TAB_ROW + g * 16;
            const u32 tbb = L_TAB + ch * TAB_CH + TAB_A + (ROLE * 16 + col) * TAB_ROW + g * 16;
            f32x4 kq = {};
#pragma unroll
            for (int ks = 0; ks < 8; ++ks) kq = mfma16(LD(u32x4, ta + ks * 64), LD(u32x4, tbb + ks * 64), kq);
            if (ROLE == 0 && aq == 0 && lane == 0) kq[0] += ch ? bfhi(dsw) : bflo(dsw);
            const int l0 = aq ? (g == 0 ? 1024 : g == 1 ? 2048 : 2052) : (g < 2 ? 4 * g : 256 * g - 256);
            const int s1 = aq ? (g == 0 ? 256 : 1) : (g < 2 ? 1 : 2 * g - 2), s2 = (!aq && g == 3) ? 256 : 2 * s1;
            const u32 k0 = L_KF + ch * KF_CH + (l0 + 8 * (ROLE * 16 + col)) * 2;
            const u32 e01 = pk2(kq[0], kq[1]), e23 = pk2(kq[2], kq[3]);
            ST(unsigned short, k0) = (unsigned short)e01; ST(unsigned short, k0 + 2 * s1) = (unsigned short)(e01 >> 16);
            ST(unsigned short, k0 + 2 * s2) = (unsigned short)e23; ST(unsigned short, k0 + 2 * (s1 + s2)) = (unsigned short)(e23 >> 16);
        }
        if (c == 1) {
            const int i8 = (w & 3) * 64 + lane, blk = i8 >> 2, pc = i8 & 3;
            u32x4 kv;
#pragma unroll
            for (int j = 0; j < 4; ++j) {
                const float t2 = (float)(2 * (32 * blk + 31 - 8 * pc - 2 * j));
                const u32 rr = pk2(t2, t2 - 2.f);
                const u16x2 e = {LD(unsigned short, L_KF + ROLE * KF_CH + (u32)bflo(rr)), LD(unsigned short, L_KF + ROLE * KF_CH + (u32)bfhi(rr))};
                kv[j] = __builtin_bit_cast(u32, e);
            }
            ST(u32x4, L_KB + ROLE * KB_CH + (blk + 32) * 80 + pc * 16) = kv;
            if (i8 < 128) ST(u32x4, L_KB + ROLE * KB_CH + blk * 80 + pc * 16) = (u32x4){0, 0, 0, 0};
            else if (i8 < 128 + 36) ST(u32x2, L_UC + ROLE * UC_CH + ((i8 - 128) / 9) * UC_COPY + ((i8 - 128) % 9) * 8) = (u32x2){0, 0};
        }
    }
    mul(7);
    STAMP(4);
    const u32 tile = (qb * 4 + tb) * 2048;
    {
        const u32x4 kf = {pk2(aqk[8], aqk[9]), pk2(aqk[10], aqk[11]), pk2(aqk[12], aqk[13]), pk2(aqk[14], aqk[15])};
        const int pr = (r & 19) | ((r & 4) << 1) | ((r & 8) >> 1);
        __builtin_amdgcn_raw_buffer_store_b128(kf, wsr, WS_KV + (hp * 2 + ROLE) * 131072 + tile + (h * 32 + pr) * 16, 0, SC1);
    }
    if (ROLE == 0) {
#pragma unroll
        for (int s = 0; s < 2; ++s) {
            const u32x4 vf = {pk2(av[8 * s], av[8 * s + 1]), pk2(av[8 * s + 2], av[8 * s + 3]), pk2(av[8 * s + 4], av[8 * s + 5]), pk2(av[8 * s + 6], av[8 * s + 7])};
            __builtin_amdgcn_raw_buffer_store_b128(vf, wsr, WS_KV + (hp * 2 + (r >> 4)) * 131072 + tile + 1024 + s * 512 + h * 256 + (r & 15) * 16, 0, SC1);
        }
    }
    float qn = 0.f, kn = 0.f;
#pragma unroll
    for (int i = 0; i < 8; ++i) { aqk[i] *= QSCALE; qn = fmaf(aqk[i], aqk[i], qn); kn = fmaf(aqk[8 + i], aqk[8 + i], kn); }
    ST(u32x4, L_QX + (ROLE * 4 + tb) * 1024 + lane * 16) = (u32x4){pk2(aqk[0], aqk[1]), pk2(aqk[2], aqk[3]), pk2(aqk[4], aqk[5]), pk2(aqk[6], aqk[7])};
    qn = halves_sum(qn); kn = halves_sum(kn);
    kn = dpp_max<0x111>(kn); kn = dpp_max<0x112>(kn); kn = dpp_max<0x114>(kn); kn = dpp_max<0x118>(kn); kn = dpp_max<0x142>(kn);
    if (h == 0) ST(float, L_QN + (ROLE * 128 + tb * 32 + r) * 4) = qn;
    if (lane == 31) ST(float, L_KN + (ROLE * 4 + tb) * 4) = kn;
    if (ROLE == 1) {
        const float s = halves_sum(gacc);
        if (h == 0) ST(float, L_G + (tb * 32 + r) * 4) = 1.f / (1.f + __expf(-s));
    }
    asm volatile("" : "+v"(ug[0]), "+v"(ug[1]), "+v"(ug[2]), "+v"(ug[3]));
#pragma unroll
    for (int i = 0; i < 4; ++i) {
        const u32 ua = L_UC + (w * 256 + i * 64 + lane + 36) * 2;
#pragma unroll
        for (int c = 0; c < 4; ++c) {
            ST(unsigned short, ua + c * (UC_COPY - 2)) = (unsigned short)ug[i];
            ST(unsigned short, ua + c * (UC_COPY - 2) + UC_CH) = (unsigned short)(ug[i] >> 16);
        }
    }
    STAMP(15);
}

__device__ __forceinline__ void conv_wave(__amdgpu_buffer_rsrc_t wsr, char* ws, int lane, int tb, int d0, u64 tag) {
    const int ch = tb & 1, mh = tb >> 1, r = lane & 31, h = lane >> 5, c = (r + 1) & 3;
    u32 ua = L_UC + ch * UC_CH + c * UC_COPY + (mh * 512 + (r + 1 - c) + 8 * h + 4) * 2;
    u32 ka = L_KB + ch * KB_CH + (30 + r - mh * 16) * 80 + h * 16;
    f32x16 y0, y1;
    __builtin_amdgcn_s_setprio(2);
    asm volatile(
        "ds_read_b64 v[32:33], v96 offset:0\nds_read_b64 v[34:35], v96 offset:8\nds_read_b128 v[36:39], v97 offset:160\n"
        "ds_read_b128 v[40:43], v97 offset:2720\nds_read_b64 v[44:45], v96 offset:2048\nds_read_b64 v[46:47], v96 offset:2056\n"
        "ds_read_b64 v[48:49], v96 offset:32\nds_read_b64 v[50:51], v96 offset:40\nds_read_b128 v[52:55], v97 offset:192\n"
        "ds_read_b128 v[56:59], v97 offset:2752\nds_read_b64 v[60:61], v96 offset:2080\nds_read_b64 v[62:63], v96 offset:2088\nv_mov_b32 v0, 0\n"
        "v_mov_b32 v1, 0\nv_mov_b32 v2, 0\nv_mov_b32 v3, 0\nv_mov_b32 v4, 0\nv_mov_b32 v5, 0\nv_mov_b32 v6, 0\nv_mov_b32 v7, 0\nv_mov_b32 v8, 0\n"
        "v_mov_b32 v9, 0\nv_mov_b32 v10, 0\nv_mov_b32 v11, 0\nv_mov_b32 v12, 0\nv_mov_b32 v13, 0\nv_mov_b32 v14, 0\nv_mov_b32 v15, 0\nv_mov_b32 v16, 0\n"
        "v_mov_b32 v17, 0\nv_mov_b32 v18, 0\nv_mov_b32 v19, 0\nv_mov_b32 v20, 0\nv_mov_b32 v21, 0\nv_mov_b32 v22, 0\nv_mov_b32 v23, 0\nv_mov_b32 v24, 0\n"
        "v_mov_b32 v25, 0\nv_mov_b32 v26, 0\nv_mov_b32 v27, 0\nv_mov_b32 v28, 0\nv_mov_b32 v29, 0\nv_mov_b32 v30, 0\nv_mov_b32 v31, 0\ns_mov_b32 s44, 8\n"
        ".p2align 3\n1:\ns_waitcnt lgkmcnt(9)\nv_mfma_f32_32x32x16_bf16 v[0:15], v[64:67], v[68:71], v[0:15]\nds_read_b64 v[64:65], v96 offset:64\n"
        "ds_read_b64 v[66:67], v96 offset:72\ns_waitcnt lgkmcnt(10)\nv_mfma_f32_32x32x16_bf16 v[16:31], v[64:67], v[72:75], v[16:31]\n"
        "ds_read_b128 v[68:71], v97 offset:80\nds_read_b128 v[72:75], v97 offset:2640\ns_waitcnt lgkmcnt(10)\n"
        "v_mfma_f32_32x32x16_bf16 v[16:31], v[76:79], v[68:71], v[16:31]\nds_read_b64 v[76:77], v96 offset:2112\nds_read_b64 v[78:79], v96 offset:2120\n"
        "s_waitcnt lgkmcnt(9)\nv_mfma_f32_32x32x16_bf16 v[0:15], v[80:83], v[84:87], v[0:15]\nds_read_b64 v[80:81], v96 offset:96\n"
        "ds_read_b64 v[82:83], v96 offset:104\ns_waitcnt lgkmcnt(10)\nv_mfma_f32_32x32x16_bf16 v[16:31], v[80:83], v[88:91], v[16:31]\n"
        "ds_read_b128 v[84:87], v97 offset:112\nds_read_b128 v[88:91], v97 offset:2672\ns_waitcnt lgkmcnt(10)\n"
        "v_mfma_f32_32x32x16_bf16 v[16:31], v[92:95], v[84:87], v[16:31]\nds_read_b64 v[92:93], v96 offset:2144\nds_read_b64 v[94:95], v96 offset:2152\n"
        "s_waitcnt lgkmcnt(9)\nv_mfma_f32_32x32x16_bf16 v[0:15], v[32:35], v[36:39], v[0:15]\nds_read_b64 v[32:33], v96 offset:128\n"
        "ds_read_b64 v[34:35], v96 offset:136\ns_waitcnt lgkmcnt(10)\nv_mfma_f32_32x32x16_bf16 v[16:31], v[32:35], v[40:43], v[16:31]\n"
        "ds_read_b128 v[36:39], v97 offset:0\nds_read_b128 v[40:43], v97 offset:2560\ns_waitcnt lgkmcnt(10)\n"
        "v_mfma_f32_32x32x16_bf16 v[16:31], v[44:47], v[36:39], v[16:31]\nds_read_b64 v[44:45], v96 offset:2176\nds_read_b64 v[46:47], v96 offset:2184\n"
        "s_waitcnt lgkmcnt(9)\nv_mfma_f32_32x32x16_bf16 v[0:15], v[48:51], v[52:55], v[0:15]\nds_read_b64 v[48:49], v96 offset:160\n"
        "ds_read_b64 v[50:51], v96 offset:168\ns_waitcnt lgkmcnt(10)\nv_mfma_f32_32x32x16_bf16 v[16:31], v[48:51], v[56:59], v[16:31]\n"
        "ds_read_b128 v[52:55], v97 offset:32\nds_read_b128 v[56:59], v97 offset:2592\ns_waitcnt lgkmcnt(10)\n"
        "v_mfma_f32_32x32x16_bf16 v[16:31], v[60:63], v[52:55], v[16:31]\nds_read_b64 v[60:61], v96 offset:2208\nds_read_b64 v[62:63], v96 offset:2216\n"
        "v_add_u32 v96, 0x80, v96\nv_subrev_u32 v97, 0xa0, v97\ns_sub_u32 s44, s44, 1\ns_cmp_lg_u32 s44, 0\ns_cbranch_scc1 1b\ns_waitcnt lgkmcnt(0)\ns_nop 7\n"
        : "=&{v[0:15]}"(y0), "=&{v[16:31]}"(y1), "+{v96}"(ua), "+{v97}"(ka) :
        : "memory", "scc", "s44", "v32", "v33", "v34", "v35", "v36", "v37", "v38", "v39", "v40", "v41", "v42", "v43", "v44", "v45", "v46", "v47", "v48",
          "v49", "v50", "v51", "v52", "v53", "v54", "v55", "v56", "v57", "v58", "v59", "v60", "v61", "v62", "v63", "v64", "v65", "v66", "v67", "v68", "v69",
          "v70", "v71", "v72", "v73", "v74", "v75", "v76", "v77", "v78", "v79", "v80", "v81", "v82", "v83", "v84", "v85", "v86", "v87", "v88", "v89", "v90",
          "v91", "v92", "v93", "v94", "v95");
    const f32x16 give = mh ? y0 : y1;
    f32x16 y = mh ? y1 : y0;
#pragma unroll
    for (int i = 0; i < 4; ++i) ST(f32x4, L_CX + tb * 4096 + i * 1024 + lane * 16) = (f32x4){give[4 * i], give[4 * i + 1], give[4 * i + 2], give[4 * i + 3]};
    asm volatile("s_waitcnt lgkmcnt(0)" ::: "memory");
    if (lane == 0) __hip_atomic_store((LAS u32*)(L_SYNC + tb * 4), 1u, __ATOMIC_RELAXED, __HIP_MEMORY_SCOPE_WORKGROUP);
    while (__hip_atomic_load((LAS u32*)(L_SYNC + (tb ^ 2) * 4), __ATOMIC_RELAXED, __HIP_MEMORY_SCOPE_WORKGROUP) == 0) __builtin_amdgcn_s_sleep(1);
    asm volatile("" ::: "memory");
#pragma unroll
    for (int i = 0; i < 4; ++i) {
        const f32x4 o = LD(f32x4, L_CX + (tb ^ 2) * 4096 + i * 1024 + lane * 16);
#pragma unroll
        for (int j = 0; j < 4; ++j) y[4 * i + j] += o[j];
    }
    asm volatile("s_waitcnt lgkmcnt(0)" : "+v"(y) :: "memory");
    if (lane == 0) __hip_atomic_fetch_add((LAS u32*)(L_SYNC + 16), 1u, __ATOMIC_RELAXED, __HIP_MEMORY_SCOPE_WORKGROUP);
    const u32 yo = WS_YT + ((d0 + ch) * 2048 + mh * 1024 + r * 32 + h * 8) * 2;
#pragma unroll
    for (int i = 0; i < 2; ++i)
        __builtin_amdgcn_raw_buffer_store_b128(widen(pk2(y[8 * i], y[8 * i + 1]), pk2(y[8 * i + 2], y[8 * i + 3]), pk2(y[8 * i + 4], y[8 * i + 5]), pk2(y[8 * i + 6], y[8 * i + 7])),
                                               wsr, yo + i * 32, 0, SC1);
    asm volatile("s_waitcnt vmcnt(0)" ::: "memory");
    __builtin_amdgcn_s_setprio(0);
    if (lane == 0) __hip_atomic_store((GAS u64*)(ws + WS_YFLAG + ((d0 + ch) * 2 + mh) * 64), tag << 16, RLX_AGENT);
}

template <int PH>
__global__ __launch_bounds__(512, 2) void s4_mha_mix(Params p_arg) {
    const Params& p = p_arg;
    __shared__ __attribute__((aligned(16))) char smem[LDS_BYTES];
    constexpr bool FUSED = PH == 7;
    asm volatile("" :: "s"((u32)(uintptr_t)smem));
    const int lane = threadIdx.x & 63, w = __builtin_amdgcn_readfirstlane(threadIdx.x >> 6);
    const int b = blockIdx.x, xg = b & 7, jb = b >> 3;
    const int hp = 8 * (xg & 1) + (jb & 7), qb = 4 * (xg >> 1) + (jb >> 3), d0 = 64 * xg + 2 * jb;
    const int tb = w & 3, a = w >> 2, r = lane & 31, h = lane >> 5;
    char* ws = p.ws;
    const __amdgpu_buffer_rsrc_t wsr = __builtin_amdgcn_make_buffer_rsrc(ws, 0, WS_END, 0x00020000);
    u64 nonce = 0, pc0;
    asm volatile("s_getpc_b64 %0" : "=s"(pc0));
    const int cb64 = hp & 7, rb64 = 2 * qb + (hp >> 3);

    if (PH & 1) {
        if (a == 0) phase1<0>(p, wsr, ws, lane, w, hp, qb, d0, nonce, pc0 STAMP_PASS); else phase1<1>(p, wsr, ws, lane, w, hp, qb, d0, nonce, pc0 STAMP_PASS);
        BAR(0);
        STAMP(5);
    }
    const u64 tag = (nonce + 1) & 0xffffffffffffull;
    if (PH & 1) {
        if (threadIdx.x < 2) {
            const f32x4 k4 = LD(f32x4, L_KN + lane * 16);
            const u32 kb = (__float_as_uint(fmaxf(fmaxf(k4.x, k4.y), fmaxf(k4.z, k4.w))) >> 16) + 1;
            __hip_atomic_store((GAS u64*)(ws + WS_KVFLAG + (hp * 16 + qb) * 128 + lane * 8), (tag << 16) | min(kb, 0x7fc0u), RLX_AGENT);
        }
        if (!FUSED) {
            for (int i = threadIdx.x; i < (8192 + 2560) / 16; i += 512)
                *(GAS u32x4*)(ws + WS_QF + b * 12288 + i * 16) = LD(u32x4, L_QX + i * 16);
        }
    }
    if (!FUSED && !(PH & 1)) {
        for (int i = threadIdx.x; i < (8192 + 2560) / 16; i += 512)
            ST(u32x4, L_QX + i * 16) = *(const GAS u32x4*)(ws + WS_QF + b * 12288 + i * 16);
        if (threadIdx.x == 0) ST(u32, L_SYNC + 16) = 4;
        __syncthreads();
    }
    if (FUSED || PH == 4) {
        const u32 vo = (lane >> 3) * 1024 + (((lane & 7) ^ ((lane >> 2) & 4)) << 4);
#pragma unroll
        for (int i = 0; i < 2; ++i) dma4(vo, vo + 7168, vo + 14336, vo + 21504, p.wo + ((w * 64 + i * 32) * 1024 + cb64 * 128), L_WO + (w * 64 + i * 32) * 128);
    }
    if ((PH & 1) && a == 1) { conv_wave(wsr, ws, lane, tb, d0, tag); STAMP(6); }

    if (PH & 2) {
        u32x4 q0 = LD(u32x4, L_QX + (a * 4 + tb) * 1024 + lane * 16), q1 = LD(u32x4, L_QX + (a * 4 + ((tb + 1) & 3)) * 1024 + lane * 16);
        u32x4 q2 = LD(u32x4, L_QX + (a * 4 + ((tb + 2) & 3)) * 1024 + lane * 16), q3 = LD(u32x4, L_QX + (a * 4 + ((tb + 3) & 3)) * 1024 + lane * 16);
        const f32x2 qn2 = LD(f32x2, L_QN + a * 512 + lane * 8);
        f32x16 o0 = {}, o1 = {}, o2 = {}, o3 = {};
        float m0 = 0.f, m1 = 0.f, m2 = 0.f, m3 = 0.f;
        const u32 kvo = WS_KV + (hp * 2 + a) * 131072 + tb * 32768;
        const u64 fl = poll_tag<FUSED>(ws + WS_KVFLAG + (hp * 16 + tb * 4 + (lane & 3)) * 128 + a * 8, lane < 4, tag);
        STAMP(7);
        u32 kb = lane < 4 ? (u32)fl & 0xffffu : 0u;
        kb = max(max(__builtin_amdgcn_readlane(kb, 0), __builtin_amdgcn_readlane(kb, 1)), max(__builtin_amdgcn_readlane(kb, 2), __builtin_amdgcn_readlane(kb, 3)));
        const bool safe = __all(fmaxf(qn2.x, qn2.y) * __uint_as_float(kb << 16) <= SAFE2);
        if (__builtin_expect(safe, 1)) {
            const u32 one = (r & 27) == 16 ? 0x3f803f80u : 0u;
            u64 base = (u64)(uintptr_t)(ws + kvo);
            asm volatile(
                "v_mov_b32 v116, v163\nv_mov_b32 v117, v163\nv_mov_b32 v118, v163\nv_mov_b32 v119, v163\nv_mov_b32 v120, v163\nv_mov_b32 v121, v163\n"
                "v_mov_b32 v122, v163\nv_mov_b32 v123, v163\nv_mov_b32 v128, v163\nv_mov_b32 v129, v163\nv_mov_b32 v130, v163\nv_mov_b32 v131, v163\n"
                "v_mov_b32 v132, v163\nv_mov_b32 v133, v163\nv_mov_b32 v134, v163\nv_mov_b32 v135, v163\nv_mov_b32 v140, v163\nv_mov_b32 v141, v163\n"
                "v_mov_b32 v142, v163\nv_mov_b32 v143, v163\nv_mov_b32 v144, v163\nv_mov_b32 v145, v163\nv_mov_b32 v146, v163\nv_mov_b32 v147, v163\n"
                "v_mov_b32 v152, v163\nv_mov_b32 v153, v163\nv_mov_b32 v154, v163\nv_mov_b32 v155, v163\nv_mov_b32 v156, v163\nv_mov_b32 v157, v163\n"
                "v_mov_b32 v158, v163\nv_mov_b32 v159, v163\ns_add_u32 s40, s40, 4096\ns_addc_u32 s41, s41, 0\n"
                "global_load_dwordx4 v[112:115], v161, s[40:41] offset:-4096 sc1\ns_mov_b64 exec, s[42:43]\n"
                "global_load_dwordx4 v[116:119], v162, s[40:41] offset:-3072 sc1\nglobal_load_dwordx4 v[120:123], v162, s[40:41] offset:-2560 sc1\ns_mov_b64 exec, -1\n"
                "global_load_dwordx4 v[124:127], v161, s[40:41] offset:-2048 sc1\ns_mov_b64 exec, s[42:43]\n"
                "global_load_dwordx4 v[128:131], v162, s[40:41] offset:-1024 sc1\nglobal_load_dwordx4 v[132:135], v162, s[40:41] offset:-512 sc1\ns_mov_b64 exec, -1\n"
                "global_load_dwordx4 v[136:139], v161, s[40:41] offset:0 sc1\ns_mov_b64 exec, s[42:43]\n"
                "global_load_dwordx4 v[140:143], v162, s[40:41] offset:1024 sc1\nglobal_load_dwordx4 v[144:147], v162, s[40:41] offset:1536 sc1\ns_mov_b64 exec, -1\n"
                "s_add_u32 s40, s40, 6144\ns_addc_u32 s41, s41, 0\ns_mov_b32 s44, 3\nv_mov_b32 v104, 0\nv_mov_b32 v105, 0\nv_mov_b32 v106, 0\nv_mov_b32 v107, 0\n"
                "v_mov_b32 v108, 0\nv_mov_b32 v109, 0\nv_mov_b32 v110, 0\nv_mov_b32 v111, 0\nv_mov_b32 v0, 0\nv_mov_b32 v1, 0\nv_mov_b32 v2, 0\nv_mov_b32 v3, 0\n"
                "v_mov_b32 v4, 0\nv_mov_b32 v5, 0\nv_mov_b32 v6, 0\nv_mov_b32 v7, 0\nv_mov_b32 v8, 0\nv_mov_b32 v9, 0\nv_mov_b32 v10, 0\nv_mov_b32 v11, 0\n"
                "v_mov_b32 v12, 0\nv_mov_b32 v13, 0\nv_mov_b32 v14, 0\nv_mov_b32 v15, 0\nv_mov_b32 v16, 0\nv_mov_b32 v17, 0\nv_mov_b32 v18, 0\nv_mov_b32 v19, 0\n"
                "v_mov_b32 v20, 0\nv_mov_b32 v21, 0\nv_mov_b32 v22, 0\nv_mov_b32 v23, 0\nv_mov_b32 v24, 0\nv_mov_b32 v25, 0\nv_mov_b32 v26, 0\nv_mov_b32 v27, 0\n"
                "v_mov_b32 v28, 0\nv_mov_b32 v29, 0\nv_mov_b32 v30, 0\nv_mov_b32 v31, 0\nv_mov_b32 v32, 0\nv_mov_b32 v33, 0\nv_mov_b32 v34, 0\nv_mov_b32 v35, 0\n"
                "v_mov_b32 v36, 0\nv_mov_b32 v37, 0\nv_mov_b32 v38, 0\nv_mov_b32 v39, 0\nv_mov_b32 v40, 0\nv_mov_b32 v41, 0\nv_mov_b32 v42, 0\nv_mov_b32 v43, 0\n"
                "v_mov_b32 v44, 0\nv_mov_b32 v45, 0\nv_mov_b32 v46, 0\nv_mov_b32 v47, 0\nv_mov_b32 v48, 0\nv_mov_b32 v49, 0\nv_mov_b32 v50, 0\nv_mov_b32 v51, 0\n"
                "v_mov_b32 v52, 0\nv_mov_b32 v53, 0\nv_mov_b32 v54, 0\nv_mov_b32 v55, 0\nv_mov_b32 v56, 0\nv_mov_b32 v57, 0\nv_mov_b32 v58, 0\nv_mov_b32 v59, 0\n"
                "v_mov_b32 v60, 0\nv_mov_b32 v61, 0\nv_mov_b32 v62, 0\nv_mov_b32 v63, 0\ns_waitcnt vmcnt(6)\n"
                "v_mfma_f32_32x32x16_bf16 v[80:95], v[112:115], v[64:67], 0\ns_nop 11\n.p2align 3\n1:\nv_exp_f32 v80, v80\nv_exp_f32 v81, v81\n"
                "v_smfmac_f32_32x32x32_bf16 v[48:63], v[156:159], v[104:111], v160\nglobal_load_dwordx4 v[148:151], v161, s[40:41] offset:-4096 sc1\n"
                "s_mov_b64 exec, s[42:43]\nglobal_load_dwordx4 v[152:155], v162, s[40:41] offset:-3072 sc1\n"
                "global_load_dwordx4 v[156:159], v162, s[40:41] offset:-2560 sc1\ns_mov_b64 exec, -1\nv_exp_f32 v82, v82\nv_exp_f32 v83, v83\nv_exp_f32 v84, v84\n"
                "v_mfma_f32_32x32x16_bf16 v[96:111], v[112:115], v[68:71], 0\nv_exp_f32 v85, v85\nv_exp_f32 v86, v86\nv_exp_f32 v87, v87\nv_exp_f32 v88, v88\n"
                "v_exp_f32 v89, v89\nv_smfmac_f32_32x32x32_bf16 v[0:15], v[116:119], v[80:87], v160\nv_exp_f32 v90, v90\nv_exp_f32 v91, v91\nv_exp_f32 v92, v92\n"
                "v_exp_f32 v93, v93\nv_exp_f32 v94, v94\nv_exp_f32 v95, v95\nv_exp_f32 v96, v96\nv_exp_f32 v97, v97\n"
                "v_smfmac_f32_32x32x32_bf16 v[0:15], v[120:123], v[88:95], v160\nv_exp_f32 v98, v98\nv_exp_f32 v99, v99\nv_exp_f32 v100, v100\n"
                "v_mfma_f32_32x32x16_bf16 v[80:95], v[112:115], v[72:75], 0\nv_exp_f32 v101, v101\nv_exp_f32 v102, v102\nv_exp_f32 v103, v103\nv_exp_f32 v104, v104\n"
                "v_exp_f32 v105, v105\nv_smfmac_f32_32x32x32_bf16 v[16:31], v[116:119], v[96:103], v160\nv_exp_f32 v106, v106\nv_exp_f32 v107, v107\n"
                "v_exp_f32 v108, v108\nv_exp_f32 v109, v109\nv_exp_f32 v110, v110\nv_exp_f32 v111, v111\nv_exp_f32 v80, v80\nv_exp_f32 v81, v81\n"
                "v_smfmac_f32_32x32x32_bf16 v[16:31], v[120:123], v[104:111], v160\nv_exp_f32 v82, v82\nv_exp_f32 v83, v83\nv_exp_f32 v84, v84\n"
                "v_mfma_f32_32x32x16_bf16 v[96:111], v[112:115], v[76:79], 0\nv_exp_f32 v85, v85\nv_exp_f32 v86, v86\nv_exp_f32 v87, v87\nv_exp_f32 v88, v88\n"
                "v_exp_f32 v89, v89\nv_smfmac_f32_32x32x32_bf16 v[32:47], v[116:119], v[80:87], v160\nv_exp_f32 v90, v90\nv_exp_f32 v91, v91\nv_exp_f32 v92, v92\n"
                "v_exp_f32 v93, v93\nv_exp_f32 v94, v94\nv_exp_f32 v95, v95\nv_exp_f32 v96, v96\nv_exp_f32 v97, v97\n"
                "v_smfmac_f32_32x32x32_bf16 v[32:47], v[120:123], v[88:95], v160\nv_exp_f32 v98, v98\nv_exp_f32 v99, v99\nv_exp_f32 v100, v100\ns_waitcnt vmcnt(6)\n"
                "v_mfma_f32_32x32x16_bf16 v[80:95], v[124:127], v[64:67], 0\nv_exp_f32 v101, v101\nv_exp_f32 v102, v102\nv_exp_f32 v103, v103\nv_exp_f32 v104, v104\n"
                "v_exp_f32 v105, v105\nv_smfmac_f32_32x32x32_bf16 v[48:63], v[116:119], v[96:103], v160\nv_exp_f32 v106, v106\nv_exp_f32 v107, v107\n"
                "v_exp_f32 v108, v108\nv_exp_f32 v109, v109\nv_exp_f32 v110, v110\nv_exp_f32 v111, v111\nv_exp_f32 v80, v80\nv_exp_f32 v81, v81\n"
                "v_smfmac_f32_32x32x32_bf16 v[48:63], v[120:123], v[104:111], v160\nglobal_load_dwordx4 v[112:115], v161, s[40:41] offset:-2048 sc1\n"
                "s_mov_b64 exec, s[42:43]\nglobal_load_dwordx4 v[116:119], v162, s[40:41] offset:-1024 sc1\n"
                "global_load_dwordx4 v[120:123], v162, s[40:41] offset:-512 sc1\ns_mov_b64 exec, -1\nv_exp_f32 v82, v82\nv_exp_f32 v83, v83\nv_exp_f32 v84, v84\n"
                "v_mfma_f32_32x32x16_bf16 v[96:111], v[124:127], v[68:71], 0\nv_exp_f32 v85, v85\nv_exp_f32 v86, v86\nv_exp_f32 v87, v87\nv_exp_f32 v88, v88\n"
                "v_exp_f32 v89, v89\nv_smfmac_f32_32x32x32_bf16 v[0:15], v[128:131], v[80:87], v160\nv_exp_f32 v90, v90\nv_exp_f32 v91, v91\nv_exp_f32 v92, v92\n"
                "v_exp_f32 v93, v93\nv_exp_f32 v94, v94\nv_exp_f32 v95, v95\nv_exp_f32 v96, v96\nv_exp_f32 v97, v97\n"
                "v_smfmac_f32_32x32x32_bf16 v[0:15], v[132:135], v[88:95], v160\nv_exp_f32 v98, v98\nv_exp_f32 v99, v99\nv_exp_f32 v100, v100\n"
                "v_mfma_f32_32x32x16_bf16 v[80:95], v[124:127], v[72:75], 0\nv_exp_f32 v101, v101\nv_exp_f32 v102, v102\nv_exp_f32 v103, v103\nv_exp_f32 v104, v104\n"
                "v_exp_f32 v105, v105\nv_smfmac_f32_32x32x32_bf16 v[16:31], v[128:131], v[96:103], v160\nv_exp_f32 v106, v106\nv_exp_f32 v107, v107\n"
                "v_exp_f32 v108, v108\nv_exp_f32 v109, v109\nv_exp_f32 v110, v110\nv_exp_f32 v111, v111\nv_exp_f32 v80, v80\nv_exp_f32 v81, v81\n"
                "v_smfmac_f32_32x32x32_bf16 v[16:31], v[132:135], v[104:111], v160\nv_exp_f32 v82, v82\nv_exp_f32 v83, v83\nv_exp_f32 v84, v84\n"
                "v_mfma_f32_32x32x16_bf16 v[96:111], v[124:127], v[76:79], 0\nv_exp_f32 v85, v85\nv_exp_f32 v86, v86\nv_exp_f32 v87, v87\nv_exp_f32 v88, v88\n"
                "v_exp_f32 v89, v89\nv_smfmac_f32_32x32x32_bf16 v[32:47], v[128:131], v[80:87], v160\nv_exp_f32 v90, v90\nv_exp_f32 v91, v91\nv_exp_f32 v92, v92\n"
                "v_exp_f32 v93, v93\nv_exp_f32 v94, v94\nv_exp_f32 v95, v95\nv_exp_f32 v96, v96\nv_exp_f32 v97, v97\n"
                "v_smfmac_f32_32x32x32_bf16 v[32:47], v[132:135], v[88:95], v160\nv_exp_f32 v98, v98\nv_exp_f32 v99, v99\nv_exp_f32 v100, v100\ns_waitcnt vmcnt(6)\n"
                "v_mfma_f32_32x32x16_bf16 v[80:95], v[136:139], v[64:67], 0\nv_exp_f32 v101, v101\nv_exp_f32 v102, v102\nv_exp_f32 v103, v103\nv_exp_f32 v104, v104\n"
                "v_exp_f32 v105, v105\nv_smfmac_f32_32x32x32_bf16 v[48:63], v[128:131], v[96:103], v160\nv_exp_f32 v106, v106\nv_exp_f32 v107, v107\n"
                "v_exp_f32 v108, v108\nv_exp_f32 v109, v109\nv_exp_f32 v110, v110\nv_exp_f32 v111, v111\nv_exp_f32 v80, v80\nv_exp_f32 v81, v81\n"
                "v_smfmac_f32_32x32x32_bf16 v[48:63], v[132:135], v[104:111], v160\nglobal_load_dwordx4 v[124:127], v161, s[40:41] offset:0 sc1\n"
                "s_mov_b64 exec, s[42:43]\nglobal_load_dwordx4 v[128:131], v162, s[40:41] offset:1024 sc1\n"
                "global_load_dwordx4 v[132:135], v162, s[40:41] offset:1536 sc1\ns_mov_b64 exec, -1\nv_exp_f32 v82, v82\nv_exp_f32 v83, v83\nv_exp_f32 v84, v84\n"
                "v_mfma_f32_32x32x16_bf16 v[96:111], v[136:139], v[68:71], 0\nv_exp_f32 v85, v85\nv_exp_f32 v86, v86\nv_exp_f32 v87, v87\nv_exp_f32 v88, v88\n"
                "v_exp_f32 v89, v89\nv_smfmac_f32_32x32x32_bf16 v[0:15], v[140:143], v[80:87], v160\nv_exp_f32 v90, v90\nv_exp_f32 v91, v91\nv_exp_f32 v92, v92\n"
                "v_exp_f32 v93, v93\nv_exp_f32 v94, v94\nv_exp_f32 v95, v95\nv_exp_f32 v96, v96\nv_exp_f32 v97, v97\n"
                "v_smfmac_f32_32x32x32_bf16 v[0:15], v[144:147], v[88:95], v160\nv_exp_f32 v98, v98\nv_exp_f32 v99, v99\nv_exp_f32 v100, v100\n"
                "v_mfma_f32_32x32x16_bf16 v[80:95], v[136:139], v[72:75], 0\nv_exp_f32 v101, v101\nv_exp_f32 v102, v102\nv_exp_f32 v103, v103\nv_exp_f32 v104, v104\n"
                "v_exp_f32 v105, v105\nv_smfmac_f32_32x32x32_bf16 v[16:31], v[140:143], v[96:103], v160\nv_exp_f32 v106, v106\nv_exp_f32 v107, v107\n"
                "v_exp_f32 v108, v108\nv_exp_f32 v109, v109\nv_exp_f32 v110, v110\nv_exp_f32 v111, v111\nv_exp_f32 v80, v80\nv_exp_f32 v81, v81\n"
                "v_smfmac_f32_32x32x32_bf16 v[16:31], v[144:147], v[104:111], v160\nv_exp_f32 v82, v82\nv_exp_f32 v83, v83\nv_exp_f32 v84, v84\n"
                "v_mfma_f32_32x32x16_bf16 v[96:111], v[136:139], v[76:79], 0\nv_exp_f32 v85, v85\nv_exp_f32 v86, v86\nv_exp_f32 v87, v87\nv_exp_f32 v88, v88\n"
                "v_exp_f32 v89, v89\nv_smfmac_f32_32x32x32_bf16 v[32:47], v[140:143], v[80:87], v160\nv_exp_f32 v90, v90\nv_exp_f32 v91, v91\nv_exp_f32 v92, v92\n"
                "v_exp_f32 v93, v93\nv_exp_f32 v94, v94\nv_exp_f32 v95, v95\nv_exp_f32 v96, v96\nv_exp_f32 v97, v97\n"
                "v_smfmac_f32_32x32x32_bf16 v[32:47], v[144:147], v[88:95], v160\nv_exp_f32 v98, v98\nv_exp_f32 v99, v99\nv_exp_f32 v100, v100\ns_waitcnt vmcnt(6)\n"
                "v_mfma_f32_32x32x16_bf16 v[80:95], v[148:151], v[64:67], 0\nv_exp_f32 v101, v101\nv_exp_f32 v102, v102\nv_exp_f32 v103, v103\nv_exp_f32 v104, v104\n"
                "v_exp_f32 v105, v105\nv_smfmac_f32_32x32x32_bf16 v[48:63], v[140:143], v[96:103], v160\nv_exp_f32 v106, v106\nv_exp_f32 v107, v107\n"
                "v_exp_f32 v108, v108\nv_exp_f32 v109, v109\nv_exp_f32 v110, v110\nv_exp_f32 v111, v111\nv_exp_f32 v80, v80\nv_exp_f32 v81, v81\n"
                "v_smfmac_f32_32x32x32_bf16 v[48:63], v[144:147], v[104:111], v160\nglobal_load_dwordx4 v[136:139], v161, s[40:41] offset:2048 sc1\n"
                "s_mov_b64 exec, s[42:43]\nglobal_load_dwordx4 v[140:143], v162, s[40:41] offset:3072 sc1\n"
                "global_load_dwordx4 v[144:147], v162, s[40:41] offset:3584 sc1\ns_mov_b64 exec, -1\nv_exp_f32 v82, v82\nv_exp_f32 v83, v83\nv_exp_f32 v84, v84\n"
                "v_mfma_f32_32x32x16_bf16 v[96:111], v[148:151], v[68:71], 0\nv_exp_f32 v85, v85\nv_exp_f32 v86, v86\nv_exp_f32 v87, v87\nv_exp_f32 v88, v88\n"
                "v_exp_f32 v89, v89\nv_smfmac_f32_32x32x32_bf16 v[0:15], v[152:155], v[80:87], v160\nv_exp_f32 v90, v90\nv_exp_f32 v91, v91\nv_exp_f32 v92, v92\n"
                "v_exp_f32 v93, v93\nv_exp_f32 v94, v94\nv_exp_f32 v95, v95\nv_exp_f32 v96, v96\nv_exp_f32 v97, v97\n"
                "v_smfmac_f32_32x32x32_bf16 v[0:15], v[156:159], v[88:95], v160\nv_exp_f32 v98, v98\nv_exp_f32 v99, v99\nv_exp_f32 v100, v100\n"
                "v_mfma_f32_32x32x16_bf16 v[80:95], v[148:151], v[72:75], 0\nv_exp_f32 v101, v101\nv_exp_f32 v102, v102\nv_exp_f32 v103, v103\nv_exp_f32 v104, v104\n"
                "v_exp_f32 v105, v105\nv_smfmac_f32_32x32x32_bf16 v[16:31], v[152:155], v[96:103], v160\nv_exp_f32 v106, v106\nv_exp_f32 v107, v107\n"
                "v_exp_f32 v108, v108\nv_exp_f32 v109, v109\nv_exp_f32 v110, v110\nv_exp_f32 v111, v111\nv_exp_f32 v80, v80\nv_exp_f32 v81, v81\n"
                "v_smfmac_f32_32x32x32_bf16 v[16:31], v[156:159], v[104:111], v160\nv_exp_f32 v82, v82\nv_exp_f32 v83, v83\nv_exp_f32 v84, v84\n"
                "v_mfma_f32_32x32x16_bf16 v[96:111], v[148:151], v[76:79], 0\nv_exp_f32 v85, v85\nv_exp_f32 v86, v86\nv_exp_f32 v87, v87\nv_exp_f32 v88, v88\n"
                "v_exp_f32 v89, v89\nv_smfmac_f32_32x32x32_bf16 v[32:47], v[152:155], v[80:87], v160\nv_exp_f32 v90, v90\nv_exp_f32 v91, v91\nv_exp_f32 v92, v92\n"
                "v_exp_f32 v93, v93\nv_exp_f32 v94, v94\nv_exp_f32 v95, v95\nv_exp_f32 v96, v96\nv_exp_f32 v97, v97\n"
                "v_smfmac_f32_32x32x32_bf16 v[32:47], v[156:159], v[88:95], v160\nv_exp_f32 v98, v98\nv_exp_f32 v99, v99\nv_exp_f32 v100, v100\ns_waitcnt vmcnt(6)\n"
                "v_mfma_f32_32x32x16_bf16 v[80:95], v[112:115], v[64:67], 0\nv_exp_f32 v101, v101\nv_exp_f32 v102, v102\nv_exp_f32 v103, v103\nv_exp_f32 v104, v104\n"
                "v_exp_f32 v105, v105\nv_smfmac_f32_32x32x32_bf16 v[48:63], v[152:155], v[96:103], v160\nv_exp_f32 v106, v106\nv_exp_f32 v107, v107\n"
                "v_exp_f32 v108, v108\nv_exp_f32 v109, v109\nv_exp_f32 v110, v110\nv_exp_f32 v111, v111\ns_add_u32 s40, s40, 8192\ns_addc_u32 s41, s41, 0\n"
                "s_sub_u32 s44, s44, 1\ns_cmp_lg_u32 s44, 0\ns_cbranch_scc1 1b\nv_exp_f32 v80, v80\nv_exp_f32 v81, v81\n"
                "v_smfmac_f32_32x32x32_bf16 v[48:63], v[156:159], v[104:111], v160\nglobal_load_dwordx4 v[148:151], v161, s[40:41] offset:-4096 sc1\n"
                "s_mov_b64 exec, s[42:43]\nglobal_load_dwordx4 v[152:155], v162, s[40:41] offset:-3072 sc1\n"
                "global_load_dwordx4 v[156:159], v162, s[40:41] offset:-2560 sc1\ns_mov_b64 exec, -1\nv_exp_f32 v82, v82\nv_exp_f32 v83, v83\nv_exp_f32 v84, v84\n"
                "v_mfma_f32_32x32x16_bf16 v[96:111], v[112:115], v[68:71], 0\nv_exp_f32 v85, v85\nv_exp_f32 v86, v86\nv_exp_f32 v87, v87\nv_exp_f32 v88, v88\n"
                "v_exp_f32 v89, v89\nv_smfmac_f32_32x32x32_bf16 v[0:15], v[116:119], v[80:87], v160\nv_exp_f32 v90, v90\nv_exp_f32 v91, v91\nv_exp_f32 v92, v92\n"
                "v_exp_f32 v93, v93\nv_exp_f32 v94, v94\nv_exp_f32 v95, v95\nv_exp_f32 v96, v96\nv_exp_f32 v97, v97\n"
                "v_smfmac_f32_32x32x32_bf16 v[0:15], v[120:123], v[88:95], v160\nv_exp_f32 v98, v98\nv_exp_f32 v99, v99\nv_exp_f32 v100, v100\n"
                "v_mfma_f32_32x32x16_bf16 v[80:95], v[112:115], v[72:75], 0\nv_exp_f32 v101, v101\nv_exp_f32 v102, v102\nv_exp_f32 v103, v103\nv_exp_f32 v104, v104\n"
                "v_exp_f32 v105, v105\nv_smfmac_f32_32x32x32_bf16 v[16:31], v[116:119], v[96:103], v160\nv_exp_f32 v106, v106\nv_exp_f32 v107, v107\n"
                "v_exp_f32 v108, v108\nv_exp_f32 v109, v109\nv_exp_f32 v110, v110\nv_exp_f32 v111, v111\nv_exp_f32 v80, v80\nv_exp_f32 v81, v81\n"
                "v_smfmac_f32_32x32x32_bf16 v[16:31], v[120:123], v[104:111], v160\nv_exp_f32 v82, v82\nv_exp_f32 v83, v83\nv_exp_f32 v84, v84\n"
                "v_mfma_f32_32x32x16_bf16 v[96:111], v[112:115], v[76:79], 0\nv_exp_f32 v85, v85\nv_exp_f32 v86, v86\nv_exp_f32 v87, v87\nv_exp_f32 v88, v88\n"
                "v_exp_f32 v89, v89\nv_smfmac_f32_32x32x32_bf16 v[32:47], v[116:119], v[80:87], v160\nv_exp_f32 v90, v90\nv_exp_f32 v91, v91\nv_exp_f32 v92, v92\n"
                "v_exp_f32 v93, v93\nv_exp_f32 v94, v94\nv_exp_f32 v95, v95\nv_exp_f32 v96, v96\nv_exp_f32 v97, v97\n"
                "v_smfmac_f32_32x32x32_bf16 v[32:47], v[120:123], v[88:95], v160\nv_exp_f32 v98, v98\nv_exp_f32 v99, v99\nv_exp_f32 v100, v100\ns_waitcnt vmcnt(6)\n"
                "v_mfma_f32_32x32x16_bf16 v[80:95], v[124:127], v[64:67], 0\nv_exp_f32 v101, v101\nv_exp_f32 v102, v102\nv_exp_f32 v103, v103\nv_exp_f32 v104, v104\n"
                "v_exp_f32 v105, v105\nv_smfmac_f32_32x32x32_bf16 v[48:63], v[116:119], v[96:103], v160\nv_exp_f32 v106, v106\nv_exp_f32 v107, v107\n"
                "v_exp_f32 v108, v108\nv_exp_f32 v109, v109\nv_exp_f32 v110, v110\nv_exp_f32 v111, v111\nv_exp_f32 v80, v80\nv_exp_f32 v81, v81\n"
                "v_smfmac_f32_32x32x32_bf16 v[48:63], v[120:123], v[104:111], v160\nv_exp_f32 v82, v82\nv_exp_f32 v83, v83\nv_exp_f32 v84, v84\n"
                "v_mfma_f32_32x32x16_bf16 v[96:111], v[124:127], v[68:71], 0\nv_exp_f32 v85, v85\nv_exp_f32 v86, v86\nv_exp_f32 v87, v87\nv_exp_f32 v88, v88\n"
                "v_exp_f32 v89, v89\nv_smfmac_f32_32x32x32_bf16 v[0:15], v[128:131], v[80:87], v160\nv_exp_f32 v90, v90\nv_exp_f32 v91, v91\nv_exp_f32 v92, v92\n"
                "v_exp_f32 v93, v93\nv_exp_f32 v94, v94\nv_exp_f32 v95, v95\nv_exp_f32 v96, v96\nv_exp_f32 v97, v97\n"
                "v_smfmac_f32_32x32x32_bf16 v[0:15], v[132:135], v[88:95], v160\nv_exp_f32 v98, v98\nv_exp_f32 v99, v99\nv_exp_f32 v100, v100\n"
                "v_mfma_f32_32x32x16_bf16 v[80:95], v[124:127], v[72:75], 0\nv_exp_f32 v101, v101\nv_exp_f32 v102, v102\nv_exp_f32 v103, v103\nv_exp_f32 v104, v104\n"
                "v_exp_f32 v105, v105\nv_smfmac_f32_32x32x32_bf16 v[16:31], v[128:131], v[96:103], v160\nv_exp_f32 v106, v106\nv_exp_f32 v107, v107\n"
                "v_exp_f32 v108, v108\nv_exp_f32 v109, v109\nv_exp_f32 v110, v110\nv_exp_f32 v111, v111\nv_exp_f32 v80, v80\nv_exp_f32 v81, v81\n"
                "v_smfmac_f32_32x32x32_bf16 v[16:31], v[132:135], v[104:111], v160\nv_exp_f32 v82, v82\nv_exp_f32 v83, v83\nv_exp_f32 v84, v84\n"
                "v_mfma_f32_32x32x16_bf16 v[96:111], v[124:127], v[76:79], 0\nv_exp_f32 v85, v85\nv_exp_f32 v86, v86\nv_exp_f32 v87, v87\nv_exp_f32 v88, v88\n"
                "v_exp_f32 v89, v89\nv_smfmac_f32_32x32x32_bf16 v[32:47], v[128:131], v[80:87], v160\nv_exp_f32 v90, v90\nv_exp_f32 v91, v91\nv_exp_f32 v92, v92\n"
                "v_exp_f32 v93, v93\nv_exp_f32 v94, v94\nv_exp_f32 v95, v95\nv_exp_f32 v96, v96\nv_exp_f32 v97, v97\n"
                "v_smfmac_f32_32x32x32_bf16 v[32:47], v[132:135], v[88:95], v160\nv_exp_f32 v98, v98\nv_exp_f32 v99, v99\nv_exp_f32 v100, v100\ns_waitcnt vmcnt(3)\n"
                "v_mfma_f32_32x32x16_bf16 v[80:95], v[136:139], v[64:67], 0\nv_exp_f32 v101, v101\nv_exp_f32 v102, v102\nv_exp_f32 v103, v103\nv_exp_f32 v104, v104\n"
                "v_exp_f32 v105, v105\nv_smfmac_f32_32x32x32_bf16 v[48:63], v[128:131], v[96:103], v160\nv_exp_f32 v106, v106\nv_exp_f32 v107, v107\n"
                "v_exp_f32 v108, v108\nv_exp_f32 v109, v109\nv_exp_f32 v110, v110\nv_exp_f32 v111, v111\nv_exp_f32 v80, v80\nv_exp_f32 v81, v81\n"
                "v_smfmac_f32_32x32x32_bf16 v[48:63], v[132:135], v[104:111], v160\nv_exp_f32 v82, v82\nv_exp_f32 v83, v83\nv_exp_f32 v84, v84\n"
                "v_mfma_f32_32x32x16_bf16 v[96:111], v[136:139], v[68:71], 0\nv_exp_f32 v85, v85\nv_exp_f32 v86, v86\nv_exp_f32 v87, v87\nv_exp_f32 v88, v88\n"
                "v_exp_f32 v89, v89\nv_smfmac_f32_32x32x32_bf16 v[0:15], v[140:143], v[80:87], v160\nv_exp_f32 v90, v90\nv_exp_f32 v91, v91\nv_exp_f32 v92, v92\n"
                "v_exp_f32 v93, v93\nv_exp_f32 v94, v94\nv_exp_f32 v95, v95\nv_exp_f32 v96, v96\nv_exp_f32 v97, v97\n"
                "v_smfmac_f32_32x32x32_bf16 v[0:15], v[144:147], v[88:95], v160\nv_exp_f32 v98, v98\nv_exp_f32 v99, v99\nv_exp_f32 v100, v100\n"
                "v_mfma_f32_32x32x16_bf16 v[80:95], v[136:139], v[72:75], 0\nv_exp_f32 v101, v101\nv_exp_f32 v102, v102\nv_exp_f32 v103, v103\nv_exp_f32 v104, v104\n"
                "v_exp_f32 v105, v105\nv_smfmac_f32_32x32x32_bf16 v[16:31], v[140:143], v[96:103], v160\nv_exp_f32 v106, v106\nv_exp_f32 v107, v107\n"
                "v_exp_f32 v108, v108\nv_exp_f32 v109, v109\nv_exp_f32 v110, v110\nv_exp_f32 v111, v111\nv_exp_f32 v80, v80\nv_exp_f32 v81, v81\n"
                "v_smfmac_f32_32x32x32_bf16 v[16:31], v[144:147], v[104:111], v160\nv_exp_f32 v82, v82\nv_exp_f32 v83, v83\nv_exp_f32 v84, v84\n"
                "v_mfma_f32_32x32x16_bf16 v[96:111], v[136:139], v[76:79], 0\nv_exp_f32 v85, v85\nv_exp_f32 v86, v86\nv_exp_f32 v87, v87\nv_exp_f32 v88, v88\n"
                "v_exp_f32 v89, v89\nv_smfmac_f32_32x32x32_bf16 v[32:47], v[140:143], v[80:87], v160\nv_exp_f32 v90, v90\nv_exp_f32 v91, v91\nv_exp_f32 v92, v92\n"
                "v_exp_f32 v93, v93\nv_exp_f32 v94, v94\nv_exp_f32 v95, v95\nv_exp_f32 v96, v96\nv_exp_f32 v97, v97\n"
                "v_smfmac_f32_32x32x32_bf16 v[32:47], v[144:147], v[88:95], v160\nv_exp_f32 v98, v98\nv_exp_f32 v99, v99\nv_exp_f32 v100, v100\ns_waitcnt vmcnt(0)\n"
                "v_mfma_f32_32x32x16_bf16 v[80:95], v[148:151], v[64:67], 0\nv_exp_f32 v101, v101\nv_exp_f32 v102, v102\nv_exp_f32 v103, v103\nv_exp_f32 v104, v104\n"
                "v_exp_f32 v105, v105\nv_smfmac_f32_32x32x32_bf16 v[48:63], v[140:143], v[96:103], v160\nv_exp_f32 v106, v106\nv_exp_f32 v107, v107\n"
                "v_exp_f32 v108, v108\nv_exp_f32 v109, v109\nv_exp_f32 v110, v110\nv_exp_f32 v111, v111\nv_exp_f32 v80, v80\nv_exp_f32 v81, v81\n"
                "v_smfmac_f32_32x32x32_bf16 v[48:63], v[144:147], v[104:111], v160\nv_exp_f32 v82, v82\nv_exp_f32 v83, v83\nv_exp_f32 v84, v84\n"
                "v_mfma_f32_32x32x16_bf16 v[96:111], v[148:151], v[68:71], 0\nv_exp_f32 v85, v85\nv_exp_f32 v86, v86\nv_exp_f32 v87, v87\nv_exp_f32 v88, v88\n"
                "v_exp_f32 v89, v89\nv_smfmac_f32_32x32x32_bf16 v[0:15], v[152:155], v[80:87], v160\nv_exp_f32 v90, v90\nv_exp_f32 v91, v91\nv_exp_f32 v92, v92\n"
                "v_exp_f32 v93, v93\nv_exp_f32 v94, v94\nv_exp_f32 v95, v95\nv_exp_f32 v96, v96\nv_exp_f32 v97, v97\n"
                "v_smfmac_f32_32x32x32_bf16 v[0:15], v[156:159], v[88:95], v160\nv_exp_f32 v98, v98\nv_exp_f32 v99, v99\nv_exp_f32 v100, v100\n"
                "v_mfma_f32_32x32x16_bf16 v[80:95], v[148:151], v[72:75], 0\nv_exp_f32 v101, v101\nv_exp_f32 v102, v102\nv_exp_f32 v103, v103\nv_exp_f32 v104, v104\n"
                "v_exp_f32 v105, v105\nv_smfmac_f32_32x32x32_bf16 v[16:31], v[152:155], v[96:103], v160\nv_exp_f32 v106, v106\nv_exp_f32 v107, v107\n"
                "v_exp_f32 v108, v108\nv_exp_f32 v109, v109\nv_exp_f32 v110, v110\nv_exp_f32 v111, v111\nv_exp_f32 v80, v80\nv_exp_f32 v81, v81\n"
                "v_smfmac_f32_32x32x32_bf16 v[16:31], v[156:159], v[104:111], v160\nv_exp_f32 v82, v82\nv_exp_f32 v83, v83\nv_exp_f32 v84, v84\n"
                "v_mfma_f32_32x32x16_bf16 v[96:111], v[148:151], v[76:79], 0\nv_exp_f32 v85, v85\nv_exp_f32 v86, v86\nv_exp_f32 v87, v87\nv_exp_f32 v88, v88\n"
                "v_exp_f32 v89, v89\nv_smfmac_f32_32x32x32_bf16 v[32:47], v[152:155], v[80:87], v160\nv_exp_f32 v90, v90\nv_exp_f32 v91, v91\nv_exp_f32 v92, v92\n"
                "v_exp_f32 v93, v93\nv_exp_f32 v94, v94\nv_exp_f32 v95, v95\nv_exp_f32 v96, v96\nv_exp_f32 v97, v97\n"
                "v_smfmac_f32_32x32x32_bf16 v[32:47], v[156:159], v[88:95], v160\nv_exp_f32 v98, v98\nv_exp_f32 v99, v99\nv_exp_f32 v100, v100\nv_exp_f32 v101, v101\n"
                "v_exp_f32 v102, v102\nv_exp_f32 v103, v103\nv_exp_f32 v104, v104\nv_exp_f32 v105, v105\n"
                "v_smfmac_f32_32x32x32_bf16 v[48:63], v[152:155], v[96:103], v160\nv_exp_f32 v106, v106\nv_exp_f32 v107, v107\nv_exp_f32 v108, v108\n"
                "v_exp_f32 v109, v109\nv_exp_f32 v110, v110\nv_exp_f32 v111, v111\ns_nop 1\nv_smfmac_f32_32x32x32_bf16 v[48:63], v[156:159], v[104:111], v160\n"
                "s_nop 11\n"
                : "=&{v[0:15]}"(o0), "=&{v[16:31]}"(o1), "=&{v[32:47]}"(o2), "=&{v[48:63]}"(o3), "+{s[40:41]}"(base)
                : "{v[64:67]}"(q0), "{v[68:71]}"(q1), "{v[72:75]}"(q2), "{v[76:79]}"(q3), "{v160}"(0xddddu), "{v161}"(lane * 16),
                  "{v162}"(h * 256 + (r & 15) * 16), "{v163}"(one), "{s[42:43]}"(0x0000ffff0000ffffull)
                : "memory", "scc", "s44", "v80", "v81", "v82", "v83", "v84", "v85", "v86", "v87", "v88", "v89", "v90", "v91", "v92", "v93", "v94", "v95",
                  "v96", "v97", "v98", "v99", "v100", "v101", "v102", "v103", "v104", "v105", "v106", "v107", "v108", "v109", "v110", "v111", "v112", "v113",
                  "v114", "v115", "v116", "v117", "v118", "v119", "v120", "v121", "v122", "v123", "v124", "v125", "v126", "v127", "v128", "v129", "v130",
                  "v131", "v132", "v133", "v134", "v135", "v136", "v137", "v138", "v139", "v140", "v141", "v142", "v143", "v144", "v145", "v146", "v147",
                  "v148", "v149", "v150", "v151", "v152", "v153", "v154", "v155", "v156", "v157", "v158", "v159");
        } else {
            m0 = m1 = m2 = m3 = -INFINITY;
            const u32x4 ones = {0x3f803f80u, 0x3f803f80u, 0x3f803f80u, 0x3f803f80u};
#pragma unroll 1
            for (int t = 0; t < 16; ++t) {
                const u32x4 kf = __builtin_amdgcn_raw_buffer_load_b128(wsr, kvo + t * 2048 + lane * 16, 0, SC1);
                u32x4 v0 = __builtin_amdgcn_raw_buffer_load_b128(wsr, kvo + t * 2048 + 1024 + h * 256 + (r & 15) * 16, 0, SC1);
                u32x4 v1 = __builtin_amdgcn_raw_buffer_load_b128(wsr, kvo + t * 2048 + 1536 + h * 256 + (r & 15) * 16, 0, SC1);
                if (r >= 16) { v0 = ones; v1 = ones; }
                auto step = [&](const u32x4& q, f32x16& o, float& m) {
                    f32x16 s = mfma32(kf, q, (f32x16){});
                    float mx = s[0];
#pragma unroll
                    for (int i = 1; i < 16; ++i) mx = fmaxf(mx, s[i]);
                    const float mn = fmaxf(m, halves_max(mx)), al = __builtin_amdgcn_exp2f(m - mn);
                    m = mn;
#pragma unroll
                    for (int i = 0; i < 16; ++i) { s[i] = __builtin_amdgcn_exp2f(s[i] - mn); o[i] *= al; }
                    const f32x8 lo = __builtin_shufflevector(s, s, 0, 1, 2, 3, 4, 5, 6, 7), hi = __builtin_shufflevector(s, s, 8, 9, 10, 11, 12, 13, 14, 15);
                    o = __builtin_amdgcn_smfmac_f32_32x32x32_bf16(__builtin_bit_cast(bf16x8, v0), __builtin_bit_cast(bf16x16, lo), o, 0xdddd, 0, 0);
                    o = __builtin_amdgcn_smfmac_f32_32x32x32_bf16(__builtin_bit_cast(bf16x8, v1), __builtin_bit_cast(bf16x16, hi), o, 0xdddd, 0, 0);
                };
                step(q0, o0, m0); step(q1, o1, m1); step(q2, o2, m2); step(q3, o3, m3);
            }
        }
        STAMP(8);
        while (__hip_atomic_load((LAS u32*)(L_SYNC + 16), __ATOMIC_RELAXED, __HIP_MEMORY_SCOPE_WORKGROUP) != 4) __builtin_amdgcn_s_sleep(1);
        asm volatile("" ::: "memory");
        auto put = [&](int i, const f32x16& o, float m) {
            const u32 rec = L_PART + ((a * 4 + ((tb + i) & 3)) * 3 + i - 1) * 2560;
            ST(f32x4, rec + lane * 16) = (f32x4){o[0], o[1], o[2], o[3]};
            ST(f32x4, rec + 1024 + lane * 16) = (f32x4){o[4], o[5], o[6], o[7]};
            ST(f32x2, rec + 2048 + lane * 8) = (f32x2){o[8], m};
        };
        put(1, o1, m1); put(2, o2, m2); put(3, o3, m3);
        BAR(0);
        {
            f32x4 pa[3], pb[3]; f32x2 lm[3];
#pragma unroll
            for (int i = 0; i < 3; ++i) {
                const u32 rec = L_PART + ((a * 4 + tb) * 3 + i) * 2560;
                pa[i] = LD(f32x4, rec + lane * 16); pb[i] = LD(f32x4, rec + 1024 + lane * 16); lm[i] = LD(f32x2, rec + 2048 + lane * 8);
            }
            const float mm = fmaxf(fmaxf(m0, lm[0].y), fmaxf(lm[1].y, lm[2].y));
            const float s0 = __builtin_amdgcn_exp2f(m0 - mm);
            float l = o0[8] * s0, acc[8];
#pragma unroll
            for (int j = 0; j < 8; ++j) acc[j] = o0[j] * s0;
#pragma unroll
            for (int i = 0; i < 3; ++i) {
                const float s = __builtin_amdgcn_exp2f(lm[i].y - mm);
                l = fmaf(lm[i].x, s, l);
#pragma unroll
                for (int j = 0; j < 4; ++j) { acc[j] = fmaf(pa[i][j], s, acc[j]); acc[4 + j] = fmaf(pb[i][j], s, acc[4 + j]); }
            }
            const float rl = __builtin_amdgcn_rcpf(l);
            const u32x4 of = {pk2(acc[0] * rl, acc[1] * rl), pk2(acc[2] * rl, acc[3] * rl), pk2(acc[4] * rl, acc[5] * rl), pk2(acc[6] * rl, acc[7] * rl)};
            __builtin_amdgcn_raw_buffer_store_b128(of, wsr, WS_OF + ((qb * 4 + tb) * 32 + hp * 2 + a) * 1024 + lane * 16, 0, SC1);
        }
        BAR(0);
        if (FUSED && threadIdx.x == 0) __hip_atomic_store((GAS u64*)(ws + WS_OFLAG + (qb * 16 + hp) * 128), tag << 16, RLX_AGENT);
        STAMP(9);
    }

    if (PH & 4) {
        const int tk = w & 1, k4 = w >> 1, g1 = (lane >> 4) & 1, q4 = (lane >> 2) & 3;
        if (!FUSED) BAR(0);
        poll_tag<FUSED>(ws + WS_YFLAG + ((cb64 * 64 + w * 8 + (lane & 7)) * 2 + (rb64 >> 4)) * 64, lane < 8, tag);
        const u32x4 yv = __builtin_amdgcn_raw_buffer_load_b128(wsr, WS_YT + ((cb64 * 64 + w * 8 + (lane >> 3)) * 2048 + rb64 * 64) * 2 + (lane & 7) * 16, 0, SC1);
        u32x4 wf[2][8];
#pragma unroll
        for (int c = 0; c < 2; ++c) {
            const u32 wa = L_WO + (k4 * 128 + 4 * h + q4) * 128 + (((c ^ (k4 >> 1) ^ (q4 >> 1)) & 1) << 6) + ((g1 ^ (k4 & 1)) << 5) + (lane & 3) * 8;
#pragma unroll
            for (int i = 0; i < 8; ++i) wf[c][i] = trd2(wa + i * 2048, wa + i * 2048 + 1024);
        }
        const float g = LD(float, L_G + ((hp >> 3) * 64 + tk * 32 + r) * 4);
        ST(u32x4, L_YT + (w * 64 + lane) * 16) = yv;
        poll_tag<FUSED>(ws + WS_OFLAG + (qb * 16 + k4 * 4 + (lane & 3)) * 128, lane < 4, tag);
        STAMP(10);
        u32x4 of[8];
#pragma unroll
        for (int i = 0; i < 8; ++i) of[i] = __builtin_amdgcn_raw_buffer_load_b128(wsr, WS_OF + ((rb64 * 2 + tk) * 32 + k4 * 8 + i) * 1024 + lane * 16, 0, SC1);
        __builtin_amdgcn_sched_barrier(0);
        f32x16 c0 = {}, c1 = {};
#pragma unroll
        for (int i = 0; i < 8; ++i) { c0 = mfma32(wf[0][i], of[i], c0); c1 = mfma32(wf[1][i], of[i], c1); }
        STAMP(11);
        auto put = [&](int i, const f32x16& c, int o) {
            const u32 rec = L_RED + ((tk * 4 + (i ^ k4)) * 3 + i - 1) * 2048 + lane * 16;
            ST(f32x4, rec) = (f32x4){c[o], c[o + 1], c[o + 2], c[o + 3]};
            ST(f32x4, rec + 1024) = (f32x4){c[o + 4], c[o + 5], c[o + 6], c[o + 7]};
        };
        put(1, c0, 8); put(2, c1, 0); put(3, c1, 8);
        BAR_LDS();
        if (FUSED && b == 0 && threadIdx.x == 0) *(GAS u64*)(ws + WS_NONCE) = nonce + 1;
        float acc[8];
#pragma unroll
        for (int j = 0; j < 8; ++j) acc[j] = c0[j];
#pragma unroll
        for (int i = 0; i < 3; ++i) {
            const u32 rec = L_RED + ((tk * 4 + k4) * 3 + i) * 2048 + lane * 16;
            const f32x4 u = LD(f32x4, rec), v = LD(f32x4, rec + 1024);
#pragma unroll
            for (int j = 0; j < 4; ++j) { acc[j] += u[j]; acc[4 + j] += v[j]; }
        }
        const u32 ya = L_YT + (k4 * 16 + 4 * h + q4) * 128 + (tk * 32 + g1 * 16 + (lane & 3) * 4) * 2;
        const u32x4 yf = trd2(ya, ya + 1024);
        u32 pk[4];
#pragma unroll
        for (int j = 0; j < 4; ++j)
            pk[j] = pk2(fmaf(g, bflo(yf[j]) - acc[2 * j], acc[2 * j]), fmaf(g, bfhi(yf[j]) - acc[2 * j + 1], acc[2 * j + 1]));
#define OUT_AUX 0
        __builtin_amdgcn_raw_buffer_store_b128(widen(pk[0], pk[1], pk[2], pk[3]), __builtin_amdgcn_make_buffer_rsrc(p.out, 0, 2048 * 512 * 2, 0x00020000),
                                               ((rb64 * 64 + tk * 32 + r) * 512 + cb64 * 64 + k4 * 16 + h * 8) * 2, 0, OUT_AUX);
        STAMP(12);
    }
}

extern "C" void kernel_launch(void* const* d_in, const int* in_sizes, int n_in, void* d_out, int out_size, void* d_ws, size_t ws_size,
                              hipStream_t stream) {
    Params p;
    p.x = (const char*)d_in[0]; p.a_re = (const char*)d_in[1]; p.a_im = (const char*)d_in[2]; p.b_re = (const char*)d_in[3];
    p.b_im = (const char*)d_in[4]; p.c_re = (const char*)d_in[5]; p.c_im = (const char*)d_in[6]; p.log_dt = (const char*)d_in[7];
    p.dskip = (const char*)d_in[8]; p.wqkv = (const char*)d_in[10]; p.wo = (const char*)d_in[11]; p.gate_w = (const char*)d_in[12];
    p.out = (char*)d_out; p.ws = (char*)d_ws;
    s4_mha_mix<7><<<256, 512, 0, stream>>>(p);
}
```

```cpp
#define v14
#include <hip/hip_runtime.h>
#include <hip/hip_bf16.h>
#include <stdint.h>
#pragma clang diagnostic ignored "-Wint-to-pointer-cast"

#define LAS __attribute__((address_space(3)))
#define GAS __attribute__((address_space(1)))
typedef unsigned u32;
typedef unsigned long long u64;
typedef __attribute__((ext_vector_type(2))) u32 u32x2;
typedef __attribute__((ext_vector_type(4))) u32 u32x4;
typedef __attribute__((ext_vector_type(8))) u32 u32x8;
typedef __attribute__((ext_vector_type(2))) float f32x2;
typedef __attribute__((ext_vector_type(4))) float f32x4;
typedef __attribute__((ext_vector_type(8))) float f32x8;
typedef __attribute__((ext_vector_type(16))) float f32x16;
typedef __attribute__((ext_vector_type(2))) __bf16 bf16x2;
typedef __attribute__((ext_vector_type(8))) __bf16 bf16x8;
typedef __attribute__((ext_vector_type(16))) __bf16 bf16x16;
typedef __attribute__((ext_vector_type(4))) short s16x4;
typedef __attribute__((ext_vector_type(2))) unsigned short u16x2;

constexpr u32 WS_KV = 0;
constexpr u32 WS_OF = 4u << 20;
constexpr u32 WS_YT = 6u << 20;
constexpr u32 WS_UT = 8u << 20;
constexpr u32 WS_KVFLAG = 10u << 20;
constexpr u32 WS_UFLAG = WS_KVFLAG + 65536;
constexpr u32 WS_OFLAG = WS_UFLAG + 65536;
constexpr u32 WS_YFLAG = WS_OFLAG + 131072;
constexpr u32 WS_NONCE = WS_YFLAG + 65536;
constexpr u32 WS_END = 11u << 20;
constexpr u32 WS_STAMP = 16u << 20;

constexpr u32 CHUNK = 28672, L_W = 16384;
constexpr u32 L_TAB = 4 * CHUNK, TAB_ROW = 272, TAB_A = 21 * TAB_ROW, TAB_CH = 53 * TAB_ROW;
constexpr u32 L_QX = L_TAB + 2 * TAB_CH;
constexpr u32 L_KF = L_QX + 8192, KF_CH = 4608;
constexpr u32 L_GW = L_KF + 2 * KF_CH;
constexpr u32 L_QN = L_GW + 1024;
constexpr u32 L_G = L_QN + 1024;
constexpr u32 L_SYNC = L_G + 512;
constexpr u32 S_PAIR = 0, S_READY = 8, S_PART = 12, S_Q = 20, S_SVC = 24, S_KF = 28, S_LANDED = 32, S_FREED = 64;
constexpr u32 L_KB = L_TAB, KB_CH = TAB_CH;
constexpr u32 L_UC = 0, UC_COPY = 4160, UC_CH = 4 * UC_COPY;
constexpr u32 L_CX = 2 * UC_CH;
constexpr u32 L_WO = 49152;
constexpr u32 L_YT = L_KF;
constexpr u32 LDS_BYTES = 163840;
static_assert(L_SYNC + 80 <= LDS_BYTES && L_CX + 3 * 4096 <= L_WO && L_WO + 65536 <= L_TAB && L_TAB + 12 * 2560 <= L_KF, "lds map");

constexpr float LOG2E = 1.4426950408889634f;
constexpr float QSCALE = 0.25f * LOG2E;
constexpr float SAFE2 = 64.f * 64.f;
constexpr int CODE_BYTES = 18000;
constexpr u32 SPIN_CAP = 1u << 17;

#define LD(T, off) (*(const LAS T*)(u32)(off))
#define ST(T, off) (*(LAS T*)(u32)(off))
#define RLX_AGENT __ATOMIC_RELAXED, __HIP_MEMORY_SCOPE_AGENT
#define SC1 16
#define LGKM0() do { asm volatile("" ::: "memory"); __builtin_amdgcn_s_waitcnt(0xc07f); } while (0)
#define BAR(N) do { LGKM0(); asm volatile("s_waitcnt vmcnt(" #N ") lgkmcnt(0)\n\ts_barrier" ::: "memory"); } while (0)
#define BAR_LDS() do { LGKM0(); asm volatile("s_waitcnt lgkmcnt(0)\n\ts_barrier" ::: "memory"); } while (0)

#define STAMP(i) do { } while (0)
#define STAMP_ARG
#define STAMP_PASS

__device__ __forceinline__ u32 pk2(float a, float b) { bf16x2 v = {(__bf16)a, (__bf16)b}; return __builtin_bit_cast(u32, v); }
__device__ __forceinline__ float dot2(u32 a, u32 b, float c) {
    return __builtin_amdgcn_fdot2_f32_bf16(__builtin_bit_cast(bf16x2, a), __builtin_bit_cast(bf16x2, b), c, false); }
__device__ __forceinline__ float bflo(u32 v) { return __uint_as_float(v << 16); }
__device__ __forceinline__ float bfhi(u32 v) { return __uint_as_float(v & 0xffff0000u); }
__device__ __forceinline__ f32x16 mfma32(u32x4 a, u32x4 b, f32x16 c) {
    return __builtin_amdgcn_mfma_f32_32x32x16_bf16(__builtin_bit_cast(bf16x8, a), __builtin_bit_cast(bf16x8, b), c, 0, 0, 0); }
__device__ __forceinline__ f32x4 mfma16(u32x4 a, u32x4 b, f32x4 c) {
    return __builtin_amdgcn_mfma_f32_16x16x32_bf16(__builtin_bit_cast(bf16x8, a), __builtin_bit_cast(bf16x8, b), c, 0, 0, 0); }
__device__ __forceinline__ u32x2 trd(u32 off) {
    return __builtin_bit_cast(u32x2, __builtin_amdgcn_ds_read_tr16_b64_v4i16((LAS s16x4*)off)); }
__device__ __forceinline__ u32x4 trd2(u32 o0, u32 o1) { const u32x2 a = trd(o0), b = trd(o1); return (u32x4){a.x, a.y, b.x, b.y}; }
__device__ __forceinline__ void dma1(u32 v0, const void* sbase, u32 dst) {
    asm volatile("s_mov_b32 m0, %2\n\ts_nop 0\n\tglobal_load_lds_dwordx4 %0, %1" :: "v"(v0), "s"(sbase), "s"(dst) : "memory"); }
template <int OFF> __device__ __forceinline__ void dma_next(u32 v0, const void* sbase) {
    asm volatile("global_load_lds_dwordx4 %0, %1 offset:%2" :: "v"(v0), "s"(sbase), "i"(OFF) : "memory"); }
__device__ __forceinline__ void dma3(u32 v0, u32 v1, const void* sbase, u32 dst) {
    asm volatile("s_mov_b32 m0, %3\n\ts_nop 0\n\tglobal_load_lds_dwordx4 %0, %2\n\tglobal_load_lds_dwordx4 %1, %2 offset:1024\n\t"
                 "global_load_lds_dwordx4 %0, %2 offset:2048" :: "v"(v0), "v"(v1), "s"(sbase), "s"(dst) : "memory"); }
__device__ __forceinline__ void dma4(u32 v0, u32 v1, u32 v2, u32 v3, const void* sbase, u32 dst) {
    asm volatile("s_mov_b32 m0, %5\n\ts_nop 0\n\tglobal_load_lds_dwordx4 %0, %4\n\tglobal_load_lds_dwordx4 %1, %4 offset:1024\n\t"
                 "global_load_lds_dwordx4 %2, %4 offset:2048\n\tglobal_load_lds_dwordx4 %3, %4 offset:3072"
                 :: "v"(v0), "v"(v1), "v"(v2), "v"(v3), "s"(sbase), "s"(dst) : "memory"); }
__device__ __forceinline__ float halves_sum(float v) {
    auto r = __builtin_amdgcn_permlane32_swap(__float_as_uint(v), __float_as_uint(v), false, false);
    return __uint_as_float(r[0]) + __uint_as_float(r[1]); }
__device__ __forceinline__ float halves_max(float v) {
    auto r = __builtin_amdgcn_permlane32_swap(__float_as_uint(v), __float_as_uint(v), false, false);
    return fmaxf(__uint_as_float(r[0]), __uint_as_float(r[1])); }
template <int CTRL> __device__ __forceinline__ u32 dpp_max(u32 v) { return max(v, (u32)__builtin_amdgcn_update_dpp(0, (int)v, CTRL, 0xf, 0xf, true)); }
__device__ __forceinline__ u32 row_max(u32 v) { return dpp_max<0x118>(dpp_max<0x114>(dpp_max<0x112>(dpp_max<0x111>(v)))); }
__device__ __forceinline__ u32x4 widen(u32 a0, u32 a1, u32 b0, u32 b1) {
    auto r0 = __builtin_amdgcn_permlane32_swap(a0, b0, false, false);
    auto r1 = __builtin_amdgcn_permlane32_swap(a1, b1, false, false);
    return (u32x4){r0[0], r1[0], r0[1], r1[1]}; }
__device__ __forceinline__ f32x2 cmul(f32x2 a, f32x2 b) {
    float x, y;
    asm("v_mul_f32 %0, %2, %4\n\tv_mul_f32 %1, %2, %5\n\tv_fma_f32 %0, -%3, %5, %0\n\tv_fmac_f32 %1, %3, %4" : "=&v"(x), "=&v"(y) : "v"(a.x), "v"(a.y), "v"(b.x), "v"(b.y));
    return (f32x2){x, y}; }
__device__ __forceinline__ void cmul2(f32x2& o0, f32x2 a, f32x2 b, f32x2& o1, f32x2 c, f32x2 d) {
    float x, y, z, w;
    asm("v_mul_f32 %0, %4, %6\n\tv_mul_f32 %2, %8, %10\n\tv_mul_f32 %1, %4, %7\n\tv_mul_f32 %3, %8, %11\n\t"
        "v_fma_f32 %0, -%5, %7, %0\n\tv_fma_f32 %2, -%9, %11, %2\n\tv_fmac_f32 %1, %5, %6\n\tv_fmac_f32 %3, %9, %10"
        : "=&v"(x), "=&v"(y), "=&v"(z), "=&v"(w) : "v"(a.x), "v"(a.y), "v"(b.x), "v"(b.y), "v"(c.x), "v"(c.y), "v"(d.x), "v"(d.y));
    o0 = (f32x2){x, y}; o1 = (f32x2){z, w}; }
__device__ __forceinline__ f32x2 cexp_n(float zr2, float th, float n) {
    const float m = __builtin_amdgcn_exp2f(zr2 * n), a = __builtin_amdgcn_fractf(th * n);
    return (f32x2){m * __builtin_amdgcn_cosf(a), m * __builtin_amdgcn_sinf(a)}; }
__device__ __forceinline__ u64 poll_tag(const char* p, bool active, u64 tag) {
    u64 v = tag << 16;
#pragma unroll 1
    for (u32 spins = 0; spins < SPIN_CAP; ++spins) {
        if (active) v = __hip_atomic_load((const GAS u64*)p, RLX_AGENT);
        if (__all((v >> 16) == tag)) break;
        __builtin_amdgcn_s_sleep(1);
    }
    asm volatile("s_waitcnt vmcnt(0)" ::: "memory");
    __builtin_amdgcn_fence(__ATOMIC_ACQUIRE, "wavefront");
    return v; }

struct Params {
    const char *x, *a_re, *a_im, *b_re, *b_im, *c_re, *c_im, *log_dt, *dskip, *wqkv, *wo, *gate_w;
    char *out, *ws;
};

#define SB() __builtin_amdgcn_sched_barrier(0)
__device__ __forceinline__ void arrive(u32 off, int lane) {
    LGKM0();
    if (lane == 0) __hip_atomic_fetch_add((LAS u32*)(L_SYNC + off), 1u, __ATOMIC_RELAXED, __HIP_MEMORY_SCOPE_WORKGROUP); }
__device__ __forceinline__ void await(u32 off, u32 n) {
    while (__hip_atomic_load((LAS u32*)(L_SYNC + off), __ATOMIC_RELAXED, __HIP_MEMORY_SCOPE_WORKGROUP) < n) __builtin_amdgcn_s_sleep(0);
    asm volatile("" ::: "memory"); }
#define WAIT_VM(N) asm volatile("s_waitcnt vmcnt(" #N ")" ::: "memory")
#define SVC(i)
__device__ __forceinline__ void post(u32 off, int tb, u32 n) { __hip_atomic_store((LAS u32*)(L_SYNC + off + tb * 4), n, __ATOMIC_RELAXED, __HIP_MEMORY_SCOPE_WORKGROUP); }
__device__ __forceinline__ u32x4 peek4(u32 off) {
    u32x4 v; asm volatile("ds_read_b128 %0, %1" : "=&v"(v) : "v"(L_SYNC + off) : "memory"); return v; }
__device__ __forceinline__ void await4(u32 off, u32 n, u32x4 v) {
    for (;;) {
        asm volatile("s_waitcnt lgkmcnt(0)" : "+v"(v) :: "memory");
        if (__builtin_amdgcn_readfirstlane(min(min(v.x, v.y), min(v.z, v.w))) >= n) break;
        v = peek4(off);
    } }

__device__ __forceinline__ u32 w_off_q(int lane) { return (lane >> 2) * 3072 + ((lane & 3) << 4); }
__device__ __forceinline__ u32 w_off_kv(int lane, int hp) { return (lane >> 3) * 3072 + (((lane & 7) ^ ((lane >> 2) & 4)) << 4) + hp * 64; }

__device__ __forceinline__ u64 phase1_mfma(const Params& p, __amdgpu_buffer_rsrc_t wsr, char* ws, int lane, int tb, int hp, int qb STAMP_ARG) {
    const int r = lane & 31, h = lane >> 5, g1 = (lane >> 4) & 1, q4 = (lane >> 2) & 3;
    const bool ISV = hp >= 8;
#define MM_PRIO 0
#define SSM_PRIO 2
    __builtin_amdgcn_s_setprio(MM_PRIO);
    dma1(lane * 16, p.gate_w, L_GW);
    {
        const u32 vq = w_off_q(lane), vk = w_off_kv(lane, hp);
#pragma unroll
        for (int c = 0; c < 3; ++c) {
            const char* wsn = p.wqkv + (tb * 16 * 3072 + hp * 64 + ((c + (hp >> 1)) & 7) * (64 * 3072));
            dma1(vq, wsn, c * CHUNK + L_W + tb * 3072); dma_next<1024>(vk, wsn); dma_next<2048>(vk + 8 * 3072 - 1024, wsn);
        }
    }
    u32x2 nv;
    u32x4 xa;
#pragma unroll
    for (int ks = 0; ks < 4; ++ks) xa[ks] = (tb * 32 + r) * 128 + (((2 * ks + h) ^ ((r >> 1) & 7)) << 4);
    u32 w0 = L_W + (8 * h + q4) * 64 + (g1 << 5) + (lane & 3) * 8;
    u32 w1 = L_W + 1024 + (8 * h + q4) * 128 + ((q4 >> 1) << 6) + (g1 << 5) + (lane & 3) * 8, w2 = w1 ^ 64;
    u32x2 ut;
#pragma unroll
    for (int i = 0; i < 2; ++i) {
        const int row = 16 * h + 4 * i + q4;
        ut[i] = (tb * 32 + row) * 128 + (((4 * (hp & 1) + 2 * g1 + ((lane >> 1) & 1)) ^ ((row >> 1) & 7)) << 4) + (lane & 1) * 8;
    }
    const u32 tile = qb * 4 + tb, uto = WS_UT + ((hp * 32 + g1 * 16 + (lane & 15)) * 2048 + tile * 32 + h * 16) * 2;
    const u32x4 srd = {(u32)(uintptr_t)ws, (u32)((uintptr_t)ws >> 32) & 0xffffu, WS_END, 0x00020000u};
    const u64 ufp = (u64)(uintptr_t)(ws + WS_UFLAG + (hp * 64 + tile) * 64);
    u32 go = (hp >> 1) * 128;
    f32x16 a0, a1, a2;
    f32x4 ga, gb;
    if (hp < 8) {
        asm volatile(
            "v_mov_b32 v147, 0\nglobal_load_dwordx2 v[142:143], v147, s[50:51]\nv_mov_b32 v0, 0\nv_mov_b32 v1, 0\nv_mov_b32 v2, 0\nv_mov_b32 v3, 0\n"
            "v_mov_b32 v4, 0\nv_mov_b32 v5, 0\nv_mov_b32 v6, 0\nv_mov_b32 v7, 0\nv_mov_b32 v8, 0\nv_mov_b32 v9, 0\nv_mov_b32 v10, 0\nv_mov_b32 v11, 0\n"
            "v_mov_b32 v12, 0\nv_mov_b32 v13, 0\nv_mov_b32 v14, 0\nv_mov_b32 v15, 0\nv_mov_b32 v16, 0\nv_mov_b32 v17, 0\nv_mov_b32 v18, 0\nv_mov_b32 v19, 0\n"
            "v_mov_b32 v20, 0\nv_mov_b32 v21, 0\nv_mov_b32 v22, 0\nv_mov_b32 v23, 0\nv_mov_b32 v24, 0\nv_mov_b32 v25, 0\nv_mov_b32 v26, 0\nv_mov_b32 v27, 0\n"
            "v_mov_b32 v28, 0\nv_mov_b32 v29, 0\nv_mov_b32 v30, 0\nv_mov_b32 v31, 0\nv_mov_b32 v32, 0\nv_mov_b32 v33, 0\nv_mov_b32 v34, 0\nv_mov_b32 v35, 0\n"
            "v_mov_b32 v36, 0\nv_mov_b32 v37, 0\nv_mov_b32 v38, 0\nv_mov_b32 v39, 0\nv_mov_b32 v40, 0\nv_mov_b32 v41, 0\nv_mov_b32 v42, 0\nv_mov_b32 v43, 0\n"
            "v_mov_b32 v44, 0\nv_mov_b32 v45, 0\nv_mov_b32 v46, 0\nv_mov_b32 v47, 0\nv_mov_b32 v156, 0\nv_mov_b32 v157, 0\nv_mov_b32 v158, 0\nv_mov_b32 v159, 0\n"
            "v_mov_b32 v160, 0\nv_mov_b32 v161, 0\nv_mov_b32 v162, 0\nv_mov_b32 v163, 0\nv_mov_b32 v137, 0\ns_mov_b32 s40, 0\ns_mov_b32 s41, 28672\n"
            "v_add_u32 v135, s42, v136\nv_mov_b32 v141, 1\ns_waitcnt vmcnt(7)\nds_write_b32 v138, v141 offset:16\nv_mov_b32 v141, 8\nLka%=:\n"
            "ds_read_b32 v140, v139\ns_waitcnt lgkmcnt(0)\nv_cmp_ge_u32 vcc, s40, v140\ns_cbranch_vccnz Lka%=\nds_read_b64_tr_b16 v[148:149], v144\n"
            "ds_read_b64_tr_b16 v[150:151], v145\nv_xor_b32 v144, 64, v144\nv_xor_b32 v145, 64, v145\nds_read_b64_tr_b16 v[152:153], v144 offset:1024\n"
            "ds_read_b64_tr_b16 v[154:155], v145 offset:1024\nds_read_b128 v[48:51], v128\nds_read_b64_tr_b16 v[52:53], v132 offset:0\n"
            "ds_read_b64_tr_b16 v[54:55], v132 offset:256\nds_read_b64_tr_b16 v[56:57], v133 offset:0\nds_read_b64_tr_b16 v[58:59], v133 offset:512\n"
            "ds_read_b64_tr_b16 v[60:61], v134 offset:0\nds_read_b64_tr_b16 v[62:63], v134 offset:512\nds_read_b128 v[64:67], v135 offset:0\n"
            "ds_write_b32 v138, v137 offset:32\ns_waitcnt lgkmcnt(9)\nbuffer_store_dwordx4 v[148:151], v146, s[44:47], 0 offen sc1\n"
            "buffer_store_dwordx4 v[152:155], v146, s[44:47], 0 offen offset:16 sc1\nds_read_b128 v[68:71], v129\nds_read_b64_tr_b16 v[72:73], v132 offset:3072\n"
            "ds_read_b64_tr_b16 v[74:75], v132 offset:3328\nds_read_b64_tr_b16 v[76:77], v133 offset:3072\nds_read_b64_tr_b16 v[78:79], v133 offset:3584\n"
            "ds_read_b64_tr_b16 v[80:81], v134 offset:3072\nds_read_b64_tr_b16 v[82:83], v134 offset:3584\nds_read_b128 v[84:87], v135 offset:32\n"
            "v_add_u32 v128, s41, v128\nv_add_u32 v129, s41, v129\n.p2align 3\nLkb%=:\ns_waitcnt lgkmcnt(9)\n"
            "v_mfma_f32_32x32x16_bf16 v[0:15], v[52:55], v[48:51], v[0:15]\nds_read_b128 v[88:91], v130\nds_read_b64_tr_b16 v[92:93], v132 offset:6144\n"
            "ds_read_b64_tr_b16 v[94:95], v132 offset:6400\nv_add_u32 v130, s41, v130\nv_mfma_f32_4x4x4_16b_bf16 v[156:159], v[64:65], v[48:49], v[156:159]\n"
            "v_mfma_f32_32x32x16_bf16 v[16:31], v[56:59], v[48:51], v[16:31]\nds_read_b64_tr_b16 v[96:97], v133 offset:6144\n"
            "ds_read_b64_tr_b16 v[98:99], v133 offset:6656\nds_read_b64_tr_b16 v[100:101], v134 offset:6144\ns_add_u32 s42, s42, 0x80\ns_and_b32 s42, s42, 0x380\n"
            "v_mfma_f32_4x4x4_16b_bf16 v[160:163], v[66:67], v[50:51], v[160:163]\nv_mfma_f32_32x32x16_bf16 v[32:47], v[60:63], v[48:51], v[32:47]\n"
            "ds_read_b64_tr_b16 v[102:103], v134 offset:6656\nds_read_b128 v[104:107], v135 offset:64\ns_waitcnt vmcnt(3)\nds_write_b32 v138, v141 offset:16\n"
            "s_waitcnt lgkmcnt(9)\nv_mfma_f32_32x32x16_bf16 v[0:15], v[72:75], v[68:71], v[0:15]\nds_read_b32 v140, v139\nds_read_b128 v[108:111], v131\n"
            "ds_read_b64_tr_b16 v[112:113], v132 offset:9216\nds_read_b64_tr_b16 v[114:115], v132 offset:9472\nv_add_u32 v131, s41, v131\n"
            "v_mfma_f32_4x4x4_16b_bf16 v[156:159], v[84:85], v[68:69], v[156:159]\nv_mfma_f32_32x32x16_bf16 v[16:31], v[76:79], v[68:71], v[16:31]\n"
            "ds_read_b64_tr_b16 v[116:117], v133 offset:9216\nds_read_b64_tr_b16 v[118:119], v133 offset:9728\nds_read_b64_tr_b16 v[120:121], v134 offset:9216\n"
            "ds_read_b64_tr_b16 v[122:123], v134 offset:9728\nv_add_u32 v132, s41, v132\nv_mfma_f32_4x4x4_16b_bf16 v[160:163], v[86:87], v[70:71], v[160:163]\n"
            "v_mfma_f32_32x32x16_bf16 v[32:47], v[80:83], v[68:71], v[32:47]\nds_read_b128 v[124:127], v135 offset:96\nv_add_u32 v133, s41, v133\n"
            "v_add_u32 v134, s41, v134\nv_add_u32 v135, s42, v136\ns_add_u32 s40, s40, 1\ns_cmp_eq_u32 s40, 8\ns_cbranch_scc1 Lkd%=\ns_waitcnt lgkmcnt(10)\n"
            "v_mfma_f32_32x32x16_bf16 v[0:15], v[92:95], v[88:91], v[0:15]\ns_waitcnt lgkmcnt(8)\nv_cmp_ge_u32 vcc, s40, v140\ns_cbranch_vccnz Lks%=\nLkc%=:\n"
            "s_mov_b32 s41, 28672\ns_cmp_eq_u32 s40, 3\ns_cmov_b32 s41, -86016\nv_mfma_f32_4x4x4_16b_bf16 v[156:159], v[104:105], v[88:89], v[156:159]\n"
            "v_mfma_f32_32x32x16_bf16 v[16:31], v[96:99], v[88:91], v[16:31]\nds_read_b128 v[48:51], v128\nds_read_b64_tr_b16 v[52:53], v132 offset:0\n"
            "ds_read_b64_tr_b16 v[54:55], v132 offset:256\nds_read_b64_tr_b16 v[56:57], v133 offset:0\nv_add_u32 v128, s41, v128\n"
            "v_mfma_f32_4x4x4_16b_bf16 v[160:163], v[106:107], v[90:91], v[160:163]\nv_mfma_f32_32x32x16_bf16 v[32:47], v[100:103], v[88:91], v[32:47]\n"
            "ds_read_b64_tr_b16 v[58:59], v133 offset:512\nds_read_b64_tr_b16 v[60:61], v134 offset:0\nds_read_b64_tr_b16 v[62:63], v134 offset:512\n"
            "ds_read_b128 v[64:67], v135 offset:0\nv_add_u32 v137, 1, v137\ns_waitcnt lgkmcnt(8)\n"
            "v_mfma_f32_32x32x16_bf16 v[0:15], v[112:115], v[108:111], v[0:15]\nds_write_b32 v138, v137 offset:32\nds_read_b128 v[68:71], v129\n"
            "ds_read_b64_tr_b16 v[72:73], v132 offset:3072\nds_read_b64_tr_b16 v[74:75], v132 offset:3328\nv_add_u32 v129, s41, v129\n"
            "v_mfma_f32_4x4x4_16b_bf16 v[156:159], v[124:125], v[108:109], v[156:159]\nv_mfma_f32_32x32x16_bf16 v[16:31], v[116:119], v[108:111], v[16:31]\n"
            "ds_read_b64_tr_b16 v[76:77], v133 offset:3072\nds_read_b64_tr_b16 v[78:79], v133 offset:3584\nds_read_b64_tr_b16 v[80:81], v134 offset:3072\n"
            "v_mfma_f32_4x4x4_16b_bf16 v[160:163], v[126:127], v[110:111], v[160:163]\nv_mfma_f32_32x32x16_bf16 v[32:47], v[120:123], v[108:111], v[32:47]\n"
            "ds_read_b64_tr_b16 v[82:83], v134 offset:3584\nds_read_b128 v[84:87], v135 offset:32\ns_cmp_lg_u32 s40, 3\ns_cbranch_scc1 Lkb%=\ns_waitcnt vmcnt(0)\n"
            "v_add_co_u32 v148, vcc, 1, v142\ns_nop 1\nv_addc_co_u32 v149, vcc, 0, v143, vcc\nv_lshlrev_b64 v[148:149], 16, v[148:149]\ns_mov_b64 exec, 1\n"
            "global_store_dwordx2 v147, v[148:149], s[48:49] sc1\ns_mov_b64 exec, -1\ns_branch Lkb%=\nLks%=:\nds_read_b32 v140, v139\ns_waitcnt lgkmcnt(0)\n"
            "v_cmp_ge_u32 vcc, s40, v140\ns_cbranch_vccnz Lks%=\ns_branch Lkc%=\nLkd%=:\ns_waitcnt lgkmcnt(10)\n"
            "v_mfma_f32_32x32x16_bf16 v[0:15], v[92:95], v[88:91], v[0:15]\nv_add_u32 v137, 1, v137\n"
            "v_mfma_f32_4x4x4_16b_bf16 v[156:159], v[104:105], v[88:89], v[156:159]\nv_mfma_f32_32x32x16_bf16 v[16:31], v[96:99], v[88:91], v[16:31]\n"
            "v_mfma_f32_4x4x4_16b_bf16 v[160:163], v[106:107], v[90:91], v[160:163]\nv_mfma_f32_32x32x16_bf16 v[32:47], v[100:103], v[88:91], v[32:47]\n"
            "s_waitcnt lgkmcnt(0)\nv_mfma_f32_32x32x16_bf16 v[0:15], v[112:115], v[108:111], v[0:15]\nds_write_b32 v138, v137 offset:32\n"
            "v_mfma_f32_4x4x4_16b_bf16 v[156:159], v[124:125], v[108:109], v[156:159]\nv_mfma_f32_32x32x16_bf16 v[16:31], v[116:119], v[108:111], v[16:31]\n"
            "v_mfma_f32_4x4x4_16b_bf16 v[160:163], v[126:127], v[110:111], v[160:163]\nv_mfma_f32_32x32x16_bf16 v[32:47], v[120:123], v[108:111], v[32:47]\n"
            "s_nop 11\n"
            : "=&{v[0:15]}"(a0), "=&{v[16:31]}"(a1), "=&{v[32:47]}"(a2), "=&{v[156:159]}"(ga), "=&{v[160:163]}"(gb), "+{v[128:131]}"(xa), "+{v132}"(w0),
              "+{v133}"(w1), "+{v134}"(w2), "=&{v[142:143]}"(nv), "+{v[144:145]}"(ut), "+{s42}"(go)
            : "{v136}"(L_GW + h * 16), "{v138}"(L_SYNC + S_LANDED + tb * 4), "{v139}"(L_SYNC + S_LANDED + (lane & 7) * 4), "{v146}"(uto), "{s[44:47]}"(srd),
              "{s[48:49]}"(ufp), "{s[50:51]}"(ws + WS_NONCE)
            : "memory", "scc", "vcc", "s40", "s41", "v48", "v49", "v50", "v51", "v52", "v53", "v54", "v55", "v56", "v57", "v58", "v59", "v60", "v61", "v62",
              "v63", "v64", "v65", "v66", "v67", "v68", "v69", "v70", "v71", "v72", "v73", "v74", "v75", "v76", "v77", "v78", "v79", "v80", "v81", "v82",
              "v83", "v84", "v85", "v86", "v87", "v88", "v89", "v90", "v91", "v92", "v93", "v94", "v95", "v96", "v97", "v98", "v99", "v100", "v101", "v102",
              "v103", "v104", "v105", "v106", "v107", "v108", "v109", "v110", "v111", "v112", "v113", "v114", "v115", "v116", "v117", "v118", "v119", "v120",
              "v121", "v122", "v123", "v124", "v125", "v126", "v127", "v135", "v137", "v140", "v141", "v147", "v148", "v149", "v150", "v151", "v152", "v153",
              "v154", "v155");
    } else {
        asm volatile(
            "v_mov_b32 v147, 0\nglobal_load_dwordx2 v[142:143], v147, s[50:51]\nv_mov_b32 v0, 0\nv_mov_b32 v1, 0\nv_mov_b32 v2, 0\nv_mov_b32 v3, 0\n"
            "v_mov_b32 v4, 0\nv_mov_b32 v5, 0\nv_mov_b32 v6, 0\nv_mov_b32 v7, 0\nv_mov_b32 v8, 0\nv_mov_b32 v9, 0\nv_mov_b32 v10, 0\nv_mov_b32 v11, 0\n"
            "v_mov_b32 v12, 0\nv_mov_b32 v13, 0\nv_mov_b32 v14, 0\nv_mov_b32 v15, 0\nv_mov_b32 v16, 0\nv_mov_b32 v17, 0\nv_mov_b32 v18, 0\nv_mov_b32 v19, 0\n"
            "v_mov_b32 v20, 0\nv_mov_b32 v21, 0\nv_mov_b32 v22, 0\nv_mov_b32 v23, 0\nv_mov_b32 v24, 0\nv_mov_b32 v25, 0\nv_mov_b32 v26, 0\nv_mov_b32 v27, 0\n"
            "v_mov_b32 v28, 0\nv_mov_b32 v29, 0\nv_mov_b32 v30, 0\nv_mov_b32 v31, 0\nv_mov_b32 v32, 0\nv_mov_b32 v33, 0\nv_mov_b32 v34, 0\nv_mov_b32 v35, 0\n"
            "v_mov_b32 v36, 0\nv_mov_b32 v37, 0\nv_mov_b32 v38, 0\nv_mov_b32 v39, 0\nv_mov_b32 v40, 0\nv_mov_b32 v41, 0\nv_mov_b32 v42, 0\nv_mov_b32 v43, 0\n"
            "v_mov_b32 v44, 0\nv_mov_b32 v45, 0\nv_mov_b32 v46, 0\nv_mov_b32 v47, 0\nv_mov_b32 v156, 0\nv_mov_b32 v157, 0\nv_mov_b32 v158, 0\nv_mov_b32 v159, 0\n"
            "v_mov_b32 v160, 0\nv_mov_b32 v161, 0\nv_mov_b32 v162, 0\nv_mov_b32 v163, 0\nv_mov_b32 v137, 0\ns_mov_b32 s40, 0\ns_mov_b32 s41, 28672\n"
            "v_add_u32 v135, s42, v136\nv_mov_b32 v141, 1\ns_waitcnt vmcnt(7)\nds_write_b32 v138, v141 offset:16\nv_mov_b32 v141, 8\nLva%=:\n"
            "ds_read_b32 v140, v139\ns_waitcnt lgkmcnt(0)\nv_cmp_ge_u32 vcc, s40, v140\ns_cbranch_vccnz Lva%=\nds_read_b64_tr_b16 v[148:149], v144\n"
            "ds_read_b64_tr_b16 v[150:151], v145\nv_xor_b32 v144, 64, v144\nv_xor_b32 v145, 64, v145\nds_read_b64_tr_b16 v[152:153], v144 offset:1024\n"
            "ds_read_b64_tr_b16 v[154:155], v145 offset:1024\nds_read_b128 v[48:51], v128\nds_read_b64_tr_b16 v[52:53], v132 offset:0\n"
            "ds_read_b64_tr_b16 v[54:55], v132 offset:256\nds_read_b64_tr_b16 v[56:57], v133 offset:0\nds_read_b64_tr_b16 v[58:59], v133 offset:512\n"
            "ds_read_b64_tr_b16 v[60:61], v134 offset:0\nds_read_b64_tr_b16 v[62:63], v134 offset:512\nds_read_b128 v[64:67], v135 offset:0\n"
            "ds_write_b32 v138, v137 offset:32\ns_waitcnt lgkmcnt(9)\nbuffer_store_dwordx4 v[148:151], v146, s[44:47], 0 offen sc1\n"
            "buffer_store_dwordx4 v[152:155], v146, s[44:47], 0 offen offset:16 sc1\nds_read_b128 v[68:71], v129\nds_read_b64_tr_b16 v[72:73], v132 offset:3072\n"
            "ds_read_b64_tr_b16 v[74:75], v132 offset:3328\nds_read_b64_tr_b16 v[76:77], v133 offset:3072\nds_read_b64_tr_b16 v[78:79], v133 offset:3584\n"
            "ds_read_b64_tr_b16 v[80:81], v134 offset:3072\nds_read_b64_tr_b16 v[82:83], v134 offset:3584\nds_read_b128 v[84:87], v135 offset:32\n"
            "v_add_u32 v128, s41, v128\nv_add_u32 v129, s41, v129\n.p2align 3\nLvb%=:\ns_waitcnt lgkmcnt(9)\n"
            "v_mfma_f32_32x32x16_bf16 v[0:15], v[52:55], v[48:51], v[0:15]\nds_read_b128 v[88:91], v130\nds_read_b64_tr_b16 v[92:93], v132 offset:6144\n"
            "ds_read_b64_tr_b16 v[94:95], v132 offset:6400\nv_add_u32 v130, s41, v130\nv_mfma_f32_4x4x4_16b_bf16 v[156:159], v[64:65], v[48:49], v[156:159]\n"
            "v_mfma_f32_32x32x16_bf16 v[16:31], v[48:51], v[56:59], v[16:31]\nds_read_b64_tr_b16 v[96:97], v133 offset:6144\n"
            "ds_read_b64_tr_b16 v[98:99], v133 offset:6656\nds_read_b64_tr_b16 v[100:101], v134 offset:6144\ns_add_u32 s42, s42, 0x80\ns_and_b32 s42, s42, 0x380\n"
            "v_mfma_f32_4x4x4_16b_bf16 v[160:163], v[66:67], v[50:51], v[160:163]\nv_mfma_f32_32x32x16_bf16 v[32:47], v[48:51], v[60:63], v[32:47]\n"
            "ds_read_b64_tr_b16 v[102:103], v134 offset:6656\nds_read_b128 v[104:107], v135 offset:64\ns_waitcnt vmcnt(3)\nds_write_b32 v138, v141 offset:16\n"
            "s_waitcnt lgkmcnt(9)\nv_mfma_f32_32x32x16_bf16 v[0:15], v[72:75], v[68:71], v[0:15]\nds_read_b32 v140, v139\nds_read_b128 v[108:111], v131\n"
            "ds_read_b64_tr_b16 v[112:113], v132 offset:9216\nds_read_b64_tr_b16 v[114:115], v132 offset:9472\nv_add_u32 v131, s41, v131\n"
            "v_mfma_f32_4x4x4_16b_bf16 v[156:159], v[84:85], v[68:69], v[156:159]\nv_mfma_f32_32x32x16_bf16 v[16:31], v[68:71], v[76:79], v[16:31]\n"
            "ds_read_b64_tr_b16 v[116:117], v133 offset:9216\nds_read_b64_tr_b16 v[118:119], v133 offset:9728\nds_read_b64_tr_b16 v[120:121], v134 offset:9216\n"
            "ds_read_b64_tr_b16 v[122:123], v134 offset:9728\nv_add_u32 v132, s41, v132\nv_mfma_f32_4x4x4_16b_bf16 v[160:163], v[86:87], v[70:71], v[160:163]\n"
            "v_mfma_f32_32x32x16_bf16 v[32:47], v[68:71], v[80:83], v[32:47]\nds_read_b128 v[124:127], v135 offset:96\nv_add_u32 v133, s41, v133\n"
            "v_add_u32 v134, s41, v134\nv_add_u32 v135, s42, v136\ns_add_u32 s40, s40, 1\ns_cmp_eq_u32 s40, 8\ns_cbranch_scc1 Lvd%=\ns_waitcnt lgkmcnt(10)\n"
            "v_mfma_f32_32x32x16_bf16 v[0:15], v[92:95], v[88:91], v[0:15]\ns_waitcnt lgkmcnt(8)\nv_cmp_ge_u32 vcc, s40, v140\ns_cbranch_vccnz Lvs%=\nLvc%=:\n"
            "s_mov_b32 s41, 28672\ns_cmp_eq_u32 s40, 3\ns_cmov_b32 s41, -86016\nv_mfma_f32_4x4x4_16b_bf16 v[156:159], v[104:105], v[88:89], v[156:159]\n"
            "v_mfma_f32_32x32x16_bf16 v[16:31], v[88:91], v[96:99], v[16:31]\nds_read_b128 v[48:51], v128\nds_read_b64_tr_b16 v[52:53], v132 offset:0\n"
            "ds_read_b64_tr_b16 v[54:55], v132 offset:256\nds_read_b64_tr_b16 v[56:57], v133 offset:0\nv_add_u32 v128, s41, v128\n"
            "v_mfma_f32_4x4x4_16b_bf16 v[160:163], v[106:107], v[90:91], v[160:163]\nv_mfma_f32_32x32x16_bf16 v[32:47], v[88:91], v[100:103], v[32:47]\n"
            "ds_read_b64_tr_b16 v[58:59], v133 offset:512\nds_read_b64_tr_b16 v[60:61], v134 offset:0\nds_read_b64_tr_b16 v[62:63], v134 offset:512\n"
            "ds_read_b128 v[64:67], v135 offset:0\nv_add_u32 v137, 1, v137\ns_waitcnt lgkmcnt(8)\n"
            "v_mfma_f32_32x32x16_bf16 v[0:15], v[112:115], v[108:111], v[0:15]\nds_write_b32 v138, v137 offset:32\nds_read_b128 v[68:71], v129\n"
            "ds_read_b64_tr_b16 v[72:73], v132 offset:3072\nds_read_b64_tr_b16 v[74:75], v132 offset:3328\nv_add_u32 v129, s41, v129\n"
            "v_mfma_f32_4x4x4_16b_bf16 v[156:159], v[124:125], v[108:109], v[156:159]\nv_mfma_f32_32x32x16_bf16 v[16:31], v[108:111], v[116:119], v[16:31]\n"
            "ds_read_b64_tr_b16 v[76:77], v133 offset:3072\nds_read_b64_tr_b16 v[78:79], v133 offset:3584\nds_read_b64_tr_b16 v[80:81], v134 offset:3072\n"
            "v_mfma_f32_4x4x4_16b_bf16 v[160:163], v[126:127], v[110:111], v[160:163]\nv_mfma_f32_32x32x16_bf16 v[32:47], v[108:111], v[120:123], v[32:47]\n"
            "ds_read_b64_tr_b16 v[82:83], v134 offset:3584\nds_read_b128 v[84:87], v135 offset:32\ns_cmp_lg_u32 s40, 3\ns_cbranch_scc1 Lvb%=\ns_waitcnt vmcnt(0)\n"
            "v_add_co_u32 v148, vcc, 1, v142\ns_nop 1\nv_addc_co_u32 v149, vcc, 0, v143, vcc\nv_lshlrev_b64 v[148:149], 16, v[148:149]\ns_mov_b64 exec, 1\n"
            "global_store_dwordx2 v147, v[148:149], s[48:49] sc1\ns_mov_b64 exec, -1\ns_branch Lvb%=\nLvs%=:\nds_read_b32 v140, v139\ns_waitcnt lgkmcnt(0)\n"
            "v_cmp_ge_u32 vcc, s40, v140\ns_cbranch_vccnz Lvs%=\ns_branch Lvc%=\nLvd%=:\ns_waitcnt lgkmcnt(10)\n"
            "v_mfma_f32_32x32x16_bf16 v[0:15], v[92:95], v[88:91], v[0:15]\nv_add_u32 v137, 1, v137\n"
            "v_mfma_f32_4x4x4_16b_bf16 v[156:159], v[104:105], v[88:89], v[156:159]\nv_mfma_f32_32x32x16_bf16 v[16:31], v[88:91], v[96:99], v[16:31]\n"
            "v_mfma_f32_4x4x4_16b_bf16 v[160:163], v[106:107], v[90:91], v[160:163]\nv_mfma_f32_32x32x16_bf16 v[32:47], v[88:91], v[100:103], v[32:47]\n"
            "s_waitcnt lgkmcnt(0)\nv_mfma_f32_32x32x16_bf16 v[0:15], v[112:115], v[108:111], v[0:15]\nds_write_b32 v138, v137 offset:32\n"
            "v_mfma_f32_4x4x4_16b_bf16 v[156:159], v[124:125], v[108:109], v[156:159]\nv_mfma_f32_32x32x16_bf16 v[16:31], v[108:111], v[116:119], v[16:31]\n"
            "v_mfma_f32_4x4x4_16b_bf16 v[160:163], v[126:127], v[110:111], v[160:163]\nv_mfma_f32_32x32x16_bf16 v[32:47], v[108:111], v[120:123], v[32:47]\n"
            "s_nop 11\n"
            : "=&{v[0:15]}"(a0), "=&{v[16:31]}"(a1), "=&{v[32:47]}"(a2), "=&{v[156:159]}"(ga), "=&{v[160:163]}"(gb), "+{v[128:131]}"(xa), "+{v132}"(w0),
              "+{v133}"(w1), "+{v134}"(w2), "=&{v[142:143]}"(nv), "+{v[144:145]}"(ut), "+{s42}"(go)
            : "{v136}"(L_GW + h * 16), "{v138}"(L_SYNC + S_LANDED + tb * 4), "{v139}"(L_SYNC + S_LANDED + (lane & 7) * 4), "{v146}"(uto), "{s[44:47]}"(srd),
              "{s[48:49]}"(ufp), "{s[50:51]}"(ws + WS_NONCE)
            : "memory", "scc", "vcc", "s40", "s41", "v48", "v49", "v50", "v51", "v52", "v53", "v54", "v55", "v56", "v57", "v58", "v59", "v60", "v61", "v62",
              "v63", "v64", "v65", "v66", "v67", "v68", "v69", "v70", "v71", "v72", "v73", "v74", "v75", "v76", "v77", "v78", "v79", "v80", "v81", "v82",
              "v83", "v84", "v85", "v86", "v87", "v88", "v89", "v90", "v91", "v92", "v93", "v94", "v95", "v96", "v97", "v98", "v99", "v100", "v101", "v102",
              "v103", "v104", "v105", "v106", "v107", "v108", "v109", "v110", "v111", "v112", "v113", "v114", "v115", "v116", "v117", "v118", "v119", "v120",
              "v121", "v122", "v123", "v124", "v125", "v126", "v127", "v135", "v137", "v140", "v141", "v147", "v148", "v149", "v150", "v151", "v152", "v153",
              "v154", "v155");
    }
    const u64 nonce = (u64)(u32)__builtin_amdgcn_readfirstlane(nv.x) | ((u64)(u32)__builtin_amdgcn_readfirstlane(nv.y) << 32);
    const u64 tag = (nonce + 1) & 0xffffffffffffull;
    STAMP(4);
#define PK8(a, o) (u32x4){pk2(a[o], a[o + 1]), pk2(a[o + 2], a[o + 3]), pk2(a[o + 4], a[o + 5]), pk2(a[o + 6], a[o + 7])}
    const int pr = (r & 19) | ((r & 4) << 1) | ((r & 8) >> 1);
    const u32 kvo = WS_KV + (hp & 7) * 524288 + tile * 2048 + (ISV ? (r >> 4) * 131072 + 1024 + h * 256 + (r & 15) * 16 : (h * 32 + pr) * 16);
    __builtin_amdgcn_raw_buffer_store_b128(PK8(a1, 0), wsr, kvo, 0, SC1);
    __builtin_amdgcn_raw_buffer_store_b128(PK8(a1, 8), wsr, kvo + (ISV ? 512 : 131072), 0, SC1);
    __builtin_amdgcn_raw_buffer_store_b128(PK8(a2, 0), wsr, kvo + 262144, 0, SC1);
    __builtin_amdgcn_raw_buffer_store_b128(PK8(a2, 8), wsr, kvo + 262144 + (ISV ? 512 : 131072), 0, SC1);
    SB();
    auto norms = [&](const f32x16& a) {
        float n0 = 0.f, n1 = 0.f;
#pragma unroll
        for (int i = 0; i < 8; ++i) { n0 = fmaf(a[i], a[i], n0); n1 = fmaf(a[8 + i], a[8 + i], n1); }
        auto t = __builtin_amdgcn_permlane32_swap(__float_as_uint(n0), __float_as_uint(n1), false, false);
        return __uint_as_float(t[0]) + __uint_as_float(t[1]);
    };
    u32 kb1 = 0, kb2 = 0;
    if (!ISV) {
        const u32 k1 = dpp_max<0x142>(row_max(__float_as_uint(norms(a1)))), k2 = dpp_max<0x142>(row_max(__float_as_uint(norms(a2))));
        kb1 = min((k1 >> 16) + 1, 0x7fc0u); kb2 = min((k2 >> 16) + 1, 0x7fc0u);
    }
    asm volatile("s_waitcnt vmcnt(0)" : "+v"(kb1), "+v"(kb2) :: "memory");
    if (r == 31) {
        __hip_atomic_store((GAS u64*)(ws + WS_KVFLAG + (hp * 64 + tile) * 64 + h * 8), (tag << 16) | kb1, RLX_AGENT);
        if (!ISV) __hip_atomic_store((GAS u64*)(ws + WS_KVFLAG + (hp * 64 + tile) * 64 + 16 + h * 8), (tag << 16) | kb2, RLX_AGENT);
    }
    STAMP(5);
    a0 *= QSCALE;
    ST(u32x4, L_QX + tb * 1024 + lane * 16) = PK8(a0, 0);
    ST(u32x4, L_QX + 4096 + tb * 1024 + lane * 16) = PK8(a0, 8);
    ST(float, L_QN + h * 512 + (tb * 32 + r) * 4) = norms(a0);
#undef PK8
    const float gl = halves_sum(ga[0] + gb[0]);
    if (h == 0) ST(float, L_G + (tb * 32 + r) * 4) = gl;
    __builtin_amdgcn_s_setprio(0);
    arrive(S_Q, lane);
    STAMP(15);
    return nonce;
}

__device__ __forceinline__ u64 phase1_service(const Params& p, __amdgpu_buffer_rsrc_t wsr, char* ws, int lane, int tb, int hp, int qb, int d0, u32 pf STAMP_ARG) {
    u64 nonce;
    if (tb < 2) {
        const u32 vq = w_off_q(lane), vk = w_off_kv(lane, hp);
        u32 vx[4];
#pragma unroll
        for (int i = 0; i < 4; ++i) vx[i] = (7 * i + (lane >> 3)) * 1024 + (((lane & 7) ^ ((4 * i + (lane >> 4)) & 7)) << 4);
        const char* wsrc = p.wqkv + (tb * 32 * 3072 + hp * 64);
        const char* xsrc = p.x + (qb * 128 + tb * 64) * 1024;
        auto group = [&](int c, int g) {
            const int cc = (c + (hp >> 1)) & 7;
            if (g < 2) dma4(vx[0], vx[1], vx[2], vx[3], xsrc + (g * 32768 + cc * 128), (c & 3) * CHUNK + (tb * 2 + g) * 4096);
            else {
                const char* wsn = wsrc + (cc * 64 + (g - 2) * 16) * 3072;
                dma1(vq, wsn, (c & 3) * CHUNK + L_W + (tb * 2 + g - 2) * 3072); dma_next<1024>(vk, wsn); dma_next<2048>(vk + 8 * 3072 - 1024, wsn);
            }
        };
        u32 tap[16];
        auto taps = [&]() {
#pragma unroll
        for (int j = 0; j < 16; ++j) {
            const float t2 = (float)(2 * (32 * (lane >> 2) + 31 - 8 * (lane & 3) + 512 * (j >> 2) - 2 * (j & 3)));
            const u32 rr = pk2(t2, t2 - 2.f);
            tap[j] = ((u32)bflo(rr) | ((u32)bfhi(rr) << 16)) + (L_KF + tb * KF_CH - 131072) * 0x10001u;
        }
#pragma unroll
        for (int j = 0; j < 16; ++j) asm volatile("" : "+v"(tap[j]));
        };
        group(0, 0); group(0, 1);
        group(1, 0); group(1, 1); taps(); WAIT_VM(8); post(S_LANDED, tb, 1); STAMP(3);
        group(2, 0); group(2, 1); WAIT_VM(8); post(S_LANDED, tb, 2); SVC(6);
        group(3, 0); group(3, 1); group(3, 2); group(3, 3); WAIT_VM(14); post(S_LANDED, tb, 3); SVC(7);
        await4(S_FREED, 1, peek4(S_FREED)); group(4, 0); group(4, 1); group(4, 2); group(4, 3); WAIT_VM(14); post(S_LANDED, tb, 4); SVC(8);
        await4(S_FREED, 2, peek4(S_FREED)); group(5, 0); group(5, 1); group(5, 2); group(5, 3); WAIT_VM(14); post(S_LANDED, tb, 5); SVC(9);
        await4(S_FREED, 3, peek4(S_FREED)); group(6, 0); group(6, 1); group(6, 2); group(6, 3); WAIT_VM(14); post(S_LANDED, tb, 6);
        await4(S_FREED, 4, peek4(S_FREED)); group(7, 0); group(7, 1); group(7, 2); group(7, 3); WAIT_VM(14); post(S_LANDED, tb, 7);
        WAIT_VM(0); post(S_LANDED, tb, 8);
        SVC(10);
        STAMP(4);
        await(S_KF, 2);
        u32x4 kv[4];
#pragma unroll
        for (int j = 0; j < 16; ++j) {
            const u16x2 e = {LD(unsigned short, 131072 + (tap[j] & 0xffffu)), LD(unsigned short, 131072 + (tap[j] >> 16))};
            kv[j >> 2][j & 3] = __builtin_bit_cast(u32, e);
        }
        const u32 kb = L_KB + tb * KB_CH + (lane >> 2) * 80 + (lane & 3) * 16;
        ST(u32x4, kb) = (u32x4){0, 0, 0, 0}; ST(u32x4, kb + 1280) = (u32x4){0, 0, 0, 0};
#pragma unroll
        for (int q = 0; q < 4; ++q) ST(u32x4, kb + 2560 + q * 1280) = kv[q];
        asm volatile("s_load_dwordx2 %0, %1, 0x0\n\ts_waitcnt lgkmcnt(0)" : "=&s"(nonce) : "s"(ws + WS_NONCE));
        arrive(S_SVC, lane);
        STAMP(2);
    } else {
        const int ch = tb & 1, col = lane & 15, g = lane >> 4;
        u32 are, aim, bre, bim, cre, cim, dtw, dsw;
        asm volatile("global_load_dword %0, %6, %7\n\tglobal_load_dword %1, %6, %8\n\tglobal_load_dword %2, %6, %9\n\t"
                     "global_load_dword %3, %6, %10\n\tglobal_load_dword %4, %6, %11\n\tglobal_load_dword %5, %6, %12"
                     : "=&v"(are), "=&v"(aim), "=&v"(bre), "=&v"(bim), "=&v"(cre), "=&v"(cim)
                     : "v"((d0 + ch) * 256 + lane * 4), "s"(p.a_re), "s"(p.a_im), "s"(p.b_re), "s"(p.b_im), "s"(p.c_re), "s"(p.c_im));
        asm volatile("s_load_dword %0, %3, 0x0\n\ts_load_dword %1, %4, 0x0\n\ts_load_dwordx2 %2, %5, 0x0\n\ts_waitcnt lgkmcnt(0)"
                     : "=&s"(dtw), "=&s"(dsw), "=&s"(nonce) : "s"(p.log_dt + d0 * 2), "s"(p.dskip + d0 * 2), "s"(ws + WS_NONCE));
        float dt = (float)(__bf16)expf(ch ? bfhi(dtw) : bflo(dtw));
        const u32 t0 = L_TAB + ch * TAB_CH + lane * 4, ta = L_TAB + ch * TAB_CH + col * TAB_ROW + g * 16;
        f32x4 kq[4] = {};
        asm volatile("s_waitcnt vmcnt(0)" : "+v"(are), "+v"(aim), "+v"(bre), "+v"(bim), "+v"(cre), "+v"(cim), "+v"(pf), "+v"(dt));
        STAMP(3);
        __builtin_amdgcn_s_setprio(SSM_PRIO);
#pragma unroll 1
        for (int half = 0; half < 2; ++half) {
#define PUT(row, v) ST(u32, t0 + (row) * TAB_ROW) = pk2((v).x, (v).y)
            const float zr2 = (dt * bflo(are)) * LOG2E, th = (dt * bflo(aim)) * 0.15915494309189535f;
            const f32x2 e1 = cexp_n(zr2, -th, 1.f), gg = cexp_n(zr2, -th, 256.f);
            f32x2 p0 = {1.f, 0.f}, p1 = cexp_n(zr2, th, 8.f), a[8], e2, e16, g2, b0, b2, b4, b6;
            cmul2(a[0], (f32x2){bflo(cre), -bflo(cim)}, (f32x2){dt * bflo(bre), -dt * bflo(bim)}, e2, e1, e1);
            cmul2(e16, p1, p1, g2, gg, gg);
            a[1] = cmul(a[0], e1);
#pragma unroll
            for (int i = 2; i < 8; i += 2) cmul2(a[i], a[i - 2], e2, a[i + 1], a[i - 1], e2);
#pragma unroll
            for (int i = 0; i < 8; ++i) PUT(i, a[i]);
            cmul2(b2, a[2], gg, b6, a[6], gg); cmul2(b0, a[0], gg, b4, a[4], gg);
            PUT(8, b0); PUT(9, b2); PUT(10, b4); PUT(11, b6);
            cmul2(b0, b0, gg, b4, b4, gg); PUT(12, b0); PUT(13, b4);
            cmul2(b0, b0, gg, b4, b4, gg); PUT(14, b0); PUT(15, b4);
            cmul2(b4, b0, gg, b0, b0, g2); PUT(16, b4); PUT(17, b0);
            cmul2(b4, b4, g2, b0, b0, g2); PUT(18, b4); PUT(19, b0);
            b4 = cmul(b4, g2); PUT(20, b4);
#pragma unroll
            for (int i = 0; i < 32; i += 2) { PUT(21 + i, p0); PUT(22 + i, p1); cmul2(p0, p0, e16, p1, p1, e16); }
#undef PUT
            LGKM0();
            if (half == 0) STAMP(11);
            u32x4 ra[2][4], rb[2][4];
#pragma unroll
            for (int ks = 0; ks < 4; ++ks)
#pragma unroll
                for (int i = 0; i < 2; ++i) { ra[i][ks] = LD(u32x4, ta + i * 16 * TAB_ROW + ks * 64); rb[i][ks] = LD(u32x4, ta + TAB_A + i * 16 * TAB_ROW + ks * 64); }
            SB();
#pragma unroll
            for (int ks = 0; ks < 4; ++ks)
#pragma unroll
                for (int i = 0; i < 4; ++i) kq[i] = mfma16(ra[i >> 1][ks], rb[i & 1][ks], kq[i]);
            if (half == 0) { asm volatile("" : "+v"(kq[0]), "+v"(kq[1]), "+v"(kq[2]), "+v"(kq[3])); STAMP(12); }
            are >>= 16; aim >>= 16; bre >>= 16; bim >>= 16; cre >>= 16; cim >>= 16;
        }
        STAMP(14);
        if (lane == 0) kq[0][0] += ch ? bfhi(dsw) : bflo(dsw);
#pragma unroll
        for (int aq = 0; aq < 2; ++aq) {
            const int l0 = aq ? (g == 0 ? 1024 : g == 1 ? 2048 : 2052) : (g < 2 ? 4 * g : 256 * g - 256);
            const int s1 = aq ? (g == 0 ? 512 : 2) : (g < 2 ? 2 : 4 * g - 4), s2 = (!aq && g == 3) ? 512 : 2 * s1;
#pragma unroll
            for (int bq = 0; bq < 2; ++bq) {
                const f32x4 k = kq[aq * 2 + bq];
                const u32 e01 = pk2(k[0], k[1]), e23 = pk2(k[2], k[3]), ko = L_KF + ch * KF_CH + (l0 + 8 * col) * 2 + bq * 256;
                ST(unsigned short, ko) = (unsigned short)e01; ST(unsigned short, ko + s1) = (unsigned short)(e01 >> 16);
                ST(unsigned short, ko + s2) = (unsigned short)e23; ST(unsigned short, ko + s1 + s2) = (unsigned short)(e23 >> 16);
            }
        }
        arrive(S_KF, lane);
        STAMP(4);
        poll_tag(ws + WS_UFLAG + (d0 >> 5) * 4096 + lane * 64, true, (nonce + 1) & 0xffffffffffffull);
        u32x4 ud[4]; u32x2 ue[4];
#pragma unroll
        for (int q = 0; q < 4; ++q) {
            ud[q] = __builtin_amdgcn_raw_buffer_load_b128(wsr, WS_UT + (d0 + ch) * 4096 + q * 1024 + lane * 16, 0, SC1);
            ue[q] = __builtin_amdgcn_raw_buffer_load_b64(wsr, WS_UT + (d0 + ch) * 4096 + max(q * 1024 + lane * 16 - 8, 0), 0, SC1);
        }
        SB();
        STAMP(1);
        await4(S_FREED, 6, peek4(S_FREED)); LGKM0();
        if (lane < 16) ST(u32x4, L_UC + ch * UC_CH + (lane >> 2) * UC_COPY + (lane & 3) * 16) = (u32x4){0, 0, 0, 0};
#pragma unroll
        for (int q = 0; q < 4; ++q) {
            const u32 m2 = (q || lane) ? ue[q].x : 0u, m1 = (q || lane) ? ue[q].y : 0u, uo = L_UC + ch * UC_CH + 64 + q * 1024 + lane * 16;
            const u32 o0 = __builtin_amdgcn_alignbit(m1, m2, 16), o1 = __builtin_amdgcn_alignbit(ud[q].x, m1, 16), o2 = __builtin_amdgcn_alignbit(ud[q].y, ud[q].x, 16);
            const u32 o3 = __builtin_amdgcn_alignbit(ud[q].z, ud[q].y, 16), o4 = __builtin_amdgcn_alignbit(ud[q].w, ud[q].z, 16);
            ST(u32x4, uo) = ud[q];
            ST(u32x4, uo + UC_COPY) = (u32x4){o1, o2, o3, o4};
            ST(u32x4, uo + 2 * UC_COPY) = (u32x4){m1, ud[q].x, ud[q].y, ud[q].z};
            ST(u32x4, uo + 3 * UC_COPY) = (u32x4){o0, o1, o2, o3};
        }
        arrive(S_SVC, lane);
        STAMP(2);
    }
    await4(S_FREED, 8, peek4(S_FREED));
    await(S_SVC, 4);
    __builtin_amdgcn_s_setprio(2);
    STAMP(5);
    return nonce;
}

__device__ __forceinline__ void conv_wave(const char* wo, __amdgpu_buffer_rsrc_t wsr, char* ws, int lane, int tb, int d0, u64 tag STAMP_ARG) {
    const int ch = tb & 1, mh = tb >> 1, r = lane & 31, h = lane >> 5, c = (31 - r) & 3;
    u32 ua = L_UC + ch * UC_CH + c * UC_COPY + (mh * 512 + r + 1 + c + 8 * h) * 2;
    u32 ka = L_KB + ch * KB_CH + (30 + r - mh * 16) * 80 + h * 16;
    f32x16 y0, y1;
    u64 wsrc = (u64)(uintptr_t)(wo + tb * 131072);
    const u32 wv = (lane >> 3) * 1024 + (((lane & 7) ^ ((lane >> 2) & 4)) << 4);
    asm volatile(
        "ds_read_b64 v[32:33], v96 offset:0\nds_read_b64 v[34:35], v96 offset:8\nds_read_b128 v[36:39], v97 offset:160\n"
        "ds_read_b128 v[40:43], v97 offset:2720\nds_read_b64 v[44:45], v96 offset:2048\nds_read_b64 v[46:47], v96 offset:2056\n"
        "ds_read_b64 v[48:49], v96 offset:32\nds_read_b64 v[50:51], v96 offset:40\nds_read_b128 v[52:55], v97 offset:192\n"
        "ds_read_b128 v[56:59], v97 offset:2752\nds_read_b64 v[60:61], v96 offset:2080\nds_read_b64 v[62:63], v96 offset:2088\nv_mov_b32 v0, 0\n"
        "v_mov_b32 v1, 0\nv_mov_b32 v2, 0\nv_mov_b32 v3, 0\nv_mov_b32 v4, 0\nv_mov_b32 v5, 0\nv_mov_b32 v6, 0\nv_mov_b32 v7, 0\nv_mov_b32 v8, 0\n"
        "v_mov_b32 v9, 0\nv_mov_b32 v10, 0\nv_mov_b32 v11, 0\nv_mov_b32 v12, 0\nv_mov_b32 v13, 0\nv_mov_b32 v14, 0\nv_mov_b32 v15, 0\nv_mov_b32 v16, 0\n"
        "v_mov_b32 v17, 0\nv_mov_b32 v18, 0\nv_mov_b32 v19, 0\nv_mov_b32 v20, 0\nv_mov_b32 v21, 0\nv_mov_b32 v22, 0\nv_mov_b32 v23, 0\nv_mov_b32 v24, 0\n"
        "v_mov_b32 v25, 0\nv_mov_b32 v26, 0\nv_mov_b32 v27, 0\nv_mov_b32 v28, 0\nv_mov_b32 v29, 0\nv_mov_b32 v30, 0\nv_mov_b32 v31, 0\ns_mov_b32 m0, s45\n"
        "s_mov_b32 s44, 8\n.p2align 3\n1:\ns_waitcnt lgkmcnt(9)\nv_mfma_f32_32x32x16_bf16 v[0:15], v[64:67], v[68:71], v[0:15]\n"
        "ds_read_b64 v[64:65], v96 offset:64\nds_read_b64 v[66:67], v96 offset:72\ns_waitcnt lgkmcnt(10)\n"
        "v_mfma_f32_32x32x16_bf16 v[16:31], v[64:67], v[72:75], v[16:31]\nds_read_b128 v[68:71], v97 offset:80\nds_read_b128 v[72:75], v97 offset:2640\n"
        "global_load_lds_dwordx4 v98, s[46:47]\ns_waitcnt lgkmcnt(10)\nv_mfma_f32_32x32x16_bf16 v[16:31], v[76:79], v[68:71], v[16:31]\n"
        "ds_read_b64 v[76:77], v96 offset:2112\nds_read_b64 v[78:79], v96 offset:2120\ns_add_u32 m0, m0, 0x400\ns_add_u32 s46, s46, 0x2000\n"
        "s_addc_u32 s47, s47, 0\ns_waitcnt lgkmcnt(9)\nv_mfma_f32_32x32x16_bf16 v[0:15], v[80:83], v[84:87], v[0:15]\nds_read_b64 v[80:81], v96 offset:96\n"
        "ds_read_b64 v[82:83], v96 offset:104\ns_waitcnt lgkmcnt(10)\nv_mfma_f32_32x32x16_bf16 v[16:31], v[80:83], v[88:91], v[16:31]\n"
        "ds_read_b128 v[84:87], v97 offset:112\nds_read_b128 v[88:91], v97 offset:2672\ns_waitcnt lgkmcnt(10)\n"
        "v_mfma_f32_32x32x16_bf16 v[16:31], v[92:95], v[84:87], v[16:31]\nds_read_b64 v[92:93], v96 offset:2144\nds_read_b64 v[94:95], v96 offset:2152\n"
        "s_waitcnt lgkmcnt(9)\nv_mfma_f32_32x32x16_bf16 v[0:15], v[32:35], v[36:39], v[0:15]\nds_read_b64 v[32:33], v96 offset:128\n"
        "ds_read_b64 v[34:35], v96 offset:136\ns_waitcnt lgkmcnt(10)\nv_mfma_f32_32x32x16_bf16 v[16:31], v[32:35], v[40:43], v[16:31]\n"
        "ds_read_b128 v[36:39], v97 offset:0\nds_read_b128 v[40:43], v97 offset:2560\nglobal_load_lds_dwordx4 v98, s[46:47]\ns_waitcnt lgkmcnt(10)\n"
        "v_mfma_f32_32x32x16_bf16 v[16:31], v[44:47], v[36:39], v[16:31]\nds_read_b64 v[44:45], v96 offset:2176\nds_read_b64 v[46:47], v96 offset:2184\n"
        "s_add_u32 m0, m0, 0x400\ns_add_u32 s46, s46, 0x2000\ns_addc_u32 s47, s47, 0\ns_waitcnt lgkmcnt(9)\n"
        "v_mfma_f32_32x32x16_bf16 v[0:15], v[48:51], v[52:55], v[0:15]\nds_read_b64 v[48:49], v96 offset:160\nds_read_b64 v[50:51], v96 offset:168\n"
        "s_waitcnt lgkmcnt(10)\nv_mfma_f32_32x32x16_bf16 v[16:31], v[48:51], v[56:59], v[16:31]\nds_read_b128 v[52:55], v97 offset:32\n"
        "ds_read_b128 v[56:59], v97 offset:2592\ns_waitcnt lgkmcnt(10)\nv_mfma_f32_32x32x16_bf16 v[16:31], v[60:63], v[52:55], v[16:31]\n"
        "ds_read_b64 v[60:61], v96 offset:2208\nds_read_b64 v[62:63], v96 offset:2216\nv_add_u32 v96, 0x80, v96\nv_subrev_u32 v97, 0xa0, v97\n"
        "s_sub_u32 s44, s44, 1\ns_cmp_lg_u32 s44, 0\ns_cbranch_scc1 1b\ns_waitcnt lgkmcnt(0)\ns_nop 7\n"
        : "=&{v[0:15]}"(y0), "=&{v[16:31]}"(y1), "+{v96}"(ua), "+{v97}"(ka), "+{s[46:47]}"(wsrc) : "{v98}"(wv), "{s45}"(L_WO + tb * 16384)
        : "memory", "scc", "s44", "v32", "v33", "v34", "v35", "v36", "v37", "v38", "v39", "v40", "v41", "v42", "v43", "v44", "v45", "v46", "v47", "v48",
          "v49", "v50", "v51", "v52", "v53", "v54", "v55", "v56", "v57", "v58", "v59", "v60", "v61", "v62", "v63", "v64", "v65", "v66", "v67", "v68", "v69",
          "v70", "v71", "v72", "v73", "v74", "v75", "v76", "v77", "v78", "v79", "v80", "v81", "v82", "v83", "v84", "v85", "v86", "v87", "v88", "v89", "v90",
          "v91", "v92", "v93", "v94", "v95");
    STAMP(10);
    const f32x16 give = mh ? y0 : y1;
    f32x16 y = mh ? y1 : y0;
#pragma unroll
    for (int i = 0; i < 4; ++i) ST(f32x4, (tb < 3 ? L_CX + tb * 4096 : L_KF) + i * 1024 + lane * 16) = (f32x4){give[4 * i], give[4 * i + 1], give[4 * i + 2], give[4 * i + 3]};
    arrive(S_PAIR + ch * 4, lane); await(S_PAIR + ch * 4, 2);
#pragma unroll
    for (int i = 0; i < 4; ++i) {
        const f32x4 o = LD(f32x4, (tb != 1 ? L_CX + (tb ^ 2) * 4096 : L_KF) + i * 1024 + lane * 16);
#pragma unroll
        for (int j = 0; j < 4; ++j) y[4 * i + j] += o[j];
    }
    asm volatile("s_waitcnt lgkmcnt(0)" : "+v"(y) :: "memory");
    const u32 yo = WS_YT + ((d0 + ch) * 2048 + mh * 1024 + r * 32 + h * 8) * 2;
#pragma unroll
    for (int i = 0; i < 2; ++i)
        __builtin_amdgcn_raw_buffer_store_b128(widen(pk2(y[8 * i], y[8 * i + 1]), pk2(y[8 * i + 2], y[8 * i + 3]), pk2(y[8 * i + 4], y[8 * i + 5]), pk2(y[8 * i + 6], y[8 * i + 7])),
                                               wsr, yo + i * 32, 0, SC1);
}

__global__ __launch_bounds__(512, 2) void s4_mha_mix(Params p_arg) {
    const Params& p = p_arg;
    __shared__ __attribute__((aligned(16))) char smem[LDS_BYTES];
    asm volatile("" :: "s"((u32)(uintptr_t)smem));
    const int lane = threadIdx.x & 63, w = __builtin_amdgcn_readfirstlane(threadIdx.x >> 6);
    const int b = blockIdx.x, xg = b & 7, jb = b >> 3;
    const int hp = 8 * (xg & 1) + (jb & 7), qb = 4 * (xg >> 1) + (jb >> 3), d0 = 64 * xg + 2 * jb;
    const int tb = w & 3, a = w >> 2, r = lane & 31, h = lane >> 5;
    char* ws = p.ws;
    const __amdgpu_buffer_rsrc_t wsr = __builtin_amdgcn_make_buffer_rsrc(ws, 0, WS_END, 0x00020000);
    u32 pf = 0;
    {
        u64 pc0;
        asm volatile("s_getpc_b64 %0" : "=s"(pc0));
        if (w >= 6) asm volatile("global_load_dword %0, %1, %2" : "=&v"(pf) : "v"(min((((jb & 3) * 2 + (w & 1)) * 64 + lane) * 64, CODE_BYTES)), "s"(pc0));
    }
    if (threadIdx.x < 20) ST(u32, L_SYNC + threadIdx.x * 4) = (threadIdx.x & ~1u) == S_LANDED / 4 + 2 ? 8 : 0;
    asm volatile("" :: "s"(p.x), "s"(p.a_re), "s"(p.c_im), "s"(p.log_dt), "s"(p.wqkv), "s"(p.ws));
    BAR_LDS();
    const u64 nonce = a == 0 ? phase1_mfma(p, wsr, ws, lane, tb, hp, qb STAMP_PASS) : phase1_service(p, wsr, ws, lane, tb, hp, qb, d0, pf STAMP_PASS);
    const u64 tag = (nonce + 1) & 0xffffffffffffull;
    if (a == 1) { conv_wave(p.wo + (hp & 7) * 128, wsr, ws, lane, tb, d0, tag STAMP_PASS); STAMP(6); }

    {
        await(S_Q, 4);
        u32x4 q0 = LD(u32x4, L_QX + (a * 4 + tb) * 1024 + lane * 16), q1 = LD(u32x4, L_QX + (a * 4 + ((tb + 1) & 3)) * 1024 + lane * 16);
        u32x4 q2 = LD(u32x4, L_QX + (a * 4 + ((tb + 2) & 3)) * 1024 + lane * 16), q3 = LD(u32x4, L_QX + (a * 4 + ((tb + 3) & 3)) * 1024 + lane * 16);
        const u32x2 qn2 = LD(u32x2, L_QN + a * 512 + lane * 8);
        float qn = __uint_as_float(max(qn2.x, qn2.y));
        asm volatile("" : "+v"(qn), "+v"(q0), "+v"(q1), "+v"(q2), "+v"(q3));
        f32x16 o0, o1, o2, o3;
        u32x8 va, vb, vc, vd;
        const u32 one = (r & 27) == 16 ? 0x3f803f80u : 0u;
        asm volatile(
            "v_mov_b32 v116, v163\nv_mov_b32 v117, v163\nv_mov_b32 v118, v163\nv_mov_b32 v119, v163\nv_mov_b32 v120, v163\nv_mov_b32 v121, v163\n"
            "v_mov_b32 v122, v163\nv_mov_b32 v123, v163\nv_mov_b32 v128, v163\nv_mov_b32 v129, v163\nv_mov_b32 v130, v163\nv_mov_b32 v131, v163\n"
            "v_mov_b32 v132, v163\nv_mov_b32 v133, v163\nv_mov_b32 v134, v163\nv_mov_b32 v135, v163\nv_mov_b32 v140, v163\nv_mov_b32 v141, v163\n"
            "v_mov_b32 v142, v163\nv_mov_b32 v143, v163\nv_mov_b32 v144, v163\nv_mov_b32 v145, v163\nv_mov_b32 v146, v163\nv_mov_b32 v147, v163\n"
            "v_mov_b32 v152, v163\nv_mov_b32 v153, v163\nv_mov_b32 v154, v163\nv_mov_b32 v155, v163\nv_mov_b32 v156, v163\nv_mov_b32 v157, v163\n"
            "v_mov_b32 v158, v163\nv_mov_b32 v159, v163\n"
            : "=&{v[116:123]}"(va), "=&{v[128:135]}"(vb),
              "=&{v[140:147]}"(vc), "=&{v[152:159]}"(vd) : "{v163}"(one));
        float m0 = 0.f, m1 = 0.f, m2 = 0.f, m3 = 0.f;
        const u32 kvo = WS_KV + (hp * 2 + a) * 131072 + tb * 32768;
        const u64 fl = poll_tag(ws + WS_KVFLAG + (((hp >> 1) + (lane & 16 ? 8 : 0)) * 64 + tb * 16 + (lane & 15)) * 64 + (lane & 16 ? 0 : (hp & 1) * 16 + a * 8), lane < 32, tag);
        if (a == 1 && lane == 0) __hip_atomic_store((GAS u64*)(ws + WS_YFLAG + ((d0 + (tb & 1)) * 2 + (tb >> 1)) * 64), tag << 16, RLX_AGENT);
        arrive(S_READY, lane);
        STAMP(7);
        const bool safe = __all(qn * __uint_as_float(__builtin_amdgcn_readlane(row_max((u32)fl << 16), 15)) <= SAFE2);
        if (__builtin_expect(safe, 1)) {
            u64 base = (u64)(uintptr_t)(ws + kvo);
            asm volatile(
                "s_add_u32 s40, s40, 4096\ns_addc_u32 s41, s41, 0\nglobal_load_dwordx4 v[112:115], v161, s[40:41] offset:-4096 sc1\ns_mov_b64 exec, s[42:43]\n"
                "global_load_dwordx4 v[116:119], v162, s[40:41] offset:-3072 sc1\nglobal_load_dwordx4 v[120:123], v162, s[40:41] offset:-2560 sc1\ns_mov_b64 exec, -1\n"
                "global_load_dwordx4 v[124:127], v161, s[40:41] offset:-2048 sc1\ns_mov_b64 exec, s[42:43]\n"
                "global_load_dwordx4 v[128:131], v162, s[40:41] offset:-1024 sc1\nglobal_load_dwordx4 v[132:135], v162, s[40:41] offset:-512 sc1\ns_mov_b64 exec, -1\n"
                "global_load_dwordx4 v[136:139], v161, s[40:41] offset:0 sc1\ns_mov_b64 exec, s[42:43]\n"
                "global_load_dwordx4 v[140:143], v162, s[40:41] offset:1024 sc1\nglobal_load_dwordx4 v[144:147], v162, s[40:41] offset:1536 sc1\ns_mov_b64 exec, -1\n"
                "s_add_u32 s40, s40, 6144\ns_addc_u32 s41, s41, 0\ns_mov_b32 s44, 3\nv_mov_b32 v104, 0\nv_mov_b32 v105, 0\nv_mov_b32 v106, 0\nv_mov_b32 v107, 0\n"
                "v_mov_b32 v108, 0\nv_mov_b32 v109, 0\nv_mov_b32 v110, 0\nv_mov_b32 v111, 0\nv_mov_b32 v164, 0\nv_mov_b32 v165, 0\nv_mov_b32 v166, 0\n"
                "v_mov_b32 v167, 0\nv_mov_b32 v168, 0\nv_mov_b32 v169, 0\nv_mov_b32 v170, 0\nv_mov_b32 v171, 0\nv_mov_b32 v172, 0\nv_mov_b32 v173, 0\n"
                "v_mov_b32 v174, 0\nv_mov_b32 v175, 0\nv_mov_b32 v176, 0\nv_mov_b32 v177, 0\nv_mov_b32 v178, 0\nv_mov_b32 v179, 0\nv_mov_b32 v180, 0\n"
                "v_mov_b32 v181, 0\nv_mov_b32 v182, 0\nv_mov_b32 v183, 0\nv_mov_b32 v184, 0\nv_mov_b32 v185, 0\nv_mov_b32 v186, 0\nv_mov_b32 v187, 0\n"
                "v_mov_b32 v188, 0\nv_mov_b32 v189, 0\nv_mov_b32 v190, 0\nv_mov_b32 v191, 0\nv_mov_b32 v192, 0\nv_mov_b32 v193, 0\nv_mov_b32 v194, 0\n"
                "v_mov_b32 v195, 0\nv_mov_b32 v196, 0\nv_mov_b32 v197, 0\nv_mov_b32 v198, 0\nv_mov_b32 v199, 0\nv_mov_b32 v200, 0\nv_mov_b32 v201, 0\n"
                "v_mov_b32 v202, 0\nv_mov_b32 v203, 0\nv_mov_b32 v204, 0\nv_mov_b32 v205, 0\nv_mov_b32 v206, 0\nv_mov_b32 v207, 0\nv_mov_b32 v208, 0\n"
                "v_mov_b32 v209, 0\nv_mov_b32 v210, 0\nv_mov_b32 v211, 0\nv_mov_b32 v212, 0\nv_mov_b32 v213, 0\nv_mov_b32 v214, 0\nv_mov_b32 v215, 0\n"
                "v_mov_b32 v216, 0\nv_mov_b32 v217, 0\nv_mov_b32 v218, 0\nv_mov_b32 v219, 0\nv_mov_b32 v220, 0\nv_mov_b32 v221, 0\nv_mov_b32 v222, 0\n"
                "v_mov_b32 v223, 0\nv_mov_b32 v224, 0\nv_mov_b32 v225, 0\nv_mov_b32 v226, 0\nv_mov_b32 v227, 0\ns_waitcnt vmcnt(6)\n"
                "v_mfma_f32_32x32x16_bf16 v[80:95], v[112:115], v[64:67], 0\ns_setprio 0\ns_nop 10\n.p2align 3\n1:\nv_exp_f32 v80, v80\nv_exp_f32 v81, v81\n"
                "v_exp_f32 v82, v82\nv_exp_f32 v83, v83\nv_smfmac_f32_32x32x32_bf16 v[212:227], v[156:159], v[104:111], v160\n"
                "global_load_dwordx4 v[148:151], v161, s[40:41] offset:-4096 sc1\ns_mov_b64 exec, s[42:43]\n"
                "global_load_dwordx4 v[152:155], v162, s[40:41] offset:-3072 sc1\nglobal_load_dwordx4 v[156:159], v162, s[40:41] offset:-2560 sc1\ns_mov_b64 exec, -1\n"
                "v_exp_f32 v84, v84\nv_exp_f32 v85, v85\nv_exp_f32 v86, v86\nv_mfma_f32_32x32x16_bf16 v[96:111], v[112:115], v[68:71], 0\nv_exp_f32 v87, v87\n"
                "v_exp_f32 v88, v88\nv_exp_f32 v89, v89\nv_exp_f32 v90, v90\nv_exp_f32 v91, v91\nv_smfmac_f32_32x32x32_bf16 v[164:179], v[116:119], v[80:87], v160\n"
                "v_exp_f32 v92, v92\nv_exp_f32 v93, v93\nv_exp_f32 v94, v94\nv_exp_f32 v95, v95\nv_exp_f32 v96, v96\nv_exp_f32 v97, v97\nv_exp_f32 v98, v98\n"
                "v_exp_f32 v99, v99\nv_smfmac_f32_32x32x32_bf16 v[164:179], v[120:123], v[88:95], v160\nv_exp_f32 v100, v100\nv_exp_f32 v101, v101\n"
                "v_exp_f32 v102, v102\nv_mfma_f32_32x32x16_bf16 v[80:95], v[112:115], v[72:75], 0\nv_exp_f32 v103, v103\nv_exp_f32 v104, v104\nv_exp_f32 v105, v105\n"
                "v_exp_f32 v106, v106\nv_exp_f32 v107, v107\nv_smfmac_f32_32x32x32_bf16 v[180:195], v[116:119], v[96:103], v160\nv_exp_f32 v108, v108\n"
                "v_exp_f32 v109, v109\nv_exp_f32 v110, v110\nv_exp_f32 v111, v111\nv_exp_f32 v80, v80\nv_exp_f32 v81, v81\nv_exp_f32 v82, v82\nv_exp_f32 v83, v83\n"
                "v_smfmac_f32_32x32x32_bf16 v[180:195], v[120:123], v[104:111], v160\nv_exp_f32 v84, v84\nv_exp_f32 v85, v85\nv_exp_f32 v86, v86\n"
                "v_mfma_f32_32x32x16_bf16 v[96:111], v[112:115], v[76:79], 0\nv_exp_f32 v87, v87\nv_exp_f32 v88, v88\nv_exp_f32 v89, v89\nv_exp_f32 v90, v90\n"
                "v_exp_f32 v91, v91\nv_smfmac_f32_32x32x32_bf16 v[196:211], v[116:119], v[80:87], v160\nv_exp_f32 v92, v92\nv_exp_f32 v93, v93\nv_exp_f32 v94, v94\n"
                "v_exp_f32 v95, v95\nv_exp_f32 v96, v96\nv_exp_f32 v97, v97\nv_exp_f32 v98, v98\nv_exp_f32 v99, v99\n"
                "v_smfmac_f32_32x32x32_bf16 v[196:211], v[120:123], v[88:95], v160\nv_exp_f32 v100, v100\nv_exp_f32 v101, v101\nv_exp_f32 v102, v102\n"
                "s_waitcnt vmcnt(6)\nv_mfma_f32_32x32x16_bf16 v[80:95], v[124:127], v[64:67], 0\nv_exp_f32 v103, v103\nv_exp_f32 v104, v104\nv_exp_f32 v105, v105\n"
                "v_exp_f32 v106, v106\nv_exp_f32 v107, v107\nv_smfmac_f32_32x32x32_bf16 v[212:227], v[116:119], v[96:103], v160\nv_exp_f32 v108, v108\n"
                "v_exp_f32 v109, v109\nv_exp_f32 v110, v110\nv_exp_f32 v111, v111\nv_exp_f32 v80, v80\nv_exp_f32 v81, v81\nv_exp_f32 v82, v82\nv_exp_f32 v83, v83\n"
                "v_smfmac_f32_32x32x32_bf16 v[212:227], v[120:123], v[104:111], v160\nglobal_load_dwordx4 v[112:115], v161, s[40:41] offset:-2048 sc1\n"
                "s_mov_b64 exec, s[42:43]\nglobal_load_dwordx4 v[116:119], v162, s[40:41] offset:-1024 sc1\n"
                "global_load_dwordx4 v[120:123], v162, s[40:41] offset:-512 sc1\ns_mov_b64 exec, -1\nv_exp_f32 v84, v84\nv_exp_f32 v85, v85\nv_exp_f32 v86, v86\n"
                "v_mfma_f32_32x32x16_bf16 v[96:111], v[124:127], v[68:71], 0\nv_exp_f32 v87, v87\nv_exp_f32 v88, v88\nv_exp_f32 v89, v89\nv_exp_f32 v90, v90\n"
                "v_exp_f32 v91, v91\nv_smfmac_f32_32x32x32_bf16 v[164:179], v[128:131], v[80:87], v160\nv_exp_f32 v92, v92\nv_exp_f32 v93, v93\nv_exp_f32 v94, v94\n"
                "v_exp_f32 v95, v95\nv_exp_f32 v96, v96\nv_exp_f32 v97, v97\nv_exp_f32 v98, v98\nv_exp_f32 v99, v99\n"
                "v_smfmac_f32_32x32x32_bf16 v[164:179], v[132:135], v[88:95], v160\nv_exp_f32 v100, v100\nv_exp_f32 v101, v101\nv_exp_f32 v102, v102\n"
                "v_mfma_f32_32x32x16_bf16 v[80:95], v[124:127], v[72:75], 0\nv_exp_f32 v103, v103\nv_exp_f32 v104, v104\nv_exp_f32 v105, v105\nv_exp_f32 v106, v106\n"
                "v_exp_f32 v107, v107\nv_smfmac_f32_32x32x32_bf16 v[180:195], v[128:131], v[96:103], v160\nv_exp_f32 v108, v108\nv_exp_f32 v109, v109\n"
                "v_exp_f32 v110, v110\nv_exp_f32 v111, v111\nv_exp_f32 v80, v80\nv_exp_f32 v81, v81\nv_exp_f32 v82, v82\nv_exp_f32 v83, v83\n"
                "v_smfmac_f32_32x32x32_bf16 v[180:195], v[132:135], v[104:111], v160\nv_exp_f32 v84, v84\nv_exp_f32 v85, v85\nv_exp_f32 v86, v86\n"
                "v_mfma_f32_32x32x16_bf16 v[96:111], v[124:127], v[76:79], 0\nv_exp_f32 v87, v87\nv_exp_f32 v88, v88\nv_exp_f32 v89, v89\nv_exp_f32 v90, v90\n"
                "v_exp_f32 v91, v91\nv_smfmac_f32_32x32x32_bf16 v[196:211], v[128:131], v[80:87], v160\nv_exp_f32 v92, v92\nv_exp_f32 v93, v93\nv_exp_f32 v94, v94\n"
                "v_exp_f32 v95, v95\nv_exp_f32 v96, v96\nv_exp_f32 v97, v97\nv_exp_f32 v98, v98\nv_exp_f32 v99, v99\n"
                "v_smfmac_f32_32x32x32_bf16 v[196:211], v[132:135], v[88:95], v160\nv_exp_f32 v100, v100\nv_exp_f32 v101, v101\nv_exp_f32 v102, v102\n"
                "s_waitcnt vmcnt(6)\nv_mfma_f32_32x32x16_bf16 v[80:95], v[136:139], v[64:67], 0\nv_exp_f32 v103, v103\nv_exp_f32 v104, v104\nv_exp_f32 v105, v105\n"
                "v_exp_f32 v106, v106\nv_exp_f32 v107, v107\nv_smfmac_f32_32x32x32_bf16 v[212:227], v[128:131], v[96:103], v160\nv_exp_f32 v108, v108\n"
                "v_exp_f32 v109, v109\nv_exp_f32 v110, v110\nv_exp_f32 v111, v111\nv_exp_f32 v80, v80\nv_exp_f32 v81, v81\nv_exp_f32 v82, v82\nv_exp_f32 v83, v83\n"
                "v_smfmac_f32_32x32x32_bf16 v[212:227], v[132:135], v[104:111], v160\nglobal_load_dwordx4 v[124:127], v161, s[40:41] offset:0 sc1\n"
                "s_mov_b64 exec, s[42:43]\nglobal_load_dwordx4 v[128:131], v162, s[40:41] offset:1024 sc1\n"
                "global_load_dwordx4 v[132:135], v162, s[40:41] offset:1536 sc1\ns_mov_b64 exec, -1\nv_exp_f32 v84, v84\nv_exp_f32 v85, v85\nv_exp_f32 v86, v86\n"
                "v_mfma_f32_32x32x16_bf16 v[96:111], v[136:139], v[68:71], 0\nv_exp_f32 v87, v87\nv_exp_f32 v88, v88\nv_exp_f32 v89, v89\nv_exp_f32 v90, v90\n"
                "v_exp_f32 v91, v91\nv_smfmac_f32_32x32x32_bf16 v[164:179], v[140:143], v[80:87], v160\nv_exp_f32 v92, v92\nv_exp_f32 v93, v93\nv_exp_f32 v94, v94\n"
                "v_exp_f32 v95, v95\nv_exp_f32 v96, v96\nv_exp_f32 v97, v97\nv_exp_f32 v98, v98\nv_exp_f32 v99, v99\n"
                "v_smfmac_f32_32x32x32_bf16 v[164:179], v[144:147], v[88:95], v160\nv_exp_f32 v100, v100\nv_exp_f32 v101, v101\nv_exp_f32 v102, v102\n"
                "v_mfma_f32_32x32x16_bf16 v[80:95], v[136:139], v[72:75], 0\nv_exp_f32 v103, v103\nv_exp_f32 v104, v104\nv_exp_f32 v105, v105\nv_exp_f32 v106, v106\n"
                "v_exp_f32 v107, v107\nv_smfmac_f32_32x32x32_bf16 v[180:195], v[140:143], v[96:103], v160\nv_exp_f32 v108, v108\nv_exp_f32 v109, v109\n"
                "v_exp_f32 v110, v110\nv_exp_f32 v111, v111\nv_exp_f32 v80, v80\nv_exp_f32 v81, v81\nv_exp_f32 v82, v82\nv_exp_f32 v83, v83\n"
                "v_smfmac_f32_32x32x32_bf16 v[180:195], v[144:147], v[104:111], v160\nv_exp_f32 v84, v84\nv_exp_f32 v85, v85\nv_exp_f32 v86, v86\n"
                "v_mfma_f32_32x32x16_bf16 v[96:111], v[136:139], v[76:79], 0\nv_exp_f32 v87, v87\nv_exp_f32 v88, v88\nv_exp_f32 v89, v89\nv_exp_f32 v90, v90\n"
                "v_exp_f32 v91, v91\nv_smfmac_f32_32x32x32_bf16 v[196:211], v[140:143], v[80:87], v160\nv_exp_f32 v92, v92\nv_exp_f32 v93, v93\nv_exp_f32 v94, v94\n"
                "v_exp_f32 v95, v95\nv_exp_f32 v96, v96\nv_exp_f32 v97, v97\nv_exp_f32 v98, v98\nv_exp_f32 v99, v99\n"
                "v_smfmac_f32_32x32x32_bf16 v[196:211], v[144:147], v[88:95], v160\nv_exp_f32 v100, v100\nv_exp_f32 v101, v101\nv_exp_f32 v102, v102\n"
                "s_waitcnt vmcnt(6)\nv_mfma_f32_32x32x16_bf16 v[80:95], v[148:151], v[64:67], 0\nv_exp_f32 v103, v103\nv_exp_f32 v104, v104\nv_exp_f32 v105, v105\n"
                "v_exp_f32 v106, v106\nv_exp_f32 v107, v107\nv_smfmac_f32_32x32x32_bf16 v[212:227], v[140:143], v[96:103], v160\nv_exp_f32 v108, v108\n"
                "v_exp_f32 v109, v109\nv_exp_f32 v110, v110\nv_exp_f32 v111, v111\nv_exp_f32 v80, v80\nv_exp_f32 v81, v81\nv_exp_f32 v82, v82\nv_exp_f32 v83, v83\n"
                "v_smfmac_f32_32x32x32_bf16 v[212:227], v[144:147], v[104:111], v160\nglobal_load_dwordx4 v[136:139], v161, s[40:41] offset:2048 sc1\n"
                "s_mov_b64 exec, s[42:43]\nglobal_load_dwordx4 v[140:143], v162, s[40:41] offset:3072 sc1\n"
                "global_load_dwordx4 v[144:147], v162, s[40:41] offset:3584 sc1\ns_mov_b64 exec, -1\nv_exp_f32 v84, v84\nv_exp_f32 v85, v85\nv_exp_f32 v86, v86\n"
                "v_mfma_f32_32x32x16_bf16 v[96:111], v[148:151], v[68:71], 0\nv_exp_f32 v87, v87\nv_exp_f32 v88, v88\nv_exp_f32 v89, v89\nv_exp_f32 v90, v90\n"
                "v_exp_f32 v91, v91\nv_smfmac_f32_32x32x32_bf16 v[164:179], v[152:155], v[80:87], v160\nv_exp_f32 v92, v92\nv_exp_f32 v93, v93\nv_exp_f32 v94, v94\n"
                "v_exp_f32 v95, v95\nv_exp_f32 v96, v96\nv_exp_f32 v97, v97\nv_exp_f32 v98, v98\nv_exp_f32 v99, v99\n"
                "v_smfmac_f32_32x32x32_bf16 v[164:179], v[156:159], v[88:95], v160\nv_exp_f32 v100, v100\nv_exp_f32 v101, v101\nv_exp_f32 v102, v102\n"
                "v_mfma_f32_32x32x16_bf16 v[80:95], v[148:151], v[72:75], 0\nv_exp_f32 v103, v103\nv_exp_f32 v104, v104\nv_exp_f32 v105, v105\nv_exp_f32 v106, v106\n"
                "v_exp_f32 v107, v107\nv_smfmac_f32_32x32x32_bf16 v[180:195], v[152:155], v[96:103], v160\nv_exp_f32 v108, v108\nv_exp_f32 v109, v109\n"
                "v_exp_f32 v110, v110\nv_exp_f32 v111, v111\nv_exp_f32 v80, v80\nv_exp_f32 v81, v81\nv_exp_f32 v82, v82\nv_exp_f32 v83, v83\n"
                "v_smfmac_f32_32x32x32_bf16 v[180:195], v[156:159], v[104:111], v160\nv_exp_f32 v84, v84\nv_exp_f32 v85, v85\nv_exp_f32 v86, v86\n"
                "v_mfma_f32_32x32x16_bf16 v[96:111], v[148:151], v[76:79], 0\nv_exp_f32 v87, v87\nv_exp_f32 v88, v88\nv_exp_f32 v89, v89\nv_exp_f32 v90, v90\n"
                "v_exp_f32 v91, v91\nv_smfmac_f32_32x32x32_bf16 v[196:211], v[152:155], v[80:87], v160\nv_exp_f32 v92, v92\nv_exp_f32 v93, v93\nv_exp_f32 v94, v94\n"
                "v_exp_f32 v95, v95\nv_exp_f32 v96, v96\nv_exp_f32 v97, v97\nv_exp_f32 v98, v98\nv_exp_f32 v99, v99\n"
                "v_smfmac_f32_32x32x32_bf16 v[196:211], v[156:159], v[88:95], v160\nv_exp_f32 v100, v100\nv_exp_f32 v101, v101\nv_exp_f32 v102, v102\n"
                "s_waitcnt vmcnt(6)\nv_mfma_f32_32x32x16_bf16 v[80:95], v[112:115], v[64:67], 0\nv_exp_f32 v103, v103\nv_exp_f32 v104, v104\nv_exp_f32 v105, v105\n"
                "v_exp_f32 v106, v106\nv_exp_f32 v107, v107\nv_smfmac_f32_32x32x32_bf16 v[212:227], v[152:155], v[96:103], v160\nv_exp_f32 v108, v108\n"
                "v_exp_f32 v109, v109\nv_exp_f32 v110, v110\nv_exp_f32 v111, v111\ns_add_u32 s40, s40, 8192\ns_addc_u32 s41, s41, 0\ns_sub_u32 s44, s44, 1\n"
                "s_cmp_lg_u32 s44, 0\ns_cbranch_scc1 1b\nv_exp_f32 v80, v80\nv_exp_f32 v81, v81\nv_exp_f32 v82, v82\nv_exp_f32 v83, v83\n"
                "v_smfmac_f32_32x32x32_bf16 v[212:227], v[156:159], v[104:111], v160\nglobal_load_dwordx4 v[148:151], v161, s[40:41] offset:-4096 sc1\n"
                "s_mov_b64 exec, s[42:43]\nglobal_load_dwordx4 v[152:155], v162, s[40:41] offset:-3072 sc1\n"
                "global_load_dwordx4 v[156:159], v162, s[40:41] offset:-2560 sc1\ns_mov_b64 exec, -1\nv_exp_f32 v84, v84\nv_exp_f32 v85, v85\nv_exp_f32 v86, v86\n"
                "v_mfma_f32_32x32x16_bf16 v[96:111], v[112:115], v[68:71], 0\nv_exp_f32 v87, v87\nv_exp_f32 v88, v88\nv_exp_f32 v89, v89\nv_exp_f32 v90, v90\n"
                "v_exp_f32 v91, v91\nv_smfmac_f32_32x32x32_bf16 v[164:179], v[116:119], v[80:87], v160\nv_exp_f32 v92, v92\nv_exp_f32 v93, v93\nv_exp_f32 v94, v94\n"
                "v_exp_f32 v95, v95\nv_exp_f32 v96, v96\nv_exp_f32 v97, v97\nv_exp_f32 v98, v98\nv_exp_f32 v99, v99\n"
                "v_smfmac_f32_32x32x32_bf16 v[164:179], v[120:123], v[88:95], v160\nv_exp_f32 v100, v100\nv_exp_f32 v101, v101\nv_exp_f32 v102, v102\n"
                "v_mfma_f32_32x32x16_bf16 v[80:95], v[112:115], v[72:75], 0\nv_exp_f32 v103, v103\nv_exp_f32 v104, v104\nv_exp_f32 v105, v105\nv_exp_f32 v106, v106\n"
                "v_exp_f32 v107, v107\nv_smfmac_f32_32x32x32_bf16 v[180:195], v[116:119], v[96:103], v160\nv_exp_f32 v108, v108\nv_exp_f32 v109, v109\n"
                "v_exp_f32 v110, v110\nv_exp_f32 v111, v111\nv_exp_f32 v80, v80\nv_exp_f32 v81, v81\nv_exp_f32 v82, v82\nv_exp_f32 v83, v83\n"
                "v_smfmac_f32_32x32x32_bf16 v[180:195], v[120:123], v[104:111], v160\nv_exp_f32 v84, v84\nv_exp_f32 v85, v85\nv_exp_f32 v86, v86\n"
                "v_mfma_f32_32x32x16_bf16 v[96:111], v[112:115], v[76:79], 0\nv_exp_f32 v87, v87\nv_exp_f32 v88, v88\nv_exp_f32 v89, v89\nv_exp_f32 v90, v90\n"
                "v_exp_f32 v91, v91\nv_smfmac_f32_32x32x32_bf16 v[196:211], v[116:119], v[80:87], v160\nv_exp_f32 v92, v92\nv_exp_f32 v93, v93\nv_exp_f32 v94, v94\n"
                "v_exp_f32 v95, v95\nv_exp_f32 v96, v96\nv_exp_f32 v97, v97\nv_exp_f32 v98, v98\nv_exp_f32 v99, v99\n"
                "v_smfmac_f32_32x32x32_bf16 v[196:211], v[120:123], v[88:95], v160\nv_exp_f32 v100, v100\nv_exp_f32 v101, v101\nv_exp_f32 v102, v102\n"
                "s_waitcnt vmcnt(6)\nv_mfma_f32_32x32x16_bf16 v[80:95], v[124:127], v[64:67], 0\nv_exp_f32 v103, v103\nv_exp_f32 v104, v104\nv_exp_f32 v105, v105\n"
                "v_exp_f32 v106, v106\nv_exp_f32 v107, v107\nv_smfmac_f32_32x32x32_bf16 v[212:227], v[116:119], v[96:103], v160\nv_exp_f32 v108, v108\n"
                "v_exp_f32 v109, v109\nv_exp_f32 v110, v110\nv_exp_f32 v111, v111\nv_exp_f32 v80, v80\nv_exp_f32 v81, v81\nv_exp_f32 v82, v82\nv_exp_f32 v83, v83\n"
                "v_smfmac_f32_32x32x32_bf16 v[212:227], v[120:123], v[104:111], v160\nv_exp_f32 v84, v84\nv_exp_f32 v85, v85\nv_exp_f32 v86, v86\n"
                "v_mfma_f32_32x32x16_bf16 v[96:111], v[124:127], v[68:71], 0\nv_exp_f32 v87, v87\nv_exp_f32 v88, v88\nv_exp_f32 v89, v89\nv_exp_f32 v90, v90\n"
                "v_exp_f32 v91, v91\nv_smfmac_f32_32x32x32_bf16 v[164:179], v[128:131], v[80:87], v160\nv_exp_f32 v92, v92\nv_exp_f32 v93, v93\nv_exp_f32 v94, v94\n"
                "v_exp_f32 v95, v95\nv_exp_f32 v96, v96\nv_exp_f32 v97, v97\nv_exp_f32 v98, v98\nv_exp_f32 v99, v99\n"
                "v_smfmac_f32_32x32x32_bf16 v[164:179], v[132:135], v[88:95], v160\nv_exp_f32 v100, v100\nv_exp_f32 v101, v101\nv_exp_f32 v102, v102\n"
                "v_mfma_f32_32x32x16_bf16 v[80:95], v[124:127], v[72:75], 0\nv_exp_f32 v103, v103\nv_exp_f32 v104, v104\nv_exp_f32 v105, v105\nv_exp_f32 v106, v106\n"
                "v_exp_f32 v107, v107\nv_smfmac_f32_32x32x32_bf16 v[180:195], v[128:131], v[96:103], v160\nv_exp_f32 v108, v108\nv_exp_f32 v109, v109\n"
                "v_exp_f32 v110, v110\nv_exp_f32 v111, v111\nv_exp_f32 v80, v80\nv_exp_f32 v81, v81\nv_exp_f32 v82, v82\nv_exp_f32 v83, v83\n"
                "v_smfmac_f32_32x32x32_bf16 v[180:195], v[132:135], v[104:111], v160\nv_exp_f32 v84, v84\nv_exp_f32 v85, v85\nv_exp_f32 v86, v86\n"
                "v_mfma_f32_32x32x16_bf16 v[96:111], v[124:127], v[76:79], 0\nv_exp_f32 v87, v87\nv_exp_f32 v88, v88\nv_exp_f32 v89, v89\nv_exp_f32 v90, v90\n"
                "v_exp_f32 v91, v91\nv_smfmac_f32_32x32x32_bf16 v[196:211], v[128:131], v[80:87], v160\nv_exp_f32 v92, v92\nv_exp_f32 v93, v93\nv_exp_f32 v94, v94\n"
                "v_exp_f32 v95, v95\nv_exp_f32 v96, v96\nv_exp_f32 v97, v97\nv_exp_f32 v98, v98\nv_exp_f32 v99, v99\n"
                "v_smfmac_f32_32x32x32_bf16 v[196:211], v[132:135], v[88:95], v160\nv_exp_f32 v100, v100\nv_exp_f32 v101, v101\nv_exp_f32 v102, v102\n"
                "s_waitcnt vmcnt(3)\nv_mfma_f32_32x32x16_bf16 v[80:95], v[136:139], v[64:67], 0\nv_exp_f32 v103, v103\nv_exp_f32 v104, v104\nv_exp_f32 v105, v105\n"
                "v_exp_f32 v106, v106\nv_exp_f32 v107, v107\nv_smfmac_f32_32x32x32_bf16 v[212:227], v[128:131], v[96:103], v160\nv_exp_f32 v108, v108\n"
                "v_exp_f32 v109, v109\nv_exp_f32 v110, v110\nv_exp_f32 v111, v111\nv_exp_f32 v80, v80\nv_exp_f32 v81, v81\nv_exp_f32 v82, v82\nv_exp_f32 v83, v83\n"
                "v_smfmac_f32_32x32x32_bf16 v[212:227], v[132:135], v[104:111], v160\nv_exp_f32 v84, v84\nv_exp_f32 v85, v85\nv_exp_f32 v86, v86\n"
                "v_mfma_f32_32x32x16_bf16 v[96:111], v[136:139], v[68:71], 0\nv_exp_f32 v87, v87\nv_exp_f32 v88, v88\nv_exp_f32 v89, v89\nv_exp_f32 v90, v90\n"
                "v_exp_f32 v91, v91\nv_smfmac_f32_32x32x32_bf16 v[164:179], v[140:143], v[80:87], v160\nv_exp_f32 v92, v92\nv_exp_f32 v93, v93\nv_exp_f32 v94, v94\n"
                "v_exp_f32 v95, v95\nv_exp_f32 v96, v96\nv_exp_f32 v97, v97\nv_exp_f32 v98, v98\nv_exp_f32 v99, v99\n"
                "v_smfmac_f32_32x32x32_bf16 v[164:179], v[144:147], v[88:95], v160\nv_exp_f32 v100, v100\nv_exp_f32 v101, v101\nv_exp_f32 v102, v102\n"
                "v_mfma_f32_32x32x16_bf16 v[80:95], v[136:139], v[72:75], 0\nv_exp_f32 v103, v103\nv_exp_f32 v104, v104\nv_exp_f32 v105, v105\nv_exp_f32 v106, v106\n"
                "v_exp_f32 v107, v107\nv_smfmac_f32_32x32x32_bf16 v[180:195], v[140:143], v[96:103], v160\nv_exp_f32 v108, v108\nv_exp_f32 v109, v109\n"
                "v_exp_f32 v110, v110\nv_exp_f32 v111, v111\nv_exp_f32 v80, v80\nv_exp_f32 v81, v81\nv_exp_f32 v82, v82\nv_exp_f32 v83, v83\n"
                "v_smfmac_f32_32x32x32_bf16 v[180:195], v[144:147], v[104:111], v160\nv_exp_f32 v84, v84\nv_exp_f32 v85, v85\nv_exp_f32 v86, v86\n"
                "v_mfma_f32_32x32x16_bf16 v[96:111], v[136:139], v[76:79], 0\nv_exp_f32 v87, v87\nv_exp_f32 v88, v88\nv_exp_f32 v89, v89\nv_exp_f32 v90, v90\n"
                "v_exp_f32 v91, v91\nv_smfmac_f32_32x32x32_bf16 v[196:211], v[140:143], v[80:87], v160\nv_exp_f32 v92, v92\nv_exp_f32 v93, v93\nv_exp_f32 v94, v94\n"
                "v_exp_f32 v95, v95\nv_exp_f32 v96, v96\nv_exp_f32 v97, v97\nv_exp_f32 v98, v98\nv_exp_f32 v99, v99\n"
                "v_smfmac_f32_32x32x32_bf16 v[196:211], v[144:147], v[88:95], v160\nv_exp_f32 v100, v100\nv_exp_f32 v101, v101\nv_exp_f32 v102, v102\n"
                "s_waitcnt vmcnt(0)\nv_mfma_f32_32x32x16_bf16 v[80:95], v[148:151], v[64:67], 0\nv_exp_f32 v103, v103\nv_exp_f32 v104, v104\nv_exp_f32 v105, v105\n"
                "v_exp_f32 v106, v106\nv_exp_f32 v107, v107\nv_smfmac_f32_32x32x32_bf16 v[212:227], v[140:143], v[96:103], v160\nv_exp_f32 v108, v108\n"
                "v_exp_f32 v109, v109\nv_exp_f32 v110, v110\nv_exp_f32 v111, v111\nv_exp_f32 v80, v80\nv_exp_f32 v81, v81\nv_exp_f32 v82, v82\nv_exp_f32 v83, v83\n"
                "v_smfmac_f32_32x32x32_bf16 v[212:227], v[144:147], v[104:111], v160\nv_exp_f32 v84, v84\nv_exp_f32 v85, v85\nv_exp_f32 v86, v86\n"
                "v_mfma_f32_32x32x16_bf16 v[96:111], v[148:151], v[68:71], 0\nv_exp_f32 v87, v87\nv_exp_f32 v88, v88\nv_exp_f32 v89, v89\nv_exp_f32 v90, v90\n"
                "v_exp_f32 v91, v91\nv_smfmac_f32_32x32x32_bf16 v[164:179], v[152:155], v[80:87], v160\nv_exp_f32 v92, v92\nv_exp_f32 v93, v93\nv_exp_f32 v94, v94\n"
                "v_exp_f32 v95, v95\nv_exp_f32 v96, v96\nv_exp_f32 v97, v97\nv_exp_f32 v98, v98\nv_exp_f32 v99, v99\n"
                "v_smfmac_f32_32x32x32_bf16 v[164:179], v[156:159], v[88:95], v160\nv_exp_f32 v100, v100\nv_exp_f32 v101, v101\nv_exp_f32 v102, v102\n"
                "v_mfma_f32_32x32x16_bf16 v[80:95], v[148:151], v[72:75], 0\nv_exp_f32 v103, v103\nv_exp_f32 v104, v104\nv_exp_f32 v105, v105\nv_exp_f32 v106, v106\n"
                "v_exp_f32 v107, v107\nv_smfmac_f32_32x32x32_bf16 v[180:195], v[152:155], v[96:103], v160\nv_exp_f32 v108, v108\nv_exp_f32 v109, v109\n"
                "v_exp_f32 v110, v110\nv_exp_f32 v111, v111\nv_exp_f32 v80, v80\nv_exp_f32 v81, v81\nv_exp_f32 v82, v82\nv_exp_f32 v83, v83\n"
                "v_smfmac_f32_32x32x32_bf16 v[180:195], v[156:159], v[104:111], v160\nv_exp_f32 v84, v84\nv_exp_f32 v85, v85\nv_exp_f32 v86, v86\n"
                "v_mfma_f32_32x32x16_bf16 v[96:111], v[148:151], v[76:79], 0\nv_exp_f32 v87, v87\nv_exp_f32 v88, v88\nv_exp_f32 v89, v89\nv_exp_f32 v90, v90\n"
                "v_exp_f32 v91, v91\nv_smfmac_f32_32x32x32_bf16 v[196:211], v[152:155], v[80:87], v160\nv_exp_f32 v92, v92\nv_exp_f32 v93, v93\nv_exp_f32 v94, v94\n"
                "v_exp_f32 v95, v95\nv_exp_f32 v96, v96\nv_exp_f32 v97, v97\nv_exp_f32 v98, v98\nv_exp_f32 v99, v99\n"
                "v_smfmac_f32_32x32x32_bf16 v[196:211], v[156:159], v[88:95], v160\nv_exp_f32 v100, v100\nv_exp_f32 v101, v101\nv_exp_f32 v102, v102\n"
                "v_exp_f32 v103, v103\nv_exp_f32 v104, v104\nv_exp_f32 v105, v105\nv_exp_f32 v106, v106\nv_exp_f32 v107, v107\n"
                "v_smfmac_f32_32x32x32_bf16 v[212:227], v[152:155], v[96:103], v160\nv_exp_f32 v108, v108\nv_exp_f32 v109, v109\nv_exp_f32 v110, v110\n"
                "v_exp_f32 v111, v111\ns_nop 1\nv_smfmac_f32_32x32x32_bf16 v[212:227], v[156:159], v[104:111], v160\ns_nop 11\n"
                : "=&{v[164:179]}"(o0), "=&{v[180:195]}"(o1), "=&{v[196:211]}"(o2), "=&{v[212:227]}"(o3), "+{v[116:123]}"(va), "+{v[128:135]}"(vb),
                  "+{v[140:147]}"(vc), "+{v[152:159]}"(vd), "+{s[40:41]}"(base)
                : "{v[64:67]}"(q0), "{v[68:71]}"(q1), "{v[72:75]}"(q2), "{v[76:79]}"(q3), "{v160}"(0xddddu), "{v161}"(lane * 16),
                  "{v162}"(h * 256 + (r & 15) * 16), "{s[42:43]}"(0x0000ffff0000ffffull)
                : "memory", "scc", "s44", "v80", "v81", "v82", "v83", "v84", "v85", "v86", "v87", "v88", "v89", "v90", "v91", "v92", "v93", "v94", "v95",
                  "v96", "v97", "v98", "v99", "v100", "v101", "v102", "v103", "v104", "v105", "v106", "v107", "v108", "v109", "v110", "v111", "v112", "v113",
                  "v114", "v115", "v124", "v125", "v126", "v127", "v136", "v137", "v138", "v139", "v148", "v149", "v150", "v151");
        } else {
            m0 = m1 = m2 = m3 = -INFINITY;
            o0 = o1 = o2 = o3 = (f32x16){};
            const u32x4 ones = {0x3f803f80u, 0x3f803f80u, 0x3f803f80u, 0x3f803f80u};
#pragma unroll 1
            for (int t = 0; t < 16; ++t) {
                const u32x4 kf = __builtin_amdgcn_raw_buffer_load_b128(wsr, kvo + t * 2048 + lane * 16, 0, SC1);
                u32x4 v0 = __builtin_amdgcn_raw_buffer_load_b128(wsr, kvo + t * 2048 + 1024 + h * 256 + (r & 15) * 16, 0, SC1);
                u32x4 v1 = __builtin_amdgcn_raw_buffer_load_b128(wsr, kvo + t * 2048 + 1536 + h * 256 + (r & 15) * 16, 0, SC1);
                if (r >= 16) { v0 = ones; v1 = ones; }
                auto step = [&](const u32x4& q, f32x16& o, float& m) {
                    f32x16 s = mfma32(kf, q, (f32x16){});
                    float mx = s[0];
#pragma unroll
                    for (int i = 1; i < 16; ++i) mx = fmaxf(mx, s[i]);
                    const float mn = fmaxf(m, halves_max(mx)), al = __builtin_amdgcn_exp2f(m - mn);
                    m = mn;
#pragma unroll
                    for (int i = 0; i < 16; ++i) { s[i] = __builtin_amdgcn_exp2f(s[i] - mn); o[i] *= al; }
                    const f32x8 lo = __builtin_shufflevector(s, s, 0, 1, 2, 3, 4, 5, 6, 7), hi = __builtin_shufflevector(s, s, 8, 9, 10, 11, 12, 13, 14, 15);
                    o = __builtin_amdgcn_smfmac_f32_32x32x32_bf16(__builtin_bit_cast(bf16x8, v0), __builtin_bit_cast(bf16x16, lo), o, 0xdddd, 0, 0);
                    o = __builtin_amdgcn_smfmac_f32_32x32x32_bf16(__builtin_bit_cast(bf16x8, v1), __builtin_bit_cast(bf16x16, hi), o, 0xdddd, 0, 0);
                };
                step(q0, o0, m0); step(q1, o1, m1); step(q2, o2, m2); step(q3, o3, m3);
            }
        }
        STAMP(8);
        await(S_READY, 8);
        auto put = [&](int i, const f32x16& o, float m) {
            const u32 rec = a * L_TAB + (((tb + i) & 3) * 3 + i - 1) * 2560;
            ST(f32x4, rec + lane * 16) = (f32x4){o[0], o[1], o[2], o[3]};
            ST(f32x4, rec + 1024 + lane * 16) = (f32x4){o[4], o[5], o[6], o[7]};
            ST(f32x2, rec + 2048 + lane * 8) = (f32x2){o[8], m};
        };
        put(1, o1, m1); put(2, o2, m2); put(3, o3, m3);
        arrive(S_PART + a * 4, lane); await(S_PART + a * 4, 4);
        {
            f32x4 pa[3], pb[3]; f32x2 lm[3];
#pragma unroll
            for (int i = 0; i < 3; ++i) {
                const u32 rec = a * L_TAB + (tb * 3 + i) * 2560;
                pa[i] = LD(f32x4, rec + lane * 16); pb[i] = LD(f32x4, rec + 1024 + lane * 16); lm[i] = LD(f32x2, rec + 2048 + lane * 8);
            }
            const float mm = fmaxf(fmaxf(m0, lm[0].y), fmaxf(lm[1].y, lm[2].y));
            const float s0 = __builtin_amdgcn_exp2f(m0 - mm);
            float l = o0[8] * s0, acc[8];
#pragma unroll
            for (int j = 0; j < 8; ++j) acc[j] = o0[j] * s0;
#pragma unroll
            for (int i = 0; i < 3; ++i) {
                const float s = __builtin_amdgcn_exp2f(lm[i].y - mm);
                l = fmaf(lm[i].x, s, l);
#pragma unroll
                for (int j = 0; j < 4; ++j) { acc[j] = fmaf(pa[i][j], s, acc[j]); acc[4 + j] = fmaf(pb[i][j], s, acc[4 + j]); }
            }
            const float rl = __builtin_amdgcn_rcpf(l);
            const u32x4 of = {pk2(acc[0] * rl, acc[1] * rl), pk2(acc[2] * rl, acc[3] * rl), pk2(acc[4] * rl, acc[5] * rl), pk2(acc[6] * rl, acc[7] * rl)};
            __builtin_amdgcn_raw_buffer_store_b128(of, wsr, WS_OF + ((qb * 4 + tb) * 32 + hp * 2 + a) * 1024 + lane * 16, 0, SC1);
        }
        asm volatile("s_waitcnt vmcnt(0)" ::: "memory");
        if (lane == 0) __hip_atomic_store((GAS u64*)(ws + WS_OFLAG + ((qb * 4 + tb) * 32 + hp * 2 + a) * 64), tag << 16, RLX_AGENT);
        STAMP(9);
    }

    if (a == 0) {
        const int rb = qb * 4 + (hp >> 3) * 2 + (tb & 1), cb = (hp & 7) * 2 + (tb >> 1), g1 = (lane >> 4) & 1, q4 = (lane >> 2) & 3;
        const u32 wa = L_WO + (4 * h + q4) * 128 + ((((tb >> 1) ^ (q4 >> 1)) & 1) << 6) + (g1 << 5) + (lane & 3) * 8;
        f32x16 acc = {};
        u32x4 yf[2];
        auto half = [&](int odd) {
            u32x4 wf[16], of[16];
#pragma unroll
            for (int i = 0; i < 16; ++i) wf[i] = trd2(wa + (2 * i + odd) * 2048, wa + (2 * i + odd) * 2048 + 1024);
            poll_tag(ws + WS_OFLAG + (rb * 32 + (lane & 15) * 2 + odd) * 64, lane < 16, tag);
            STAMP(10);
#pragma unroll
            for (int i = 0; i < 16; ++i) of[i] = __builtin_amdgcn_raw_buffer_load_b128(wsr, WS_OF + (rb * 32 + odd) * 1024 + lane * 16, i * 2048, SC1);
            __builtin_amdgcn_sched_barrier(0);
#pragma unroll
            for (int i = 0; i < 16; ++i) acc = mfma32(wf[i], of[i], acc);
        };
        half(0);
        {
            poll_tag(ws + WS_YFLAG + ((cb * 32 + r) * 2 + (rb >> 5)) * 64, lane < 32, tag);
#pragma unroll
            for (int i = 0; i < 2; ++i)
                ST(u32x4, L_YT + tb * 2048 + i * 1024 + lane * 16) =
                    __builtin_amdgcn_raw_buffer_load_b128(wsr, WS_YT + ((cb * 32 + i * 16 + (lane >> 2)) * 2048 + rb * 32) * 2 + (lane & 3) * 16, 0, SC1);
            LGKM0();
            const u32 ya = L_YT + tb * 2048 + (4 * h + q4) * 64 + g1 * 32 + (lane & 3) * 8;
            yf[0] = trd2(ya, ya + 512); yf[1] = trd2(ya + 1024, ya + 1536);
        }
        const float g = 1.f / (1.f + __expf(-LD(float, L_G + ((hp >> 3) * 64 + (tb & 1) * 32 + r) * 4)));
        half(1);
        STAMP(11);
        if (b == 0 && threadIdx.x == 0) *(GAS u64*)(ws + WS_NONCE) = nonce + 1;
        const __amdgpu_buffer_rsrc_t outr = __builtin_amdgcn_make_buffer_rsrc(p.out, 0, 2048 * 512 * 2, 0x00020000);
#pragma unroll
        for (int i = 0; i < 2; ++i) {
            u32 pk[4];
#pragma unroll
            for (int j = 0; j < 4; ++j) {
                const u32 y2 = yf[i][j]; const float b0 = acc[8 * i + 2 * j], b1 = acc[8 * i + 2 * j + 1];
                pk[j] = pk2(fmaf(g, bflo(y2) - b0, b0), fmaf(g, bfhi(y2) - b1, b1));
            }
            __builtin_amdgcn_raw_buffer_store_b128(widen(pk[0], pk[1], pk[2], pk[3]), outr, ((rb * 32 + r) * 512 + cb * 32 + i * 16 + h * 8) * 2, 0, 0);
        }
        STAMP(12);
    }
}

extern "C" void kernel_launch(void* const* d_in, const int* in_sizes, int n_in, void* d_out, int out_size, void* d_ws, size_t ws_size,
                              hipStream_t stream) {
    Params p;
    p.x = (const char*)d_in[0]; p.a_re = (const char*)d_in[1]; p.a_im = (const char*)d_in[2]; p.b_re = (const char*)d_in[3];
    p.b_im = (const char*)d_in[4]; p.c_re = (const char*)d_in[5]; p.c_im = (const char*)d_in[6]; p.log_dt = (const char*)d_in[7];
    p.dskip = (const char*)d_in[8]; p.wqkv = (const char*)d_in[10]; p.wo = (const char*)d_in[11]; p.gate_w = (const char*)d_in[12];
    p.out = (char*)d_out; p.ws = (char*)d_ws;
    s4_mha_mix<<<256, 512, 0, stream>>>(p);
}
```

```cpp
#include <hip/hip_runtime.h>
#include <hip/hip_bf16.h>
#include <stdint.h>
#pragma clang diagnostic ignored "-Wint-to-pointer-cast"

#define LAS __attribute__((address_space(3)))
#define GAS __attribute__((address_space(1)))
typedef unsigned u32;
typedef unsigned long long u64;
typedef __attribute__((ext_vector_type(2))) u32 u32x2;
typedef __attribute__((ext_vector_type(4))) u32 u32x4;
typedef __attribute__((ext_vector_type(8))) u32 u32x8;
typedef __attribute__((ext_vector_type(2))) float f32x2;
typedef __attribute__((ext_vector_type(4))) float f32x4;
typedef __attribute__((ext_vector_type(8))) float f32x8;
typedef __attribute__((ext_vector_type(16))) float f32x16;
typedef __attribute__((ext_vector_type(2))) __bf16 bf16x2;
typedef __attribute__((ext_vector_type(8))) __bf16 bf16x8;
typedef __attribute__((ext_vector_type(16))) __bf16 bf16x16;
typedef __attribute__((ext_vector_type(4))) short s16x4;
typedef __attribute__((ext_vector_type(2))) unsigned short u16x2;

constexpr u32 WS_KV = 0;
constexpr u32 WS_OF = 4u << 20;
constexpr u32 WS_YT = 6u << 20;
constexpr u32 WS_UT = 8u << 20;
constexpr u32 WS_KVFLAG = 10u << 20;
constexpr u32 WS_UFLAG = WS_KVFLAG + 65536;
constexpr u32 WS_OFLAG = WS_UFLAG + 65536;
constexpr u32 WS_YFLAG = WS_OFLAG + 131072;
constexpr u32 WS_NONCE = WS_YFLAG + 65536;
constexpr u32 WS_GP = WS_NONCE + 4096;
constexpr u32 WS_END = 11u << 20;

constexpr u32 CHUNK = 28672, L_W = 16384;
constexpr u32 L_TAB = 4 * CHUNK, TAB_ROW = 272, TAB_A = 21 * TAB_ROW, TAB_CH = 53 * TAB_ROW;
constexpr u32 L_QX = L_TAB + 2 * TAB_CH;
constexpr u32 L_KF = L_QX + 8192, KF_CH = 4608;
constexpr u32 L_QN = L_KF + 2 * KF_CH;
constexpr u32 L_SYNC = L_QN + 1024;
constexpr u32 S_PAIR = 0, S_READY = 8, S_PART = 12, S_Q = 20, S_SVC = 24, S_WO = 28, S_LANDED = 32, S_FREED = 64;
constexpr u32 L_KB = L_TAB, KB_CH = TAB_CH;
constexpr u32 L_UC = 0, UC_COPY = 4160, UC_CH = 4 * UC_COPY;
constexpr u32 L_CX = 2 * UC_CH;
constexpr u32 L_WO = 49152;
constexpr u32 L_YT = L_KF;
constexpr u32 LDS_BYTES = 163840;
static_assert(L_SYNC + 96 <= LDS_BYTES && L_CX + 3 * 4096 <= L_WO && L_WO + 65536 <= L_TAB && L_TAB + 12 * 3072 <= L_KF, "lds map");

constexpr float LOG2E = 1.4426950408889634f;
constexpr float QSCALE = 0.25f * LOG2E;
constexpr float SAFE2 = 64.f * 64.f;
constexpr int CODE_BYTES = 24000;
constexpr u32 SPIN_CAP = 1u << 15;

#define LD(T, off) (*(const LAS T*)(u32)(off))
#define ST(T, off) (*(LAS T*)(u32)(off))
#define RLX_AGENT __ATOMIC_RELAXED, __HIP_MEMORY_SCOPE_AGENT
#define SC1 16
#define LGKM0() do { asm volatile("" ::: "memory"); __builtin_amdgcn_s_waitcnt(0xc07f); } while (0)
#define BAR(N) do { LGKM0(); asm volatile("s_waitcnt vmcnt(" #N ") lgkmcnt(0)\n\ts_barrier" ::: "memory"); } while (0)
#define BAR_LDS() do { LGKM0(); asm volatile("s_waitcnt lgkmcnt(0)\n\ts_barrier" ::: "memory"); } while (0)

__device__ __forceinline__ u32 pk2(float a, float b) { bf16x2 v = {(__bf16)a, (__bf16)b}; return __builtin_bit_cast(u32, v); }
__device__ __forceinline__ float dot2(u32 a, u32 b, float c) {
    return __builtin_amdgcn_fdot2_f32_bf16(__builtin_bit_cast(bf16x2, a), __builtin_bit_cast(bf16x2, b), c, false); }
__device__ __forceinline__ float bflo(u32 v) { return __uint_as_float(v << 16); }
__device__ __forceinline__ float bfhi(u32 v) { return __uint_as_float(v & 0xffff0000u); }
__device__ __forceinline__ f32x16 mfma32(u32x4 a, u32x4 b, f32x16 c) {
    return __builtin_amdgcn_mfma_f32_32x32x16_bf16(__builtin_bit_cast(bf16x8, a), __builtin_bit_cast(bf16x8, b), c, 0, 0, 0); }
__device__ __forceinline__ f32x4 mfma16(u32x4 a, u32x4 b, f32x4 c) {
    return __builtin_amdgcn_mfma_f32_16x16x32_bf16(__builtin_bit_cast(bf16x8, a), __builtin_bit_cast(bf16x8, b), c, 0, 0, 0); }
__device__ __forceinline__ u32x2 trd(u32 off) {
    return __builtin_bit_cast(u32x2, __builtin_amdgcn_ds_read_tr16_b64_v4i16((LAS s16x4*)off)); }
__device__ __forceinline__ u32x4 trd2(u32 o0, u32 o1) { const u32x2 a = trd(o0), b = trd(o1); return (u32x4){a.x, a.y, b.x, b.y}; }
__device__ __forceinline__ void dma1(u32 v0, const void* sbase, u32 dst) {
    asm volatile("s_mov_b32 m0, %2\n\ts_nop 0\n\tglobal_load_lds_dwordx4 %0, %1" :: "v"(v0), "s"(sbase), "s"(dst) : "memory"); }
template <int OFF> __device__ __forceinline__ void dma_next(u32 v0, const void* sbase) {
    asm volatile("global_load_lds_dwordx4 %0, %1 offset:%2" :: "v"(v0), "s"(sbase), "i"(OFF) : "memory"); }
__device__ __forceinline__ void dma3(u32 v0, u32 v1, const void* sbase, u32 dst) {
    asm volatile("s_mov_b32 m0, %3\n\ts_nop 0\n\tglobal_load_lds_dwordx4 %0, %2\n\tglobal_load_lds_dwordx4 %1, %2 offset:1024\n\t"
                 "global_load_lds_dwordx4 %0, %2 offset:2048" :: "v"(v0), "v"(v1), "s"(sbase), "s"(dst) : "memory"); }
__device__ __forceinline__ void dma4(u32 v0, u32 v1, u32 v2, u32 v3, const void* sbase, u32 dst) {
    asm volatile("s_mov_b32 m0, %5\n\ts_nop 0\n\tglobal_load_lds_dwordx4 %0, %4\n\tglobal_load_lds_dwordx4 %1, %4 offset:1024\n\t"
                 "global_load_lds_dwordx4 %2, %4 offset:2048\n\tglobal_load_lds_dwordx4 %3, %4 offset:3072"
                 :: "v"(v0), "v"(v1), "v"(v2), "v"(v3), "s"(sbase), "s"(dst) : "memory"); }
__device__ __forceinline__ float halves_sum(float v) {
    auto r = __builtin_amdgcn_permlane32_swap(__float_as_uint(v), __float_as_uint(v), false, false);
    return __uint_as_float(r[0]) + __uint_as_float(r[1]); }
__device__ __forceinline__ float halves_max(float v) {
    auto r = __builtin_amdgcn_permlane32_swap(__float_as_uint(v), __float_as_uint(v), false, false);
    return fmaxf(__uint_as_float(r[0]), __uint_as_float(r[1])); }
template <int CTRL> __device__ __forceinline__ u32 dpp_max(u32 v) { return max(v, (u32)__builtin_amdgcn_update_dpp(0, (int)v, CTRL, 0xf, 0xf, true)); }
__device__ __forceinline__ u32 row_max(u32 v) { return dpp_max<0x118>(dpp_max<0x114>(dpp_max<0x112>(dpp_max<0x111>(v)))); }
__device__ __forceinline__ u32x4 widen(u32 a0, u32 a1, u32 b0, u32 b1) {
    auto r0 = __builtin_amdgcn_permlane32_swap(a0, b0, false, false);
    auto r1 = __builtin_amdgcn_permlane32_swap(a1, b1, false, false);
    return (u32x4){r0[0], r1[0], r0[1], r1[1]}; }
__device__ __forceinline__ f32x2 cmul(f32x2 a, f32x2 b) {
    float x, y;
    asm("v_mul_f32 %0, %2, %4\n\tv_mul_f32 %1, %2, %5\n\tv_fma_f32 %0, -%3, %5, %0\n\tv_fmac_f32 %1, %3, %4" : "=&v"(x), "=&v"(y) : "v"(a.x), "v"(a.y), "v"(b.x), "v"(b.y));
    return (f32x2){x, y}; }
__device__ __forceinline__ void cmul2(f32x2& o0, f32x2 a, f32x2 b, f32x2& o1, f32x2 c, f32x2 d) {
    float x, y, z, w;
    asm("v_mul_f32 %0, %4, %6\n\tv_mul_f32 %2, %8, %10\n\tv_mul_f32 %1, %4, %7\n\tv_mul_f32 %3, %8, %11\n\t"
        "v_fma_f32 %0, -%5, %7, %0\n\tv_fma_f32 %2, -%9, %11, %2\n\tv_fmac_f32 %1, %5, %6\n\tv_fmac_f32 %3, %9, %10"
        : "=&v"(x), "=&v"(y), "=&v"(z), "=&v"(w) : "v"(a.x), "v"(a.y), "v"(b.x), "v"(b.y), "v"(c.x), "v"(c.y), "v"(d.x), "v"(d.y));
    o0 = (f32x2){x, y}; o1 = (f32x2){z, w}; }
__device__ __forceinline__ f32x2 cexp_n(float zr2, float th, float n) {
    const float m = __builtin_amdgcn_exp2f(zr2 * n), a = __builtin_amdgcn_fractf(th * n);
    return (f32x2){m * __builtin_amdgcn_cosf(a), m * __builtin_amdgcn_sinf(a)}; }
__device__ __forceinline__ u64 poll_tag(const char* p, bool active, u64 tag) {
    u64 v = tag << 16;
#pragma unroll 1
    for (u32 spins = 0; spins < SPIN_CAP; ++spins) {
        if (active) v = __hip_atomic_load((const GAS u64*)p, RLX_AGENT);
        if (__all((v >> 16) == tag)) break;
        __builtin_amdgcn_s_sleep(1);
    }
    asm volatile("s_waitcnt vmcnt(0)" ::: "memory");
    __builtin_amdgcn_fence(__ATOMIC_ACQUIRE, "wavefront");
    return v; }

struct Params {
    const char *x, *a_re, *a_im, *b_re, *b_im, *c_re, *c_im, *log_dt, *dskip, *wqkv, *wo, *gate_w;
    char *out, *ws;
};

#define SB() __builtin_amdgcn_sched_barrier(0)
__device__ __forceinline__ void arrive(u32 off) {
    LGKM0();
    asm volatile("s_mov_b64 exec, 1\n\tds_add_u32 %0, %1\n\ts_mov_b64 exec, -1" :: "v"(L_SYNC + off), "v"(1u) : "memory"); }
#define LD_RLX(off) __hip_atomic_load((LAS u32*)(L_SYNC + (off)), __ATOMIC_RELAXED, __HIP_MEMORY_SCOPE_WORKGROUP)
__device__ __forceinline__ void await(u32 off, u32 n) {
    while (__hip_atomic_load((LAS u32*)(L_SYNC + off), __ATOMIC_RELAXED, __HIP_MEMORY_SCOPE_WORKGROUP) < n) __builtin_amdgcn_s_sleep(0);
    asm volatile("" ::: "memory"); }
#define WAIT_VM(N) asm volatile("s_waitcnt vmcnt(" #N ")" ::: "memory")
__device__ __forceinline__ void post(u32 off, int tb, u32 n) { __hip_atomic_store((LAS u32*)(L_SYNC + off + tb * 4), n, __ATOMIC_RELAXED, __HIP_MEMORY_SCOPE_WORKGROUP); }
__device__ __forceinline__ u32x4 peek4(u32 off) {
    u32x4 v; asm volatile("ds_read_b128 %0, %1" : "=&v"(v) : "v"(L_SYNC + off) : "memory"); return v; }
__device__ __forceinline__ void await4(u32 off, u32 n, u32x4 v) {
    for (;;) {
        asm volatile("s_waitcnt lgkmcnt(0)" : "+v"(v) :: "memory");
        if (__builtin_amdgcn_readfirstlane(min(min(v.x, v.y), min(v.z, v.w))) >= n) break;
        v = peek4(off);
    } }

__device__ __forceinline__ u32 w_off_q(int lane) { return (lane >> 2) * 3072 + ((lane & 3) << 4); }
__device__ __forceinline__ u32 w_off_kv(int lane, int hp) { return (lane >> 3) * 3072 + (((lane & 7) ^ ((lane >> 2) & 4)) << 4) + hp * 64; }

template <class F> __device__ __forceinline__ void table_a(float dt, float are, float aim, float bre, float bim, float cre, float cim, F put) {
    const float zr2 = (dt * are) * LOG2E, th = (dt * aim) * -0.15915494309189535f;
    const f32x2 e1 = cexp_n(zr2, th, 1.f), gg = cexp_n(zr2, th, 256.f);
    f32x2 a[8], e2, g2, b0, b2, b4, b6;
#define PUT(row, v) put(row, pk2((v).x, (v).y))
    cmul2(a[0], (f32x2){cre, -cim}, (f32x2){dt * bre, -dt * bim}, e2, e1, e1);
    cmul2(a[1], a[0], e1, g2, gg, gg);
#pragma unroll
    for (int i = 2; i < 8; i += 2) cmul2(a[i], a[i - 2], e2, a[i + 1], a[i - 1], e2);
#pragma unroll
    for (int i = 0; i < 8; ++i) PUT(i, a[i]);
    cmul2(b2, a[2], gg, b6, a[6], gg); cmul2(b0, a[0], gg, b4, a[4], gg);
    PUT(8, b0); PUT(9, b2); PUT(10, b4); PUT(11, b6);
    cmul2(b0, b0, gg, b4, b4, gg); PUT(12, b0); PUT(13, b4);
    cmul2(b0, b0, gg, b4, b4, gg); PUT(14, b0); PUT(15, b4);
    cmul2(b4, b0, gg, b0, b0, g2); PUT(16, b4); PUT(17, b0);
    cmul2(b4, b4, g2, b0, b0, g2); PUT(18, b4); PUT(19, b0);
    b4 = cmul(b4, g2); PUT(20, b4);
}
template <class F> __device__ __forceinline__ void table_b(float dt, float are, float aim, F put) {
    f32x2 p0 = {1.f, 0.f}, p1 = cexp_n((dt * are) * LOG2E, (dt * aim) * 0.15915494309189535f, 8.f);
    const f32x2 e16 = cmul(p1, p1);
#pragma unroll
    for (int i = 0; i < 32; i += 2) { PUT(21 + i, p0); PUT(22 + i, p1); cmul2(p0, p0, e16, p1, p1, e16); }
#undef PUT
}
#define LOAD_PARAMS(d) asm volatile("global_load_dword %0, %6, %7\n\tglobal_load_dword %1, %6, %8\n\tglobal_load_dword %2, %6, %9\n\t" \
        "global_load_dword %3, %6, %10\n\tglobal_load_dword %4, %6, %11\n\tglobal_load_dword %5, %6, %12" \
        : "=&v"(are), "=&v"(aim), "=&v"(bre), "=&v"(bim), "=&v"(cre), "=&v"(cim) \
        : "v"((d) * 256 + lane * 4), "s"(p.a_re), "s"(p.a_im), "s"(p.b_re), "s"(p.b_im), "s"(p.c_re), "s"(p.c_im))

__device__ __forceinline__ u64 phase1_mfma(const Params& p, __amdgpu_buffer_rsrc_t wsr, char* ws, int lane, int tb, int hp, int qb) {
    const int r = lane & 31, h = lane >> 5, g1 = (lane >> 4) & 1, q4 = (lane >> 2) & 3;
    const bool ISV = hp >= 8;
    __builtin_amdgcn_s_setprio(0);
    u32x8 gw;
    asm volatile("s_load_dwordx8 %0, %1, 0x0" : "=&{s[56:63]}"(gw) : "s"(p.gate_w + (hp * 32 + (tb & 1) * 16) * 2));
    SB();
    u32x2 nv;
    u32x4 xa;
#pragma unroll
    for (int ks = 0; ks < 4; ++ks) xa[ks] = (tb * 32 + r) * 128 + (((2 * ks + h) ^ ((r >> 1) & 7)) << 4);
    u32 w0 = L_W + (8 * h + q4) * 64 + (g1 << 5) + (lane & 3) * 8;
    u32 w1 = L_W + 1024 + (8 * h + q4) * 128 + ((q4 >> 1) << 6) + (g1 << 5) + (lane & 3) * 8, w2 = w1 ^ 64;
    u32x2 ut;
#pragma unroll
    for (int i = 0; i < 2; ++i) {
        const int row = 16 * (lane >> 4) + 4 * i + q4;
        ut[i] = ((tb >> 1) * 64 + row) * 128 + (((4 * (hp & 1) + 2 * (tb & 1) + ((lane >> 1) & 1)) ^ ((row >> 1) & 7)) << 4) + (lane & 1) * 8;
    }
    u32 usw = L_QX + tb * 2048 + (lane & 15) * 128 + (lane >> 4) * 32;
    const u32 tile = qb * 4 + tb;
    u32 uto = WS_UT + ((hp * 32 + (tb & 1) * 16 + (lane >> 3)) * 2048 + qb * 128 + (tb >> 1) * 64) * 2 + (lane & 7) * 16;
    u32 gxa = ((tb >> 1) * 64 + lane) * 128 + (((4 * (hp & 1) + 2 * (tb & 1)) ^ ((lane >> 1) & 7)) << 4);
    u32 gpo = WS_GP + ((qb * 32 + hp * 2 + (tb & 1)) * 128 + (tb >> 1) * 64 + lane) * 4;
    f32x16 a0, a1, a2;
    const u32x4 srd = {(u32)(uintptr_t)ws, (u32)((uintptr_t)ws >> 32) & 0xffffu, WS_END, 0x00020000u};
    const u64 ufp = (u64)(uintptr_t)(ws + WS_UFLAG + (hp * 64 + tile) * 64);
    asm volatile(
        "v_mov_b32 v119, 0\nglobal_load_dwordx2 v[124:125], v119, s[50:51]\nv_mov_b64 v[0:1], 0\nv_mov_b64 v[2:3], 0\nv_mov_b64 v[4:5], 0\n"
        "v_mov_b64 v[6:7], 0\nv_mov_b64 v[8:9], 0\nv_mov_b64 v[10:11], 0\nv_mov_b64 v[12:13], 0\nv_mov_b64 v[14:15], 0\nv_mov_b64 v[16:17], 0\n"
        "v_mov_b64 v[18:19], 0\nv_mov_b64 v[20:21], 0\nv_mov_b64 v[22:23], 0\nv_mov_b64 v[24:25], 0\nv_mov_b64 v[26:27], 0\nv_mov_b64 v[28:29], 0\n"
        "v_mov_b64 v[30:31], 0\nv_mov_b64 v[32:33], 0\nv_mov_b64 v[34:35], 0\nv_mov_b64 v[36:37], 0\nv_mov_b64 v[38:39], 0\nv_mov_b64 v[40:41], 0\n"
        "v_mov_b64 v[42:43], 0\nv_mov_b64 v[44:45], 0\nv_mov_b64 v[46:47], 0\ns_mov_b32 s40, 0\ns_mov_b32 s41, 28672\nv_mov_b32 v122, 8\ns_waitcnt vmcnt(1)\n"
        "ds_write_b32 v120, v122 offset:16\nLa%=:\nds_read_b32 v122, v121\ns_waitcnt lgkmcnt(0)\nv_cmp_ge_u32 vcc, s40, v122\ns_cbranch_vccnz La%=\n"
        "ds_read_b128 v[48:51], v112\nds_read_b64_tr_b16 v[52:53], v116 offset:0\nds_read_b64_tr_b16 v[54:55], v116 offset:256\n"
        "ds_read_b64_tr_b16 v[56:57], v117 offset:0\nds_read_b64_tr_b16 v[58:59], v117 offset:512\nds_read_b64_tr_b16 v[60:61], v118 offset:0\n"
        "ds_read_b64_tr_b16 v[62:63], v118 offset:512\nds_write_b32 v120, v119 offset:32\nds_read_b128 v[64:67], v113\n"
        "ds_read_b64_tr_b16 v[68:69], v116 offset:3072\nds_read_b64_tr_b16 v[70:71], v116 offset:3328\nds_read_b64_tr_b16 v[72:73], v117 offset:3072\n"
        "ds_read_b64_tr_b16 v[74:75], v117 offset:3584\nds_read_b64_tr_b16 v[76:77], v118 offset:3072\nds_read_b64_tr_b16 v[78:79], v118 offset:3584\n"
        "v_add_u32 v112, s41, v112\nv_add_u32 v113, s41, v113\ns_cmp_lg_u32 s43, 0\ns_cbranch_scc1 Lvb%=\n.p2align 3\nLkb%=:\ns_waitcnt lgkmcnt(8)\n"
        "v_mfma_f32_32x32x16_bf16 v[0:15], v[52:55], v[48:51], v[0:15]\nds_read_b128 v[80:83], v114\nds_read_b64_tr_b16 v[84:85], v116 offset:6144\n"
        "ds_read_b64_tr_b16 v[86:87], v116 offset:6400\nv_add_u32 v114, s41, v114\nv_mfma_f32_32x32x16_bf16 v[16:31], v[56:59], v[48:51], v[16:31]\n"
        "ds_read_b64_tr_b16 v[88:89], v117 offset:6144\nds_read_b64_tr_b16 v[90:91], v117 offset:6656\n"
        "v_mfma_f32_32x32x16_bf16 v[32:47], v[60:63], v[48:51], v[32:47]\nds_read_b64_tr_b16 v[92:93], v118 offset:6144\n"
        "ds_read_b64_tr_b16 v[94:95], v118 offset:6656\ns_waitcnt lgkmcnt(7)\nv_mfma_f32_32x32x16_bf16 v[0:15], v[68:71], v[64:67], v[0:15]\n"
        "ds_read_b32 v122, v121\nds_read_b128 v[96:99], v115\nds_read_b64_tr_b16 v[100:101], v116 offset:9216\n"
        "ds_read_b64_tr_b16 v[102:103], v116 offset:9472\nv_add_u32 v115, s41, v115\nv_mfma_f32_32x32x16_bf16 v[16:31], v[72:75], v[64:67], v[16:31]\n"
        "ds_read_b64_tr_b16 v[104:105], v117 offset:9216\nds_read_b64_tr_b16 v[106:107], v117 offset:9728\nv_add_u32 v116, s41, v116\ns_add_u32 s40, s40, 1\n"
        "v_mfma_f32_32x32x16_bf16 v[32:47], v[76:79], v[64:67], v[32:47]\nds_read_b64_tr_b16 v[108:109], v118 offset:9216\n"
        "ds_read_b64_tr_b16 v[110:111], v118 offset:9728\nv_add_u32 v117, s41, v117\nv_add_u32 v118, s41, v118\ns_cmp_eq_u32 s40, 8\ns_cbranch_scc1 Lkd%=\n"
        "s_waitcnt lgkmcnt(8)\nv_mfma_f32_32x32x16_bf16 v[0:15], v[84:87], v[80:83], v[0:15]\ns_cmp_eq_u32 s40, 1\ns_cbranch_scc1 Lku%=\ns_waitcnt lgkmcnt(7)\n"
        "v_cmp_ge_u32 vcc, s40, v122\ns_cbranch_vccnz Lks%=\nLkc%=:\ns_mov_b32 s41, 28672\ns_cmp_eq_u32 s40, 3\ns_cmov_b32 s41, -86016\n"
        "v_mfma_f32_32x32x16_bf16 v[16:31], v[88:91], v[80:83], v[16:31]\nds_read_b128 v[48:51], v112\nds_read_b64_tr_b16 v[52:53], v116 offset:0\n"
        "ds_read_b64_tr_b16 v[54:55], v116 offset:256\nds_read_b64_tr_b16 v[56:57], v117 offset:0\nv_add_u32 v112, s41, v112\n"
        "v_mfma_f32_32x32x16_bf16 v[32:47], v[92:95], v[80:83], v[32:47]\nds_read_b64_tr_b16 v[58:59], v117 offset:512\n"
        "ds_read_b64_tr_b16 v[60:61], v118 offset:0\nds_read_b64_tr_b16 v[62:63], v118 offset:512\nv_add_u32 v119, 1, v119\ns_waitcnt lgkmcnt(7)\n"
        "v_mfma_f32_32x32x16_bf16 v[0:15], v[100:103], v[96:99], v[0:15]\nds_write_b32 v120, v119 offset:32\nds_read_b128 v[64:67], v113\n"
        "ds_read_b64_tr_b16 v[68:69], v116 offset:3072\nds_read_b64_tr_b16 v[70:71], v116 offset:3328\nv_add_u32 v113, s41, v113\n"
        "v_mfma_f32_32x32x16_bf16 v[16:31], v[104:107], v[96:99], v[16:31]\nds_read_b64_tr_b16 v[72:73], v117 offset:3072\n"
        "ds_read_b64_tr_b16 v[74:75], v117 offset:3584\nv_mfma_f32_32x32x16_bf16 v[32:47], v[108:111], v[96:99], v[32:47]\n"
        "ds_read_b64_tr_b16 v[76:77], v118 offset:3072\nds_read_b64_tr_b16 v[78:79], v118 offset:3584\ns_cmp_lg_u32 s40, 2\ns_cbranch_scc1 Lkb%=\n"
        "s_waitcnt vmcnt(0)\nv_add_co_u32 v134, vcc, 1, v124\nv_mov_b32 v136, 0\ns_nop 0\nv_addc_co_u32 v135, vcc, 0, v125, vcc\n"
        "v_lshlrev_b64 v[134:135], 16, v[134:135]\ns_mov_b64 exec, 1\nglobal_store_dwordx2 v136, v[134:135], s[48:49] sc1\ns_mov_b64 exec, -1\ns_branch Lkb%=\n"
        "Lku%=:\nds_read_b64_tr_b16 v[48:49], v126\nds_read_b64_tr_b16 v[50:51], v127\nv_xor_b32 v126, 64, v126\nv_xor_b32 v127, 64, v127\n"
        "ds_read_b64_tr_b16 v[52:53], v126 offset:1024\nds_read_b64_tr_b16 v[54:55], v127 offset:1024\nds_read_b128 v[64:67], v130\nv_xor_b32 v130, 16, v130\n"
        "ds_read_b128 v[68:71], v130\nv_mov_b32 v123, 0\ns_waitcnt lgkmcnt(2)\nds_write_b128 v131, v[48:51]\nds_write_b128 v131, v[52:55] offset:16\n"
        "ds_read_b128 v[48:51], v132\nds_read_b128 v[52:55], v132 offset:1024\ns_waitcnt lgkmcnt(4)\nv_dot2c_f32_bf16 v123, s56, v64\n"
        "v_dot2c_f32_bf16 v123, s57, v65\nv_dot2c_f32_bf16 v123, s58, v66\nv_dot2c_f32_bf16 v123, s59, v67\nv_dot2c_f32_bf16 v123, s60, v68\n"
        "v_dot2c_f32_bf16 v123, s61, v69\nv_dot2c_f32_bf16 v123, s62, v70\nv_dot2c_f32_bf16 v123, s63, v71\nv_add_u32 v64, 0x8000, v128\ns_waitcnt lgkmcnt(0)\n"
        "buffer_store_dwordx4 v[48:51], v128, s[44:47], 0 offen sc1\nbuffer_store_dwordx4 v[52:55], v64, s[44:47], 0 offen sc1\n"
        "buffer_store_dword v123, v129, s[44:47], 0 offen sc1\nLks%=:\nv_add_u32 v122, 1, v119\ns_waitcnt lgkmcnt(0)\nds_write_b32 v120, v122 offset:32\n"
        "Lkt%=:\nds_read_b32 v122, v121\ns_waitcnt lgkmcnt(0)\nv_cmp_ge_u32 vcc, s40, v122\ns_cbranch_vccnz Lkt%=\ns_branch Lkc%=\nLkd%=:\n"
        "s_waitcnt lgkmcnt(8)\nv_mfma_f32_32x32x16_bf16 v[0:15], v[84:87], v[80:83], v[0:15]\nv_add_u32 v119, 1, v119\n"
        "v_mfma_f32_32x32x16_bf16 v[16:31], v[88:91], v[80:83], v[16:31]\nv_mfma_f32_32x32x16_bf16 v[32:47], v[92:95], v[80:83], v[32:47]\n"
        "s_waitcnt lgkmcnt(0)\nv_mfma_f32_32x32x16_bf16 v[0:15], v[100:103], v[96:99], v[0:15]\nds_write_b32 v120, v119 offset:32\n"
        "v_mfma_f32_32x32x16_bf16 v[16:31], v[104:107], v[96:99], v[16:31]\nv_mfma_f32_32x32x16_bf16 v[32:47], v[108:111], v[96:99], v[32:47]\n"
        "s_branch Lend%=\n.p2align 3\nLvb%=:\ns_waitcnt lgkmcnt(8)\nv_mfma_f32_32x32x16_bf16 v[0:15], v[52:55], v[48:51], v[0:15]\n"
        "ds_read_b128 v[80:83], v114\nds_read_b64_tr_b16 v[84:85], v116 offset:6144\nds_read_b64_tr_b16 v[86:87], v116 offset:6400\nv_add_u32 v114, s41, v114\n"
        "v_mfma_f32_32x32x16_bf16 v[16:31], v[48:51], v[56:59], v[16:31]\nds_read_b64_tr_b16 v[88:89], v117 offset:6144\n"
        "ds_read_b64_tr_b16 v[90:91], v117 offset:6656\nv_mfma_f32_32x32x16_bf16 v[32:47], v[48:51], v[60:63], v[32:47]\n"
        "ds_read_b64_tr_b16 v[92:93], v118 offset:6144\nds_read_b64_tr_b16 v[94:95], v118 offset:6656\ns_waitcnt lgkmcnt(7)\n"
        "v_mfma_f32_32x32x16_bf16 v[0:15], v[68:71], v[64:67], v[0:15]\nds_read_b32 v122, v121\nds_read_b128 v[96:99], v115\n"
        "ds_read_b64_tr_b16 v[100:101], v116 offset:9216\nds_read_b64_tr_b16 v[102:103], v116 offset:9472\nv_add_u32 v115, s41, v115\n"
        "v_mfma_f32_32x32x16_bf16 v[16:31], v[64:67], v[72:75], v[16:31]\nds_read_b64_tr_b16 v[104:105], v117 offset:9216\n"
        "ds_read_b64_tr_b16 v[106:107], v117 offset:9728\nv_add_u32 v116, s41, v116\ns_add_u32 s40, s40, 1\n"
        "v_mfma_f32_32x32x16_bf16 v[32:47], v[64:67], v[76:79], v[32:47]\nds_read_b64_tr_b16 v[108:109], v118 offset:9216\n"
        "ds_read_b64_tr_b16 v[110:111], v118 offset:9728\nv_add_u32 v117, s41, v117\nv_add_u32 v118, s41, v118\ns_cmp_eq_u32 s40, 8\ns_cbranch_scc1 Lvd%=\n"
        "s_waitcnt lgkmcnt(8)\nv_mfma_f32_32x32x16_bf16 v[0:15], v[84:87], v[80:83], v[0:15]\ns_cmp_eq_u32 s40, 1\ns_cbranch_scc1 Lvu%=\ns_waitcnt lgkmcnt(7)\n"
        "v_cmp_ge_u32 vcc, s40, v122\ns_cbranch_vccnz Lvs%=\nLvc%=:\ns_mov_b32 s41, 28672\ns_cmp_eq_u32 s40, 3\ns_cmov_b32 s41, -86016\n"
        "v_mfma_f32_32x32x16_bf16 v[16:31], v[80:83], v[88:91], v[16:31]\nds_read_b128 v[48:51], v112\nds_read_b64_tr_b16 v[52:53], v116 offset:0\n"
        "ds_read_b64_tr_b16 v[54:55], v116 offset:256\nds_read_b64_tr_b16 v[56:57], v117 offset:0\nv_add_u32 v112, s41, v112\n"
        "v_mfma_f32_32x32x16_bf16 v[32:47], v[80:83], v[92:95], v[32:47]\nds_read_b64_tr_b16 v[58:59], v117 offset:512\n"
        "ds_read_b64_tr_b16 v[60:61], v118 offset:0\nds_read_b64_tr_b16 v[62:63], v118 offset:512\nv_add_u32 v119, 1, v119\ns_waitcnt lgkmcnt(7)\n"
        "v_mfma_f32_32x32x16_bf16 v[0:15], v[100:103], v[96:99], v[0:15]\nds_write_b32 v120, v119 offset:32\nds_read_b128 v[64:67], v113\n"
        "ds_read_b64_tr_b16 v[68:69], v116 offset:3072\nds_read_b64_tr_b16 v[70:71], v116 offset:3328\nv_add_u32 v113, s41, v113\n"
        "v_mfma_f32_32x32x16_bf16 v[16:31], v[96:99], v[104:107], v[16:31]\nds_read_b64_tr_b16 v[72:73], v117 offset:3072\n"
        "ds_read_b64_tr_b16 v[74:75], v117 offset:3584\nv_mfma_f32_32x32x16_bf16 v[32:47], v[96:99], v[108:111], v[32:47]\n"
        "ds_read_b64_tr_b16 v[76:77], v118 offset:3072\nds_read_b64_tr_b16 v[78:79], v118 offset:3584\ns_cmp_lg_u32 s40, 2\ns_cbranch_scc1 Lvb%=\n"
        "s_waitcnt vmcnt(0)\nv_add_co_u32 v134, vcc, 1, v124\nv_mov_b32 v136, 0\ns_nop 0\nv_addc_co_u32 v135, vcc, 0, v125, vcc\n"
        "v_lshlrev_b64 v[134:135], 16, v[134:135]\ns_mov_b64 exec, 1\nglobal_store_dwordx2 v136, v[134:135], s[48:49] sc1\ns_mov_b64 exec, -1\ns_branch Lvb%=\n"
        "Lvu%=:\nds_read_b64_tr_b16 v[48:49], v126\nds_read_b64_tr_b16 v[50:51], v127\nv_xor_b32 v126, 64, v126\nv_xor_b32 v127, 64, v127\n"
        "ds_read_b64_tr_b16 v[52:53], v126 offset:1024\nds_read_b64_tr_b16 v[54:55], v127 offset:1024\nds_read_b128 v[64:67], v130\nv_xor_b32 v130, 16, v130\n"
        "ds_read_b128 v[68:71], v130\nv_mov_b32 v123, 0\ns_waitcnt lgkmcnt(2)\nds_write_b128 v131, v[48:51]\nds_write_b128 v131, v[52:55] offset:16\n"
        "ds_read_b128 v[48:51], v132\nds_read_b128 v[52:55], v132 offset:1024\ns_waitcnt lgkmcnt(4)\nv_dot2c_f32_bf16 v123, s56, v64\n"
        "v_dot2c_f32_bf16 v123, s57, v65\nv_dot2c_f32_bf16 v123, s58, v66\nv_dot2c_f32_bf16 v123, s59, v67\nv_dot2c_f32_bf16 v123, s60, v68\n"
        "v_dot2c_f32_bf16 v123, s61, v69\nv_dot2c_f32_bf16 v123, s62, v70\nv_dot2c_f32_bf16 v123, s63, v71\nv_add_u32 v64, 0x8000, v128\ns_waitcnt lgkmcnt(0)\n"
        "buffer_store_dwordx4 v[48:51], v128, s[44:47], 0 offen sc1\nbuffer_store_dwordx4 v[52:55], v64, s[44:47], 0 offen sc1\n"
        "buffer_store_dword v123, v129, s[44:47], 0 offen sc1\nLvs%=:\nv_add_u32 v122, 1, v119\ns_waitcnt lgkmcnt(0)\nds_write_b32 v120, v122 offset:32\n"
        "Lvt%=:\nds_read_b32 v122, v121\ns_waitcnt lgkmcnt(0)\nv_cmp_ge_u32 vcc, s40, v122\ns_cbranch_vccnz Lvt%=\ns_branch Lvc%=\nLvd%=:\n"
        "s_waitcnt lgkmcnt(8)\nv_mfma_f32_32x32x16_bf16 v[0:15], v[84:87], v[80:83], v[0:15]\nv_add_u32 v119, 1, v119\n"
        "v_mfma_f32_32x32x16_bf16 v[16:31], v[80:83], v[88:91], v[16:31]\nv_mfma_f32_32x32x16_bf16 v[32:47], v[80:83], v[92:95], v[32:47]\n"
        "s_waitcnt lgkmcnt(0)\nv_mfma_f32_32x32x16_bf16 v[0:15], v[100:103], v[96:99], v[0:15]\nds_write_b32 v120, v119 offset:32\n"
        "v_mfma_f32_32x32x16_bf16 v[16:31], v[96:99], v[104:107], v[16:31]\nv_mfma_f32_32x32x16_bf16 v[32:47], v[96:99], v[108:111], v[32:47]\nLend%=:\n"
        "s_nop 11\n"
        : "=&{v[0:15]}"(a0), "=&{v[16:31]}"(a1), "=&{v[32:47]}"(a2), "+{v[112:115]}"(xa), "+{v116}"(w0), "+{v117}"(w1), "+{v118}"(w2), "=&{v[124:125]}"(nv),
          "+{v[126:127]}"(ut), "+{v130}"(gxa)
        : "{v120}"(L_SYNC + S_LANDED + tb * 4), "{v121}"(L_SYNC + S_LANDED + (lane & 7) * 4), "{s[50:51]}"(ws + WS_NONCE), "{s43}"(hp >> 3),
          "{v128}"(uto), "{v129}"(gpo), "{v131}"(usw), "{v132}"(L_QX + tb * 2048 + lane * 16), "{s[44:47]}"(srd), "{s[48:49]}"(ufp), "{s[56:63]}"(gw)
        : "memory", "scc", "vcc", "s40", "s41", "v48", "v49", "v50", "v51", "v52", "v53", "v54", "v55", "v56", "v57", "v58", "v59", "v60", "v61", "v62",
          "v63", "v64", "v65", "v66", "v67", "v68", "v69", "v70", "v71", "v72", "v73", "v74", "v75", "v76", "v77", "v78", "v79", "v80", "v81", "v82", "v83",
          "v84", "v85", "v86", "v87", "v88", "v89", "v90", "v91", "v92", "v93", "v94", "v95", "v96", "v97", "v98", "v99", "v100", "v101", "v102", "v103",
          "v104", "v105", "v106", "v107", "v108", "v109", "v110", "v111", "v119", "v122", "v123", "v134", "v135", "v136");
    const u64 nonce = (u64)(u32)__builtin_amdgcn_readfirstlane(nv.x) | ((u64)(u32)__builtin_amdgcn_readfirstlane(nv.y) << 32);
    const u64 tag = (nonce + 1) & 0xffffffffffffull;
    __builtin_amdgcn_s_setprio(3);
#define PK8(a, o) (u32x4){pk2(a[o], a[o + 1]), pk2(a[o + 2], a[o + 3]), pk2(a[o + 4], a[o + 5]), pk2(a[o + 6], a[o + 7])}
    const int pr = (r & 19) | ((r & 4) << 1) | ((r & 8) >> 1);
    const u32 kvo = WS_KV + (hp & 7) * 524288 + tile * 2048 + (ISV ? (r >> 4) * 131072 + 1024 + h * 256 + (r & 15) * 16 : (h * 32 + pr) * 16);
    __builtin_amdgcn_raw_buffer_store_b128(PK8(a1, 0), wsr, kvo, 0, SC1);
    __builtin_amdgcn_raw_buffer_store_b128(PK8(a1, 8), wsr, kvo + (ISV ? 512 : 131072), 0, SC1);
    __builtin_amdgcn_raw_buffer_store_b128(PK8(a2, 0), wsr, kvo + 262144, 0, SC1);
    __builtin_amdgcn_raw_buffer_store_b128(PK8(a2, 8), wsr, kvo + 262144 + (ISV ? 512 : 131072), 0, SC1);
    SB();
    auto norms = [&](const f32x16& a) {
        float n0 = 0.f, n1 = 0.f;
#pragma unroll
        for (int i = 0; i < 8; ++i) { n0 = fmaf(a[i], a[i], n0); n1 = fmaf(a[8 + i], a[8 + i], n1); }
        auto t = __builtin_amdgcn_permlane32_swap(__float_as_uint(n0), __float_as_uint(n1), false, false);
        return __uint_as_float(t[0]) + __uint_as_float(t[1]);
    };
    u32 kb1 = 0, kb2 = 0;
    if (!ISV) {
        const u32 k1 = dpp_max<0x142>(row_max(__float_as_uint(norms(a1)))), k2 = dpp_max<0x142>(row_max(__float_as_uint(norms(a2))));
        kb1 = min((k1 >> 16) + 1, 0x7fc0u); kb2 = min((k2 >> 16) + 1, 0x7fc0u);
    }
    asm volatile("s_waitcnt vmcnt(0)" : "+v"(kb1), "+v"(kb2) :: "memory");
    if (r == 31) {
        __hip_atomic_store((GAS u64*)(ws + WS_KVFLAG + (hp * 64 + tile) * 64 + h * 8), (tag << 16) | kb1, RLX_AGENT);
        if (!ISV) __hip_atomic_store((GAS u64*)(ws + WS_KVFLAG + (hp * 64 + tile) * 64 + 16 + h * 8), (tag << 16) | kb2, RLX_AGENT);
    }
    a0 *= QSCALE;
    ST(u32x4, L_QX + tb * 1024 + lane * 16) = PK8(a0, 0);
    ST(u32x4, L_QX + 4096 + tb * 1024 + lane * 16) = PK8(a0, 8);
    ST(float, L_QN + h * 512 + (tb * 32 + r) * 4) = norms(a0);
    arrive(S_Q);
#undef PK8
    return nonce;
}

__device__ __forceinline__ u64 phase1_service(const Params& p, __amdgpu_buffer_rsrc_t wsr, char* ws, int lane, int tb, int hp, int qb, int d0, u32 pf,
                                              const u32 (&vx)[4], u32 vq, u32 vk) {
    u64 nonce;
    if (tb < 2) {
        const char* wsrc = p.wqkv + (tb * 32 * 3072 + hp * 64);
        const char* xsrc = p.x + (qb * 128 + tb * 64) * 1024;
        auto group = [&](int c, int g) {
            const int cc = (c + (hp >> 1)) & 7;
            if (g < 2) dma4(vx[0], vx[1], vx[2], vx[3], xsrc + (g * 32768 + cc * 128), (c & 3) * CHUNK + (tb * 2 + g) * 4096);
            else {
                const char* wsn = wsrc + (cc * 64 + (g - 2) * 16) * 3072;
                dma1(vq, wsn, (c & 3) * CHUNK + L_W + (tb * 2 + g - 2) * 3072); dma_next<1024>(vk, wsn); dma_next<2048>(vk + 8 * 3072 - 1024, wsn);
            }
        };
        u32x4 ud[4]; u32x2 ue[4];
        auto load_u = [&]() {
#pragma unroll
            for (int q = 0; q < 4; ++q) {
                ud[q] = __builtin_amdgcn_raw_buffer_load_b128(wsr, WS_UT + (d0 + tb) * 4096 + q * 1024 + lane * 16, 0, SC1);
                ue[q] = __builtin_amdgcn_raw_buffer_load_b64(wsr, WS_UT + (d0 + tb) * 4096 + max(q * 1024 + lane * 16 - 8, 0), 0, SC1);
            }
        };
        u64 uf;
        asm volatile("s_load_dwordx2 %0, %1, 0x0" : "=&s"(nonce) : "s"(ws + WS_NONCE));
        u32 landed = 0, last = 8;
        auto report = [&](u32 issued) {
            u32 sts;
            asm volatile("s_getreg_b32 %0, hwreg(HW_REG_IB_STS)" : "=s"(sts) :: "memory");
            if (issued >= last && issued - last >= ((sts & 15u) | ((sts >> 18) & 48u))) { post(S_LANDED, tb, ++landed); last += 14; }
        };
        auto slot_free = [&](u32 c, u32 issued) {
            for (;;) {
                u32x4 v = peek4(S_FREED);
                asm volatile("s_waitcnt lgkmcnt(0)" : "+v"(v) :: "memory");
                if (__builtin_amdgcn_readfirstlane(min(min(v.x, v.y), min(v.z, v.w))) >= c - 3) break;
                report(issued);
            }
        };
        group(1, 0); group(1, 1); WAIT_VM(8); report(16); group(1, 2); report(19); group(1, 3); report(22);
        group(2, 0); report(26); group(2, 1); report(30); group(2, 2); report(33); group(2, 3); report(36);
        group(3, 0); report(40); group(3, 1); report(44); group(3, 2); report(47); group(3, 3); report(50);
        asm volatile("s_waitcnt lgkmcnt(0)" : "+s"(nonce)); slot_free(4, 50); group(4, 0); report(54); group(4, 1); report(58); group(4, 2); report(61); group(4, 3); report(64);
        slot_free(5, 64); group(5, 0); report(68); group(5, 1); report(72); group(5, 2); report(75); group(5, 3); report(78);
        slot_free(6, 78); group(6, 0); report(82); group(6, 1); report(86); group(6, 2); report(89); group(6, 3); report(92); asm volatile("global_load_dwordx2 %0, %1, %2 sc1" : "=&v"(uf) : "v"(lane * 64), "s"(ws + WS_UFLAG + (d0 >> 5) * 4096));
        slot_free(7, 92); group(7, 0); report(96); group(7, 1); report(100); group(7, 2); report(103); group(7, 3); report(106);
        asm volatile("s_waitcnt vmcnt(14)" : "+v"(uf) :: "memory");
        const bool early = __all((uf >> 16) == ((nonce + 1) & 0xffffffffffffull));
        if (early) { __builtin_amdgcn_fence(__ATOMIC_ACQUIRE, "wavefront"); load_u(); SB(); }
        last += early ? 0 : 8;
        while (landed < 8) report(114);
        if (!early) { poll_tag(ws + WS_UFLAG + (d0 >> 5) * 4096 + lane * 64, true, (nonce + 1) & 0xffffffffffffull); load_u(); }
        SB();
        await4(S_FREED, 6, peek4(S_FREED)); LGKM0();
        if (lane < 16) ST(u32x4, L_UC + tb * UC_CH + (lane >> 2) * UC_COPY + (lane & 3) * 16) = (u32x4){0, 0, 0, 0};
#pragma unroll
        for (int q = 0; q < 4; ++q) {
            const u32 m2 = (q || lane) ? ue[q].x : 0u, m1 = (q || lane) ? ue[q].y : 0u, uo = L_UC + tb * UC_CH + 64 + q * 1024 + lane * 16;
            const u32 o0 = __builtin_amdgcn_alignbit(m1, m2, 16), o1 = __builtin_amdgcn_alignbit(ud[q].x, m1, 16), o2 = __builtin_amdgcn_alignbit(ud[q].y, ud[q].x, 16);
            const u32 o3 = __builtin_amdgcn_alignbit(ud[q].z, ud[q].y, 16), o4 = __builtin_amdgcn_alignbit(ud[q].w, ud[q].z, 16);
            ST(u32x4, uo) = ud[q];
            ST(u32x4, uo + UC_COPY) = (u32x4){o1, o2, o3, o4};
            ST(u32x4, uo + 2 * UC_COPY) = (u32x4){m1, ud[q].x, ud[q].y, ud[q].z};
            ST(u32x4, uo + 3 * UC_COPY) = (u32x4){o0, o1, o2, o3};
        }
        arrive(S_SVC);
    } else {
        const int ch = tb & 1, col = lane & 15, g = lane >> 4;
        u32 are, aim, bre, bim, cre, cim, dtw, dsw;
        LOAD_PARAMS(d0 + ch);
        asm volatile("s_load_dword %0, %3, 0x0\n\ts_load_dword %1, %4, 0x0\n\ts_load_dwordx2 %2, %5, 0x0\n\ts_barrier"
                     : "=&s"(dtw), "=&s"(dsw), "=&s"(nonce) : "s"(p.log_dt + d0 * 2), "s"(p.dskip + d0 * 2), "s"(ws + WS_NONCE) : "memory");
        u32 tap[8];
        auto taps = [&](int j0) {
#pragma unroll
            for (int j = j0; j < j0 + 4; ++j) {
                const float t2 = (float)(2 * (32 * (lane >> 2) + 31 - 8 * (lane & 3) + 512 * (j >> 2) - 2 * (j & 3)));
                const u32 rr = pk2(t2, t2 - 2.f);
                tap[j] = ((u32)bflo(rr) | ((u32)bfhi(rr) << 16)) + (L_KF + ch * KF_CH - 131072) * 0x10001u;
                asm volatile("" : "+v"(tap[j]));
            }
        };
        taps(0); taps(4);
        u32 ko[2][4];
#pragma unroll
        for (int aq = 0; aq < 2; ++aq) {
            const int l0 = aq ? (g == 0 ? 1024 : g == 1 ? 2048 : 2052) : (g < 2 ? 4 * g : 256 * g - 256);
            const int s1 = aq ? (g == 0 ? 512 : 2) : (g < 2 ? 2 : 4 * g - 4), s2 = (!aq && g == 3) ? 512 : 2 * s1;
#pragma unroll
            for (int i = 0; i < 4; ++i) { ko[aq][i] = L_KF + ch * KF_CH + (l0 + 8 * col) * 2 + (i & 1) * s1 + (i >> 1) * s2; asm volatile("" : "+v"(ko[aq][i])); }
        }
        asm volatile("s_waitcnt lgkmcnt(0)" : "+s"(dtw), "+s"(dsw), "+s"(nonce));
        float dt = (float)(__bf16)expf(ch ? bfhi(dtw) : bflo(dtw));
        const u32 t0 = L_TAB + ch * TAB_CH + lane * 4, ta = L_TAB + ch * TAB_CH + col * TAB_ROW + g * 16;
        f32x4 kq[4] = {};
        asm volatile("s_waitcnt vmcnt(0)" : "+v"(are), "+v"(aim), "+v"(bre), "+v"(bim), "+v"(cre), "+v"(cim), "+v"(pf), "+v"(dt));
        __builtin_amdgcn_s_setprio(2);
#pragma unroll 1
        for (int half = 0; half < 2; ++half) {
            auto put = [&](int row, u32 v) { ST(u32, t0 + row * TAB_ROW) = v; };
            table_a(dt, bflo(are), bflo(aim), bflo(bre), bflo(bim), bflo(cre), bflo(cim), put);
            table_b(dt, bflo(are), bflo(aim), put);
            LGKM0();
            u32x4 ra[2][4], rb[2][4];
#pragma unroll
            for (int ks = 0; ks < 4; ++ks)
#pragma unroll
                for (int i = 0; i < 2; ++i) { ra[i][ks] = LD(u32x4, ta + i * 16 * TAB_ROW + ks * 64); rb[i][ks] = LD(u32x4, ta + TAB_A + i * 16 * TAB_ROW + ks * 64); }
            SB();
#pragma unroll
            for (int ks = 0; ks < 4; ++ks)
#pragma unroll
                for (int i = 0; i < 4; ++i) kq[i] = mfma16(ra[i >> 1][ks], rb[i & 1][ks], kq[i]);
            if (half == 0) { asm volatile("" : "+v"(kq[0]), "+v"(kq[1]), "+v"(kq[2]), "+v"(kq[3])); }
            are >>= 16; aim >>= 16; bre >>= 16; bim >>= 16; cre >>= 16; cim >>= 16;
        }
        if (lane == 0) kq[0][0] += ch ? bfhi(dsw) : bflo(dsw);
#pragma unroll
        for (int i = 0; i < 4; ++i) {
            const u32 e01 = pk2(kq[i][0], kq[i][1]), e23 = pk2(kq[i][2], kq[i][3]), o = (i & 1) * 256;
            ST(unsigned short, ko[i >> 1][0] + o) = (unsigned short)e01; ST(unsigned short, ko[i >> 1][1] + o) = (unsigned short)(e01 >> 16);
            ST(unsigned short, ko[i >> 1][2] + o) = (unsigned short)e23; ST(unsigned short, ko[i >> 1][3] + o) = (unsigned short)(e23 >> 16);
        }
        LGKM0();
        u32x4 kv[4];
#pragma unroll
        for (int j = 0; j < 8; ++j) {
            const u16x2 e = {LD(unsigned short, 131072 + (tap[j] & 0xffffu)), LD(unsigned short, 131072 + (tap[j] >> 16))};
            kv[j >> 2][j & 3] = __builtin_bit_cast(u32, e);
        }
#pragma unroll
        for (int q = 2; q < 4; ++q) {
            const u32 o = L_KF + ch * KF_CH + q * 1024 + (lane >> 2) * 64 + 48 - (lane & 3) * 16;
            const u32 lo = LD(unsigned short, o), hi = LD(unsigned short, o + 16);
            kv[q] = (u32x4){hi * 0x10001u, hi | ((lane & 1 ? lo : hi) << 16), lo * 0x10001u, lo * 0x10001u};
        }
        const u32 kb = L_KB + ch * KB_CH + (lane >> 2) * 80 + (lane & 3) * 16;
        ST(u32x4, kb) = (u32x4){0, 0, 0, 0}; ST(u32x4, kb + 1280) = (u32x4){0, 0, 0, 0};
#pragma unroll
        for (int q = 0; q < 4; ++q) ST(u32x4, kb + 2560 + q * 1280) = kv[q];
        arrive(S_SVC);
    }
    await(S_SVC, 4);
    __builtin_amdgcn_s_setprio(2);
    return nonce;
}

__device__ __forceinline__ void conv_wave(__amdgpu_buffer_rsrc_t wsr, char* ws, int lane, int tb, int d0, u64 tag) {
    const int ch = tb & 1, mh = tb >> 1, r = lane & 31, h = lane >> 5, c = (31 - r) & 3;
    u32 ua = L_UC + ch * UC_CH + c * UC_COPY + (mh * 512 + r + 1 + c + 8 * h) * 2;
    u32 ka = L_KB + ch * KB_CH + (30 + r - mh * 16) * 80 + h * 16;
    f32x16 y0, y1;
    asm volatile(
        "ds_read_b64 v[32:33], v96 offset:0\nds_read_b64 v[34:35], v96 offset:8\nds_read_b128 v[36:39], v97 offset:160\n"
        "ds_read_b128 v[40:43], v97 offset:2720\nds_read_b64 v[44:45], v96 offset:2048\nds_read_b64 v[46:47], v96 offset:2056\n"
        "ds_read_b64 v[48:49], v96 offset:32\nds_read_b64 v[50:51], v96 offset:40\nds_read_b128 v[52:55], v97 offset:192\n"
        "ds_read_b128 v[56:59], v97 offset:2752\nds_read_b64 v[60:61], v96 offset:2080\nds_read_b64 v[62:63], v96 offset:2088\nv_mov_b64 v[0:1], 0\n"
        "v_mov_b64 v[2:3], 0\nv_mov_b64 v[4:5], 0\nv_mov_b64 v[6:7], 0\nv_mov_b64 v[8:9], 0\nv_mov_b64 v[10:11], 0\nv_mov_b64 v[12:13], 0\n"
        "v_mov_b64 v[14:15], 0\nv_mov_b64 v[16:17], 0\nv_mov_b64 v[18:19], 0\nv_mov_b64 v[20:21], 0\nv_mov_b64 v[22:23], 0\nv_mov_b64 v[24:25], 0\n"
        "v_mov_b64 v[26:27], 0\nv_mov_b64 v[28:29], 0\nv_mov_b64 v[30:31], 0\ns_mov_b32 s44, 8\n.p2align 3\n1:\ns_waitcnt lgkmcnt(9)\n"
        "v_mfma_f32_32x32x16_bf16 v[0:15], v[32:35], v[36:39], v[0:15]\nds_read_b64 v[64:65], v96 offset:64\nds_read_b64 v[66:67], v96 offset:72\n"
        "s_waitcnt lgkmcnt(10)\nv_mfma_f32_32x32x16_bf16 v[16:31], v[32:35], v[40:43], v[16:31]\nds_read_b128 v[68:71], v97 offset:80\n"
        "ds_read_b128 v[72:75], v97 offset:2640\ns_waitcnt lgkmcnt(10)\nv_mfma_f32_32x32x16_bf16 v[16:31], v[44:47], v[36:39], v[16:31]\n"
        "ds_read_b64 v[76:77], v96 offset:2112\nds_read_b64 v[78:79], v96 offset:2120\ns_waitcnt lgkmcnt(9)\n"
        "v_mfma_f32_32x32x16_bf16 v[0:15], v[48:51], v[52:55], v[0:15]\nds_read_b64 v[80:81], v96 offset:96\nds_read_b64 v[82:83], v96 offset:104\n"
        "s_waitcnt lgkmcnt(10)\nv_mfma_f32_32x32x16_bf16 v[16:31], v[48:51], v[56:59], v[16:31]\nds_read_b128 v[84:87], v97 offset:112\n"
        "ds_read_b128 v[88:91], v97 offset:2672\ns_waitcnt lgkmcnt(10)\nv_mfma_f32_32x32x16_bf16 v[16:31], v[60:63], v[52:55], v[16:31]\n"
        "ds_read_b64 v[92:93], v96 offset:2144\nds_read_b64 v[94:95], v96 offset:2152\ns_waitcnt lgkmcnt(9)\n"
        "v_mfma_f32_32x32x16_bf16 v[0:15], v[64:67], v[68:71], v[0:15]\nds_read_b64 v[32:33], v96 offset:128\nds_read_b64 v[34:35], v96 offset:136\n"
        "s_waitcnt lgkmcnt(10)\nv_mfma_f32_32x32x16_bf16 v[16:31], v[64:67], v[72:75], v[16:31]\nds_read_b128 v[36:39], v97 offset:0\n"
        "ds_read_b128 v[40:43], v97 offset:2560\ns_waitcnt lgkmcnt(10)\nv_mfma_f32_32x32x16_bf16 v[16:31], v[76:79], v[68:71], v[16:31]\n"
        "ds_read_b64 v[44:45], v96 offset:2176\nds_read_b64 v[46:47], v96 offset:2184\ns_waitcnt lgkmcnt(9)\n"
        "v_mfma_f32_32x32x16_bf16 v[0:15], v[80:83], v[84:87], v[0:15]\nds_read_b64 v[48:49], v96 offset:160\nds_read_b64 v[50:51], v96 offset:168\n"
        "s_waitcnt lgkmcnt(10)\nv_mfma_f32_32x32x16_bf16 v[16:31], v[80:83], v[88:91], v[16:31]\nds_read_b128 v[52:55], v97 offset:32\n"
        "ds_read_b128 v[56:59], v97 offset:2592\ns_waitcnt lgkmcnt(10)\nv_mfma_f32_32x32x16_bf16 v[16:31], v[92:95], v[84:87], v[16:31]\n"
        "ds_read_b64 v[60:61], v96 offset:2208\nds_read_b64 v[62:63], v96 offset:2216\nv_add_u32 v96, 0x80, v96\nv_subrev_u32 v97, 0xa0, v97\n"
        "s_sub_u32 s44, s44, 1\ns_cmp_lg_u32 s44, 0\ns_cbranch_scc1 1b\ns_waitcnt lgkmcnt(0)\ns_nop 7\n"
        : "=&{v[0:15]}"(y0), "=&{v[16:31]}"(y1), "+{v96}"(ua), "+{v97}"(ka) :
        : "memory", "scc", "s44", "v32", "v33", "v34", "v35", "v36", "v37", "v38", "v39", "v40", "v41", "v42", "v43", "v44", "v45", "v46", "v47", "v48",
          "v49", "v50", "v51", "v52", "v53", "v54", "v55", "v56", "v57", "v58", "v59", "v60", "v61", "v62", "v63", "v64", "v65", "v66", "v67", "v68", "v69",
          "v70", "v71", "v72", "v73", "v74", "v75", "v76", "v77", "v78", "v79", "v80", "v81", "v82", "v83", "v84", "v85", "v86", "v87", "v88", "v89", "v90",
          "v91", "v92", "v93", "v94", "v95");
    auto finish = [&](f32x16 y, const f32x16& give) {
#pragma unroll
        for (int i = 0; i < 4; ++i) ST(f32x4, (tb < 3 ? L_CX + tb * 4096 : L_KF) + i * 1024 + lane * 16) = (f32x4){give[4 * i], give[4 * i + 1], give[4 * i + 2], give[4 * i + 3]};
        arrive(S_PAIR + ch * 4); await(S_PAIR + ch * 4, 2);
#pragma unroll
        for (int i = 0; i < 4; ++i) {
            const f32x4 o = LD(f32x4, (tb != 1 ? L_CX + (tb ^ 2) * 4096 : L_KF) + i * 1024 + lane * 16);
#pragma unroll
            for (int j = 0; j < 4; ++j) y[4 * i + j] += o[j];
        }
        const u32 sc = tb != 1 ? L_CX + (tb ^ 2) * 4096 : L_KF;
        asm volatile("" ::: "memory");
#pragma unroll
        for (int i = 0; i < 4; ++i) ST(u32x2, sc + r * 64 + i * 16 + h * 8) = (u32x2){pk2(y[4 * i], y[4 * i + 1]), pk2(y[4 * i + 2], y[4 * i + 3])};
        asm volatile("" ::: "memory");
#pragma unroll
        for (int i = 0; i < 2; ++i)
            __builtin_amdgcn_raw_buffer_store_b128(LD(u32x4, sc + i * 1024 + lane * 16), wsr, WS_YT + ((d0 + ch) * 2048 + mh * 1024) * 2 + i * 1024 + lane * 16, 0, SC1);
        asm volatile("" ::: "memory");
    };
    if (mh) finish(y1, y0); else finish(y0, y1);
}

__global__ __launch_bounds__(512, 2) void s4_mha_mix(Params p) {
    __shared__ __attribute__((aligned(16))) char smem[LDS_BYTES];
    asm volatile("" :: "s"((u32)(uintptr_t)smem));
    const int lane = threadIdx.x & 63, w = __builtin_amdgcn_readfirstlane(threadIdx.x >> 6);
    const int b = blockIdx.x, xg = b & 7, jb = b >> 3;
    const int hp = 8 * (xg & 1) + (jb & 7), qb = 4 * (xg >> 1) + (jb >> 3), d0 = 64 * xg + 2 * jb;
    const int tb = w & 3, a = w >> 2, r = lane & 31, h = lane >> 5;
    u32 pf = 0;
    {
        u64 pc0;
        asm volatile("s_getpc_b64 %0" : "=s"(pc0));
        if (w >= 6) asm volatile("global_load_dword %0, %1, %2" : "=&v"(pf) : "v"(min((((jb & 3) * 2 + (w & 1)) * 64 + lane) * 64, CODE_BYTES)), "s"(pc0));
    }
    if (threadIdx.x < 24) ST(u32, L_SYNC + threadIdx.x * 4) = (threadIdx.x & ~1u) == S_LANDED / 4 + 2 ? 8 : 0;
    u32 vx[4], vq = w_off_q(lane), vk = w_off_kv(lane, hp);
#pragma unroll
    for (int i = 0; i < 4; ++i) vx[i] = (7 * i + (lane >> 3)) * 1024 + (((lane & 7) ^ ((4 * i + (lane >> 4)) & 7)) << 4);
    asm volatile("" : "+v"(vx[0]), "+v"(vx[1]), "+v"(vx[2]), "+v"(vx[3]), "+v"(vq), "+v"(vk));
    asm volatile("" :: "s"(p.x), "s"(p.a_re), "s"(p.c_im), "s"(p.log_dt), "s"(p.wqkv), "s"(p.ws));
    if (w < 6) {
        if (w < 4) {
            const char* wsn = p.wqkv + (tb * 16 * 3072 + hp * 64 + (hp >> 1) * (64 * 3072));
            dma1(vq, wsn, L_W + tb * 3072); dma_next<1024>(vk, wsn); dma_next<2048>(vk + 8 * 3072 - 1024, wsn);
        } else {
#pragma unroll
            for (int g = 0; g < 2; ++g) dma4(vx[0], vx[1], vx[2], vx[3], p.x + ((qb * 128 + tb * 64 + g * 32) * 1024 + (hp >> 1) * 128), (tb * 2 + g) * 4096);
        }
        BAR_LDS();
    }
    char* ws = p.ws;
    const __amdgpu_buffer_rsrc_t wsr = __builtin_amdgcn_make_buffer_rsrc(ws, 0, WS_END, 0x00020000);
    const u64 nonce = a == 0 ? phase1_mfma(p, wsr, ws, lane, tb, hp, qb) : phase1_service(p, wsr, ws, lane, tb, hp, qb, d0, pf, vx, vq, vk);
    const u64 tag = (nonce + 1) & 0xffffffffffffull;
    if (a == 1) { conv_wave(wsr, ws, lane, tb, d0, tag); }

    {
        await(S_Q, 4);
        u32x4 q0 = LD(u32x4, L_QX + (a * 4 + ((tb + 1) & 3)) * 1024 + lane * 16), q1 = LD(u32x4, L_QX + (a * 4 + (tb ^ 2)) * 1024 + lane * 16);
        u32x4 q2 = LD(u32x4, L_QX + (a * 4 + ((tb + 3) & 3)) * 1024 + lane * 16), q3 = LD(u32x4, L_QX + (a * 4 + tb) * 1024 + lane * 16);
        const u32 rec0 = a * L_TAB + ((tb + 1) & 3) * 9216 + lane * 16, rec1 = a * L_TAB + (tb ^ 2) * 9216 + 3072 + lane * 16;
        const u32 rec2 = a * L_TAB + ((tb + 3) & 3) * 9216 + 6144 + lane * 16;
        const u32x2 qn2 = LD(u32x2, L_QN + a * 512 + lane * 8);
        float qn = __uint_as_float(max(qn2.x, qn2.y));
        asm volatile("" : "+v"(qn), "+v"(q0), "+v"(q1), "+v"(q2), "+v"(q3));
        f32x16 o0, o1, o2, o3;
        u32x8 va, vb, vc, vd;
        const u32 one = (r & 27) == 16 ? 0x3f803f80u : 0u;
        asm volatile(
            "v_mov_b32 v116, v163\nv_mov_b32 v117, v163\nv_mov_b64 v[118:119], v[116:117]\nv_mov_b64 v[120:121], v[116:117]\nv_mov_b64 v[122:123], v[116:117]\n"
            "v_mov_b64 v[128:129], v[116:117]\nv_mov_b64 v[130:131], v[116:117]\nv_mov_b64 v[132:133], v[116:117]\nv_mov_b64 v[134:135], v[116:117]\n"
            "v_mov_b64 v[140:141], v[116:117]\nv_mov_b64 v[142:143], v[116:117]\nv_mov_b64 v[144:145], v[116:117]\nv_mov_b64 v[146:147], v[116:117]\n"
            "v_mov_b64 v[152:153], v[116:117]\nv_mov_b64 v[154:155], v[116:117]\nv_mov_b64 v[156:157], v[116:117]\nv_mov_b64 v[158:159], v[116:117]\n"
            : "=&{v[116:123]}"(va), "=&{v[128:135]}"(vb),
              "=&{v[140:147]}"(vc), "=&{v[152:159]}"(vd) : "{v163}"(one));
        float m3 = 0.f;
        const u32 kvo = WS_KV + (hp * 2 + a) * 131072 + tb * 32768;
        const u64 fl = poll_tag(ws + WS_KVFLAG + (((hp >> 1) + (lane & 16 ? 8 : 0)) * 64 + tb * 16 + (lane & 15)) * 64 + (lane & 16 ? 0 : (hp & 1) * 16 + a * 8), lane < 32, tag);
        if (a == 1 && lane == 0) __hip_atomic_store((GAS u64*)(ws + WS_YFLAG + ((d0 + (tb & 1)) * 2 + (tb >> 1)) * 64), tag << 16, RLX_AGENT);
        arrive(S_READY);
        const bool safe = __all(qn * __uint_as_float(__builtin_amdgcn_readlane(row_max((u32)fl << 16), 15)) <= SAFE2);
        if (__builtin_expect(safe, 1)) {
            u64 base = (u64)(uintptr_t)(ws + kvo);
            u32 prio_role = a;
            asm volatile(
                "s_add_u32 s40, s40, 4096\ns_addc_u32 s41, s41, 0\nglobal_load_dwordx4 v[112:115], v161, s[40:41] offset:-4096 sc1\ns_mov_b64 exec, s[42:43]\n"
                "global_load_dwordx4 v[116:119], v162, s[40:41] offset:-3072 sc1\nglobal_load_dwordx4 v[120:123], v162, s[40:41] offset:-2560 sc1\ns_mov_b64 exec, -1\n"
                "global_load_dwordx4 v[124:127], v161, s[40:41] offset:-2048 sc1\ns_mov_b64 exec, s[42:43]\n"
                "global_load_dwordx4 v[128:131], v162, s[40:41] offset:-1024 sc1\nglobal_load_dwordx4 v[132:135], v162, s[40:41] offset:-512 sc1\ns_mov_b64 exec, -1\n"
                "global_load_dwordx4 v[136:139], v161, s[40:41] offset:0 sc1\ns_mov_b64 exec, s[42:43]\n"
                "global_load_dwordx4 v[140:143], v162, s[40:41] offset:1024 sc1\nglobal_load_dwordx4 v[144:147], v162, s[40:41] offset:1536 sc1\ns_mov_b64 exec, -1\n"
                "s_add_u32 s40, s40, 6144\ns_addc_u32 s41, s41, 0\ns_mov_b32 s44, 3\nv_mov_b64 v[80:81], 0\nv_mov_b64 v[82:83], 0\ns_nop 1\n"
                "v_mfma_f32_32x32x16_bf16 v[96:111], v[80:83], v[80:83], 0\nv_mfma_f32_32x32x16_bf16 v[164:179], v[80:83], v[80:83], 0\n"
                "v_mfma_f32_32x32x16_bf16 v[180:195], v[80:83], v[80:83], 0\nv_mfma_f32_32x32x16_bf16 v[196:211], v[80:83], v[80:83], 0\n"
                "v_mfma_f32_32x32x16_bf16 v[212:227], v[80:83], v[80:83], 0\ns_waitcnt vmcnt(6)\nv_mfma_f32_32x32x16_bf16 v[80:95], v[112:115], v[64:67], 0\n"
                "s_setprio 0\ns_cmp_eq_u32 s45, 0\ns_cbranch_scc1 5f\ns_setprio 1\n5:\ns_nop 10\n.p2align 3\n1:\ns_cmp_lg_u32 s44, 1\ns_cbranch_scc1 6f\ns_setprio 0\n"
                "6:\nv_exp_f32 v80, v80\nv_exp_f32 v81, v81\nv_exp_f32 v82, v82\nv_exp_f32 v83, v83\n"
                "v_smfmac_f32_32x32x32_bf16 v[212:227], v[156:159], v[104:111], v160\nglobal_load_dwordx4 v[148:151], v161, s[40:41] offset:-4096 sc1\n"
                "s_mov_b64 exec, s[42:43]\nglobal_load_dwordx4 v[152:155], v162, s[40:41] offset:-3072 sc1\n"
                "global_load_dwordx4 v[156:159], v162, s[40:41] offset:-2560 sc1\ns_mov_b64 exec, -1\nv_exp_f32 v84, v84\nv_exp_f32 v85, v85\nv_exp_f32 v86, v86\n"
                "v_mfma_f32_32x32x16_bf16 v[96:111], v[112:115], v[68:71], 0\nv_exp_f32 v87, v87\nv_exp_f32 v88, v88\nv_exp_f32 v89, v89\nv_exp_f32 v90, v90\n"
                "v_exp_f32 v91, v91\nv_smfmac_f32_32x32x32_bf16 v[164:179], v[116:119], v[80:87], v160\nv_exp_f32 v92, v92\nv_exp_f32 v93, v93\nv_exp_f32 v94, v94\n"
                "v_exp_f32 v95, v95\nv_exp_f32 v96, v96\nv_exp_f32 v97, v97\nv_exp_f32 v98, v98\nv_exp_f32 v99, v99\n"
                "v_smfmac_f32_32x32x32_bf16 v[164:179], v[120:123], v[88:95], v160\nv_exp_f32 v100, v100\nv_exp_f32 v101, v101\nv_exp_f32 v102, v102\n"
                "v_mfma_f32_32x32x16_bf16 v[80:95], v[112:115], v[72:75], 0\nv_exp_f32 v103, v103\nv_exp_f32 v104, v104\nv_exp_f32 v105, v105\nv_exp_f32 v106, v106\n"
                "v_exp_f32 v107, v107\nv_smfmac_f32_32x32x32_bf16 v[180:195], v[116:119], v[96:103], v160\nv_exp_f32 v108, v108\nv_exp_f32 v109, v109\n"
                "v_exp_f32 v110, v110\nv_exp_f32 v111, v111\nv_exp_f32 v80, v80\nv_exp_f32 v81, v81\nv_exp_f32 v82, v82\nv_exp_f32 v83, v83\n"
                "v_smfmac_f32_32x32x32_bf16 v[180:195], v[120:123], v[104:111], v160\nv_exp_f32 v84, v84\nv_exp_f32 v85, v85\nv_exp_f32 v86, v86\n"
                "v_mfma_f32_32x32x16_bf16 v[96:111], v[112:115], v[76:79], 0\nv_exp_f32 v87, v87\nv_exp_f32 v88, v88\nv_exp_f32 v89, v89\nv_exp_f32 v90, v90\n"
                "v_exp_f32 v91, v91\nv_smfmac_f32_32x32x32_bf16 v[196:211], v[116:119], v[80:87], v160\nv_exp_f32 v92, v92\nv_exp_f32 v93, v93\nv_exp_f32 v94, v94\n"
                "v_exp_f32 v95, v95\nv_exp_f32 v96, v96\nv_exp_f32 v97, v97\nv_exp_f32 v98, v98\nv_exp_f32 v99, v99\n"
                "v_smfmac_f32_32x32x32_bf16 v[196:211], v[120:123], v[88:95], v160\nv_exp_f32 v100, v100\nv_exp_f32 v101, v101\nv_exp_f32 v102, v102\n"
                "s_waitcnt vmcnt(6)\nv_mfma_f32_32x32x16_bf16 v[80:95], v[124:127], v[64:67], 0\nv_exp_f32 v103, v103\nv_exp_f32 v104, v104\nv_exp_f32 v105, v105\n"
                "v_exp_f32 v106, v106\nv_exp_f32 v107, v107\nv_smfmac_f32_32x32x32_bf16 v[212:227], v[116:119], v[96:103], v160\nv_exp_f32 v108, v108\n"
                "v_exp_f32 v109, v109\nv_exp_f32 v110, v110\nv_exp_f32 v111, v111\nv_exp_f32 v80, v80\nv_exp_f32 v81, v81\nv_exp_f32 v82, v82\nv_exp_f32 v83, v83\n"
                "v_smfmac_f32_32x32x32_bf16 v[212:227], v[120:123], v[104:111], v160\nglobal_load_dwordx4 v[112:115], v161, s[40:41] offset:-2048 sc1\n"
                "s_mov_b64 exec, s[42:43]\nglobal_load_dwordx4 v[116:119], v162, s[40:41] offset:-1024 sc1\n"
                "global_load_dwordx4 v[120:123], v162, s[40:41] offset:-512 sc1\ns_mov_b64 exec, -1\nv_exp_f32 v84, v84\nv_exp_f32 v85, v85\nv_exp_f32 v86, v86\n"
                "v_mfma_f32_32x32x16_bf16 v[96:111], v[124:127], v[68:71], 0\nv_exp_f32 v87, v87\nv_exp_f32 v88, v88\nv_exp_f32 v89, v89\nv_exp_f32 v90, v90\n"
                "v_exp_f32 v91, v91\nv_smfmac_f32_32x32x32_bf16 v[164:179], v[128:131], v[80:87], v160\nv_exp_f32 v92, v92\nv_exp_f32 v93, v93\nv_exp_f32 v94, v94\n"
                "v_exp_f32 v95, v95\nv_exp_f32 v96, v96\nv_exp_f32 v97, v97\nv_exp_f32 v98, v98\nv_exp_f32 v99, v99\n"
                "v_smfmac_f32_32x32x32_bf16 v[164:179], v[132:135], v[88:95], v160\nv_exp_f32 v100, v100\nv_exp_f32 v101, v101\nv_exp_f32 v102, v102\n"
                "v_mfma_f32_32x32x16_bf16 v[80:95], v[124:127], v[72:75], 0\nv_exp_f32 v103, v103\nv_exp_f32 v104, v104\nv_exp_f32 v105, v105\nv_exp_f32 v106, v106\n"
                "v_exp_f32 v107, v107\nv_smfmac_f32_32x32x32_bf16 v[180:195], v[128:131], v[96:103], v160\nv_exp_f32 v108, v108\nv_exp_f32 v109, v109\n"
                "v_exp_f32 v110, v110\nv_exp_f32 v111, v111\nv_exp_f32 v80, v80\nv_exp_f32 v81, v81\nv_exp_f32 v82, v82\nv_exp_f32 v83, v83\n"
                "v_smfmac_f32_32x32x32_bf16 v[180:195], v[132:135], v[104:111], v160\nv_exp_f32 v84, v84\nv_exp_f32 v85, v85\nv_exp_f32 v86, v86\n"
                "v_mfma_f32_32x32x16_bf16 v[96:111], v[124:127], v[76:79], 0\nv_exp_f32 v87, v87\nv_exp_f32 v88, v88\nv_exp_f32 v89, v89\nv_exp_f32 v90, v90\n"
                "v_exp_f32 v91, v91\nv_smfmac_f32_32x32x32_bf16 v[196:211], v[128:131], v[80:87], v160\nv_exp_f32 v92, v92\nv_exp_f32 v93, v93\nv_exp_f32 v94, v94\n"
                "v_exp_f32 v95, v95\nv_exp_f32 v96, v96\nv_exp_f32 v97, v97\nv_exp_f32 v98, v98\nv_exp_f32 v99, v99\n"
                "v_smfmac_f32_32x32x32_bf16 v[196:211], v[132:135], v[88:95], v160\nv_exp_f32 v100, v100\nv_exp_f32 v101, v101\nv_exp_f32 v102, v102\n"
                "s_waitcnt vmcnt(6)\nv_mfma_f32_32x32x16_bf16 v[80:95], v[136:139], v[64:67], 0\nv_exp_f32 v103, v103\nv_exp_f32 v104, v104\nv_exp_f32 v105, v105\n"
                "v_exp_f32 v106, v106\nv_exp_f32 v107, v107\nv_smfmac_f32_32x32x32_bf16 v[212:227], v[128:131], v[96:103], v160\nv_exp_f32 v108, v108\n"
                "v_exp_f32 v109, v109\nv_exp_f32 v110, v110\nv_exp_f32 v111, v111\nv_exp_f32 v80, v80\nv_exp_f32 v81, v81\nv_exp_f32 v82, v82\nv_exp_f32 v83, v83\n"
                "v_smfmac_f32_32x32x32_bf16 v[212:227], v[132:135], v[104:111], v160\nglobal_load_dwordx4 v[124:127], v161, s[40:41] offset:0 sc1\n"
                "s_mov_b64 exec, s[42:43]\nglobal_load_dwordx4 v[128:131], v162, s[40:41] offset:1024 sc1\n"
                "global_load_dwordx4 v[132:135], v162, s[40:41] offset:1536 sc1\ns_mov_b64 exec, -1\nv_exp_f32 v84, v84\nv_exp_f32 v85, v85\nv_exp_f32 v86, v86\n"
                "v_mfma_f32_32x32x16_bf16 v[96:111], v[136:139], v[68:71], 0\nv_exp_f32 v87, v87\nv_exp_f32 v88, v88\nv_exp_f32 v89, v89\nv_exp_f32 v90, v90\n"
                "v_exp_f32 v91, v91\nv_smfmac_f32_32x32x32_bf16 v[164:179], v[140:143], v[80:87], v160\nv_exp_f32 v92, v92\nv_exp_f32 v93, v93\nv_exp_f32 v94, v94\n"
                "v_exp_f32 v95, v95\nv_exp_f32 v96, v96\nv_exp_f32 v97, v97\nv_exp_f32 v98, v98\nv_exp_f32 v99, v99\n"
                "v_smfmac_f32_32x32x32_bf16 v[164:179], v[144:147], v[88:95], v160\nv_exp_f32 v100, v100\nv_exp_f32 v101, v101\nv_exp_f32 v102, v102\n"
                "v_mfma_f32_32x32x16_bf16 v[80:95], v[136:139], v[72:75], 0\nv_exp_f32 v103, v103\nv_exp_f32 v104, v104\nv_exp_f32 v105, v105\nv_exp_f32 v106, v106\n"
                "v_exp_f32 v107, v107\nv_smfmac_f32_32x32x32_bf16 v[180:195], v[140:143], v[96:103], v160\nv_exp_f32 v108, v108\nv_exp_f32 v109, v109\n"
                "v_exp_f32 v110, v110\nv_exp_f32 v111, v111\nv_exp_f32 v80, v80\nv_exp_f32 v81, v81\nv_exp_f32 v82, v82\nv_exp_f32 v83, v83\n"
                "v_smfmac_f32_32x32x32_bf16 v[180:195], v[144:147], v[104:111], v160\nv_exp_f32 v84, v84\nv_exp_f32 v85, v85\nv_exp_f32 v86, v86\n"
                "v_mfma_f32_32x32x16_bf16 v[96:111], v[136:139], v[76:79], 0\nv_exp_f32 v87, v87\nv_exp_f32 v88, v88\nv_exp_f32 v89, v89\nv_exp_f32 v90, v90\n"
                "v_exp_f32 v91, v91\nv_smfmac_f32_32x32x32_bf16 v[196:211], v[140:143], v[80:87], v160\nv_exp_f32 v92, v92\nv_exp_f32 v93, v93\nv_exp_f32 v94, v94\n"
                "v_exp_f32 v95, v95\nv_exp_f32 v96, v96\nv_exp_f32 v97, v97\nv_exp_f32 v98, v98\nv_exp_f32 v99, v99\n"
                "v_smfmac_f32_32x32x32_bf16 v[196:211], v[144:147], v[88:95], v160\nv_exp_f32 v100, v100\nv_exp_f32 v101, v101\nv_exp_f32 v102, v102\n"
                "s_waitcnt vmcnt(6)\nv_mfma_f32_32x32x16_bf16 v[80:95], v[148:151], v[64:67], 0\nv_exp_f32 v103, v103\nv_exp_f32 v104, v104\nv_exp_f32 v105, v105\n"
                "v_exp_f32 v106, v106\nv_exp_f32 v107, v107\nv_smfmac_f32_32x32x32_bf16 v[212:227], v[140:143], v[96:103], v160\nv_exp_f32 v108, v108\n"
                "v_exp_f32 v109, v109\nv_exp_f32 v110, v110\nv_exp_f32 v111, v111\nv_exp_f32 v80, v80\nv_exp_f32 v81, v81\nv_exp_f32 v82, v82\nv_exp_f32 v83, v83\n"
                "v_smfmac_f32_32x32x32_bf16 v[212:227], v[144:147], v[104:111], v160\nglobal_load_dwordx4 v[136:139], v161, s[40:41] offset:2048 sc1\n"
                "s_mov_b64 exec, s[42:43]\nglobal_load_dwordx4 v[140:143], v162, s[40:41] offset:3072 sc1\n"
                "global_load_dwordx4 v[144:147], v162, s[40:41] offset:3584 sc1\ns_mov_b64 exec, -1\nv_exp_f32 v84, v84\nv_exp_f32 v85, v85\nv_exp_f32 v86, v86\n"
                "v_mfma_f32_32x32x16_bf16 v[96:111], v[148:151], v[68:71], 0\nv_exp_f32 v87, v87\nv_exp_f32 v88, v88\nv_exp_f32 v89, v89\nv_exp_f32 v90, v90\n"
                "v_exp_f32 v91, v91\nv_smfmac_f32_32x32x32_bf16 v[164:179], v[152:155], v[80:87], v160\nv_exp_f32 v92, v92\nv_exp_f32 v93, v93\nv_exp_f32 v94, v94\n"
                "v_exp_f32 v95, v95\nv_exp_f32 v96, v96\nv_exp_f32 v97, v97\nv_exp_f32 v98, v98\nv_exp_f32 v99, v99\n"
                "v_smfmac_f32_32x32x32_bf16 v[164:179], v[156:159], v[88:95], v160\nv_exp_f32 v100, v100\nv_exp_f32 v101, v101\nv_exp_f32 v102, v102\n"
                "v_mfma_f32_32x32x16_bf16 v[80:95], v[148:151], v[72:75], 0\nv_exp_f32 v103, v103\nv_exp_f32 v104, v104\nv_exp_f32 v105, v105\nv_exp_f32 v106, v106\n"
                "v_exp_f32 v107, v107\nv_smfmac_f32_32x32x32_bf16 v[180:195], v[152:155], v[96:103], v160\nv_exp_f32 v108, v108\nv_exp_f32 v109, v109\n"
                "v_exp_f32 v110, v110\nv_exp_f32 v111, v111\nv_exp_f32 v80, v80\nv_exp_f32 v81, v81\nv_exp_f32 v82, v82\nv_exp_f32 v83, v83\n"
                "v_smfmac_f32_32x32x32_bf16 v[180:195], v[156:159], v[104:111], v160\nv_exp_f32 v84, v84\nv_exp_f32 v85, v85\nv_exp_f32 v86, v86\n"
                "v_mfma_f32_32x32x16_bf16 v[96:111], v[148:151], v[76:79], 0\nv_exp_f32 v87, v87\nv_exp_f32 v88, v88\nv_exp_f32 v89, v89\nv_exp_f32 v90, v90\n"
                "v_exp_f32 v91, v91\nv_smfmac_f32_32x32x32_bf16 v[196:211], v[152:155], v[80:87], v160\nv_exp_f32 v92, v92\nv_exp_f32 v93, v93\nv_exp_f32 v94, v94\n"
                "v_exp_f32 v95, v95\nv_exp_f32 v96, v96\nv_exp_f32 v97, v97\nv_exp_f32 v98, v98\nv_exp_f32 v99, v99\n"
                "v_smfmac_f32_32x32x32_bf16 v[196:211], v[156:159], v[88:95], v160\nv_exp_f32 v100, v100\nv_exp_f32 v101, v101\nv_exp_f32 v102, v102\n"
                "s_waitcnt vmcnt(6)\nv_mfma_f32_32x32x16_bf16 v[80:95], v[112:115], v[64:67], 0\nv_exp_f32 v103, v103\nv_exp_f32 v104, v104\nv_exp_f32 v105, v105\n"
                "v_exp_f32 v106, v106\nv_exp_f32 v107, v107\nv_smfmac_f32_32x32x32_bf16 v[212:227], v[152:155], v[96:103], v160\nv_exp_f32 v108, v108\n"
                "v_exp_f32 v109, v109\nv_exp_f32 v110, v110\nv_exp_f32 v111, v111\ns_add_u32 s40, s40, 8192\ns_addc_u32 s41, s41, 0\ns_sub_u32 s44, s44, 1\n"
                "s_cmp_lg_u32 s44, 0\ns_cbranch_scc1 1b\nv_exp_f32 v80, v80\nv_exp_f32 v81, v81\nv_exp_f32 v82, v82\nv_exp_f32 v83, v83\n"
                "v_smfmac_f32_32x32x32_bf16 v[212:227], v[156:159], v[104:111], v160\nglobal_load_dwordx4 v[148:151], v161, s[40:41] offset:-4096 sc1\n"
                "s_mov_b64 exec, s[42:43]\nglobal_load_dwordx4 v[152:155], v162, s[40:41] offset:-3072 sc1\n"
                "global_load_dwordx4 v[156:159], v162, s[40:41] offset:-2560 sc1\ns_mov_b64 exec, -1\nv_exp_f32 v84, v84\nv_exp_f32 v85, v85\nv_exp_f32 v86, v86\n"
                "v_mfma_f32_32x32x16_bf16 v[96:111], v[112:115], v[68:71], 0\nv_exp_f32 v87, v87\nv_exp_f32 v88, v88\nv_exp_f32 v89, v89\nv_exp_f32 v90, v90\n"
                "v_exp_f32 v91, v91\nv_smfmac_f32_32x32x32_bf16 v[164:179], v[116:119], v[80:87], v160\nv_exp_f32 v92, v92\nv_exp_f32 v93, v93\nv_exp_f32 v94, v94\n"
                "v_exp_f32 v95, v95\nds_read_b32 v231, v232\nv_exp_f32 v96, v96\nv_exp_f32 v97, v97\nv_exp_f32 v98, v98\nv_exp_f32 v99, v99\n"
                "v_smfmac_f32_32x32x32_bf16 v[164:179], v[120:123], v[88:95], v160\nv_exp_f32 v100, v100\nv_exp_f32 v101, v101\nv_exp_f32 v102, v102\n"
                "v_mfma_f32_32x32x16_bf16 v[80:95], v[112:115], v[72:75], 0\nv_exp_f32 v103, v103\nv_exp_f32 v104, v104\nv_exp_f32 v105, v105\nv_exp_f32 v106, v106\n"
                "v_exp_f32 v107, v107\nv_smfmac_f32_32x32x32_bf16 v[180:195], v[116:119], v[96:103], v160\nv_exp_f32 v108, v108\nv_exp_f32 v109, v109\n"
                "v_exp_f32 v110, v110\nv_exp_f32 v111, v111\nv_exp_f32 v80, v80\nv_exp_f32 v81, v81\nv_exp_f32 v82, v82\nv_exp_f32 v83, v83\n"
                "v_smfmac_f32_32x32x32_bf16 v[180:195], v[120:123], v[104:111], v160\nv_exp_f32 v84, v84\nv_exp_f32 v85, v85\nv_exp_f32 v86, v86\n"
                "v_mfma_f32_32x32x16_bf16 v[96:111], v[112:115], v[76:79], 0\nv_exp_f32 v87, v87\nv_exp_f32 v88, v88\nv_exp_f32 v89, v89\nv_exp_f32 v90, v90\n"
                "v_exp_f32 v91, v91\nv_smfmac_f32_32x32x32_bf16 v[196:211], v[116:119], v[80:87], v160\nv_exp_f32 v92, v92\nv_exp_f32 v93, v93\nv_exp_f32 v94, v94\n"
                "v_exp_f32 v95, v95\ns_waitcnt lgkmcnt(0)\nv_cmp_gt_u32 vcc, 8, v231\ns_cbranch_vccnz 3f\n2:\nv_exp_f32 v96, v96\nv_exp_f32 v97, v97\n"
                "v_exp_f32 v98, v98\nv_exp_f32 v99, v99\nv_smfmac_f32_32x32x32_bf16 v[196:211], v[120:123], v[88:95], v160\nv_exp_f32 v100, v100\n"
                "v_exp_f32 v101, v101\nv_exp_f32 v102, v102\ns_waitcnt vmcnt(6)\nv_mfma_f32_32x32x16_bf16 v[80:95], v[124:127], v[64:67], 0\nv_exp_f32 v103, v103\n"
                "v_exp_f32 v104, v104\nv_exp_f32 v105, v105\nv_exp_f32 v106, v106\nv_exp_f32 v107, v107\n"
                "v_smfmac_f32_32x32x32_bf16 v[212:227], v[116:119], v[96:103], v160\nv_exp_f32 v108, v108\nv_exp_f32 v109, v109\nv_exp_f32 v110, v110\n"
                "v_exp_f32 v111, v111\nv_exp_f32 v80, v80\nv_exp_f32 v81, v81\nv_exp_f32 v82, v82\nv_exp_f32 v83, v83\n"
                "v_smfmac_f32_32x32x32_bf16 v[212:227], v[120:123], v[104:111], v160\nv_exp_f32 v84, v84\nv_exp_f32 v85, v85\nv_exp_f32 v86, v86\n"
                "v_mfma_f32_32x32x16_bf16 v[96:111], v[124:127], v[68:71], 0\nv_exp_f32 v87, v87\nv_exp_f32 v88, v88\nv_exp_f32 v89, v89\nv_exp_f32 v90, v90\n"
                "v_exp_f32 v91, v91\nv_smfmac_f32_32x32x32_bf16 v[164:179], v[128:131], v[80:87], v160\nv_exp_f32 v92, v92\nv_exp_f32 v93, v93\nv_exp_f32 v94, v94\n"
                "v_exp_f32 v95, v95\nv_exp_f32 v96, v96\nv_exp_f32 v97, v97\nv_exp_f32 v98, v98\nv_exp_f32 v99, v99\n"
                "v_smfmac_f32_32x32x32_bf16 v[164:179], v[132:135], v[88:95], v160\nv_exp_f32 v100, v100\nv_exp_f32 v101, v101\nv_exp_f32 v102, v102\n"
                "v_mfma_f32_32x32x16_bf16 v[80:95], v[124:127], v[72:75], 0\nv_exp_f32 v103, v103\nv_exp_f32 v104, v104\nv_exp_f32 v105, v105\nv_exp_f32 v106, v106\n"
                "v_exp_f32 v107, v107\nv_smfmac_f32_32x32x32_bf16 v[180:195], v[128:131], v[96:103], v160\nv_exp_f32 v108, v108\nv_exp_f32 v109, v109\n"
                "v_exp_f32 v110, v110\nv_exp_f32 v111, v111\nv_exp_f32 v80, v80\nv_exp_f32 v81, v81\nv_exp_f32 v82, v82\nv_exp_f32 v83, v83\n"
                "v_smfmac_f32_32x32x32_bf16 v[180:195], v[132:135], v[104:111], v160\nv_exp_f32 v84, v84\nv_exp_f32 v85, v85\nv_exp_f32 v86, v86\n"
                "v_mfma_f32_32x32x16_bf16 v[96:111], v[124:127], v[76:79], 0\nv_exp_f32 v87, v87\nv_exp_f32 v88, v88\nv_exp_f32 v89, v89\nv_exp_f32 v90, v90\n"
                "v_exp_f32 v91, v91\nv_smfmac_f32_32x32x32_bf16 v[196:211], v[128:131], v[80:87], v160\nv_exp_f32 v92, v92\nv_exp_f32 v93, v93\nv_exp_f32 v94, v94\n"
                "v_exp_f32 v95, v95\nv_exp_f32 v96, v96\nv_exp_f32 v97, v97\nv_exp_f32 v98, v98\nv_exp_f32 v99, v99\n"
                "v_smfmac_f32_32x32x32_bf16 v[196:211], v[132:135], v[88:95], v160\nv_exp_f32 v100, v100\nv_exp_f32 v101, v101\nv_exp_f32 v102, v102\n"
                "s_waitcnt vmcnt(3)\nv_mfma_f32_32x32x16_bf16 v[80:95], v[136:139], v[64:67], 0\nv_exp_f32 v103, v103\nv_exp_f32 v104, v104\nv_exp_f32 v105, v105\n"
                "v_exp_f32 v106, v106\nv_exp_f32 v107, v107\nv_smfmac_f32_32x32x32_bf16 v[212:227], v[128:131], v[96:103], v160\nv_exp_f32 v108, v108\n"
                "v_exp_f32 v109, v109\nv_exp_f32 v110, v110\nv_exp_f32 v111, v111\nv_exp_f32 v80, v80\nv_exp_f32 v81, v81\nv_exp_f32 v82, v82\nv_exp_f32 v83, v83\n"
                "v_smfmac_f32_32x32x32_bf16 v[212:227], v[132:135], v[104:111], v160\nv_exp_f32 v84, v84\nv_exp_f32 v85, v85\nv_exp_f32 v86, v86\n"
                "v_mfma_f32_32x32x16_bf16 v[96:111], v[136:139], v[68:71], 0\nv_exp_f32 v87, v87\nv_exp_f32 v88, v88\nv_exp_f32 v89, v89\nv_exp_f32 v90, v90\n"
                "v_exp_f32 v91, v91\nv_smfmac_f32_32x32x32_bf16 v[164:179], v[140:143], v[80:87], v160\nv_exp_f32 v92, v92\nv_exp_f32 v93, v93\nv_exp_f32 v94, v94\n"
                "v_exp_f32 v95, v95\nv_exp_f32 v96, v96\nv_exp_f32 v97, v97\nv_exp_f32 v98, v98\nv_exp_f32 v99, v99\n"
                "v_smfmac_f32_32x32x32_bf16 v[164:179], v[144:147], v[88:95], v160\nv_exp_f32 v100, v100\nv_exp_f32 v101, v101\nv_exp_f32 v102, v102\n"
                "v_mfma_f32_32x32x16_bf16 v[80:95], v[136:139], v[72:75], 0\nv_exp_f32 v103, v103\nv_exp_f32 v104, v104\nv_exp_f32 v105, v105\nv_exp_f32 v106, v106\n"
                "v_exp_f32 v107, v107\nv_smfmac_f32_32x32x32_bf16 v[180:195], v[140:143], v[96:103], v160\nv_exp_f32 v108, v108\nv_exp_f32 v109, v109\n"
                "v_exp_f32 v110, v110\nv_exp_f32 v111, v111\nv_exp_f32 v80, v80\nv_exp_f32 v81, v81\nv_exp_f32 v82, v82\nv_exp_f32 v83, v83\n"
                "v_smfmac_f32_32x32x32_bf16 v[180:195], v[144:147], v[104:111], v160\nv_exp_f32 v84, v84\nv_exp_f32 v85, v85\nv_exp_f32 v86, v86\n"
                "v_mfma_f32_32x32x16_bf16 v[96:111], v[136:139], v[76:79], 0\nv_exp_f32 v87, v87\nv_exp_f32 v88, v88\nv_exp_f32 v89, v89\nv_exp_f32 v90, v90\n"
                "v_exp_f32 v91, v91\nv_smfmac_f32_32x32x32_bf16 v[196:211], v[140:143], v[80:87], v160\nv_exp_f32 v92, v92\nv_exp_f32 v93, v93\nv_exp_f32 v94, v94\n"
                "v_exp_f32 v95, v95\nv_exp_f32 v96, v96\nv_exp_f32 v97, v97\nv_exp_f32 v98, v98\nv_exp_f32 v99, v99\n"
                "v_smfmac_f32_32x32x32_bf16 v[196:211], v[144:147], v[88:95], v160\nv_exp_f32 v100, v100\nv_exp_f32 v101, v101\nv_exp_f32 v102, v102\n"
                "s_waitcnt vmcnt(0)\nv_mfma_f32_32x32x16_bf16 v[80:95], v[148:151], v[64:67], 0\nv_exp_f32 v103, v103\nv_exp_f32 v104, v104\nv_exp_f32 v105, v105\n"
                "v_exp_f32 v106, v106\nv_exp_f32 v107, v107\nv_smfmac_f32_32x32x32_bf16 v[212:227], v[140:143], v[96:103], v160\nv_exp_f32 v108, v108\n"
                "v_exp_f32 v109, v109\nv_exp_f32 v110, v110\nv_exp_f32 v111, v111\nv_exp_f32 v80, v80\nv_exp_f32 v81, v81\nv_exp_f32 v82, v82\nv_exp_f32 v83, v83\n"
                "v_smfmac_f32_32x32x32_bf16 v[212:227], v[144:147], v[104:111], v160\nv_exp_f32 v84, v84\nv_exp_f32 v85, v85\nv_exp_f32 v86, v86\n"
                "v_mfma_f32_32x32x16_bf16 v[96:111], v[148:151], v[68:71], 0\nv_exp_f32 v87, v87\nv_exp_f32 v88, v88\nv_exp_f32 v89, v89\nv_exp_f32 v90, v90\n"
                "v_exp_f32 v91, v91\nv_smfmac_f32_32x32x32_bf16 v[164:179], v[152:155], v[80:87], v160\nv_exp_f32 v92, v92\nv_exp_f32 v93, v93\nv_exp_f32 v94, v94\n"
                "v_exp_f32 v95, v95\nv_exp_f32 v96, v96\nv_exp_f32 v97, v97\nv_exp_f32 v98, v98\nv_exp_f32 v99, v99\n"
                "v_smfmac_f32_32x32x32_bf16 v[164:179], v[156:159], v[88:95], v160\nv_exp_f32 v100, v100\nv_exp_f32 v101, v101\nv_exp_f32 v102, v102\n"
                "v_mfma_f32_32x32x16_bf16 v[80:95], v[148:151], v[72:75], 0\nv_exp_f32 v103, v103\nv_exp_f32 v104, v104\nv_exp_f32 v105, v105\nv_exp_f32 v106, v106\n"
                "v_exp_f32 v107, v107\nv_smfmac_f32_32x32x32_bf16 v[180:195], v[152:155], v[96:103], v160\nv_exp_f32 v108, v108\nv_exp_f32 v109, v109\n"
                "v_exp_f32 v110, v110\nv_exp_f32 v111, v111\nds_write_b128 v228, v[164:167]\nds_write_b128 v228, v[168:171] offset:1024\n"
                "ds_write_b64 v228, v[172:173] offset:2048\nv_exp_f32 v80, v80\nv_exp_f32 v81, v81\nv_exp_f32 v82, v82\nv_exp_f32 v83, v83\n"
                "v_smfmac_f32_32x32x32_bf16 v[180:195], v[156:159], v[104:111], v160\nv_exp_f32 v84, v84\nv_exp_f32 v85, v85\nv_exp_f32 v86, v86\n"
                "v_mfma_f32_32x32x16_bf16 v[96:111], v[148:151], v[76:79], 0\nv_exp_f32 v87, v87\nv_exp_f32 v88, v88\nv_exp_f32 v89, v89\nv_exp_f32 v90, v90\n"
                "v_exp_f32 v91, v91\nv_smfmac_f32_32x32x32_bf16 v[196:211], v[152:155], v[80:87], v160\nv_exp_f32 v92, v92\nv_exp_f32 v93, v93\nv_exp_f32 v94, v94\n"
                "v_exp_f32 v95, v95\nds_write_b128 v229, v[180:183]\nds_write_b128 v229, v[184:187] offset:1024\nds_write_b64 v229, v[188:189] offset:2048\n"
                "v_exp_f32 v96, v96\nv_exp_f32 v97, v97\nv_exp_f32 v98, v98\nv_exp_f32 v99, v99\nv_smfmac_f32_32x32x32_bf16 v[196:211], v[156:159], v[88:95], v160\n"
                "v_exp_f32 v100, v100\nv_exp_f32 v101, v101\nv_exp_f32 v102, v102\nv_exp_f32 v103, v103\nv_exp_f32 v104, v104\nv_exp_f32 v105, v105\n"
                "v_exp_f32 v106, v106\nv_exp_f32 v107, v107\nv_smfmac_f32_32x32x32_bf16 v[212:227], v[152:155], v[96:103], v160\nv_exp_f32 v108, v108\n"
                "v_exp_f32 v109, v109\nv_exp_f32 v110, v110\nv_exp_f32 v111, v111\nds_write_b128 v230, v[196:199]\nds_write_b128 v230, v[200:203] offset:1024\n"
                "ds_write_b64 v230, v[204:205] offset:2048\nv_smfmac_f32_32x32x32_bf16 v[212:227], v[156:159], v[104:111], v160\nv_mov_b32 v231, 1\n"
                "s_waitcnt lgkmcnt(0)\ns_mov_b64 exec, 1\nds_add_u32 v233, v231\ns_mov_b64 exec, -1\ns_branch 4f\n3:\ns_sleep 1\nds_read_b32 v231, v232\n"
                "s_waitcnt lgkmcnt(0)\nv_cmp_gt_u32 vcc, 8, v231\ns_cbranch_vccnz 3b\ns_branch 2b\n4:\ns_nop 5\n"
                : "=&{v[164:179]}"(o0), "=&{v[180:195]}"(o1), "=&{v[196:211]}"(o2), "=&{v[212:227]}"(o3), "+{v[116:123]}"(va), "+{v[128:135]}"(vb),
                  "+{v[140:147]}"(vc), "+{v[152:159]}"(vd), "+{s[40:41]}"(base)
                : "{v[64:67]}"(q0), "{v[68:71]}"(q1), "{v[72:75]}"(q2), "{v[76:79]}"(q3), "{v160}"(0xddddu), "{v161}"(lane * 16),
                  "{v162}"(h * 256 + (r & 15) * 16), "{s[42:43]}"(0x0000ffff0000ffffull), "{v228}"(rec0), "{v229}"(rec1), "{v230}"(rec2),
                  "{v232}"(L_SYNC + S_READY), "{v233}"(L_SYNC + S_PART + a * 4), "{s45}"(a)
                : "memory", "scc", "vcc", "s44", "v231", "v80", "v81", "v82", "v83", "v84", "v85", "v86", "v87", "v88", "v89", "v90", "v91", "v92", "v93",
                  "v94", "v95", "v96", "v97", "v98", "v99", "v100", "v101", "v102", "v103", "v104", "v105", "v106", "v107", "v108", "v109", "v110", "v111",
                  "v112", "v113", "v114", "v115", "v124", "v125", "v126", "v127", "v136", "v137", "v138", "v139", "v148", "v149", "v150", "v151");
        } else {
            float m0 = -INFINITY, m1 = -INFINITY, m2 = -INFINITY;
            m3 = -INFINITY;
            o0 = o1 = o2 = o3 = (f32x16){};
            const u32x4 ones = {0x3f803f80u, 0x3f803f80u, 0x3f803f80u, 0x3f803f80u};
#pragma unroll 1
            for (int t = 0; t < 16; ++t) {
                const u32x4 kf = __builtin_amdgcn_raw_buffer_load_b128(wsr, kvo + t * 2048 + lane * 16, 0, SC1);
                u32x4 v0 = __builtin_amdgcn_raw_buffer_load_b128(wsr, kvo + t * 2048 + 1024 + h * 256 + (r & 15) * 16, 0, SC1);
                u32x4 v1 = __builtin_amdgcn_raw_buffer_load_b128(wsr, kvo + t * 2048 + 1536 + h * 256 + (r & 15) * 16, 0, SC1);
                if (r >= 16) { v0 = ones; v1 = ones; }
                auto step = [&](const u32x4& q, f32x16& o, float& m) {
                    f32x16 s = mfma32(kf, q, (f32x16){});
                    float mx = s[0];
#pragma unroll
                    for (int i = 1; i < 16; ++i) mx = fmaxf(mx, s[i]);
                    const float mn = fmaxf(m, halves_max(mx)), al = __builtin_amdgcn_exp2f(m - mn);
                    m = mn;
#pragma unroll
                    for (int i = 0; i < 16; ++i) { s[i] = __builtin_amdgcn_exp2f(s[i] - mn); o[i] *= al; }
                    const f32x8 lo = __builtin_shufflevector(s, s, 0, 1, 2, 3, 4, 5, 6, 7), hi = __builtin_shufflevector(s, s, 8, 9, 10, 11, 12, 13, 14, 15);
                    o = __builtin_amdgcn_smfmac_f32_32x32x32_bf16(__builtin_bit_cast(bf16x8, v0), __builtin_bit_cast(bf16x16, lo), o, 0xdddd, 0, 0);
                    o = __builtin_amdgcn_smfmac_f32_32x32x32_bf16(__builtin_bit_cast(bf16x8, v1), __builtin_bit_cast(bf16x16, hi), o, 0xdddd, 0, 0);
                };
                step(q0, o0, m0); step(q1, o1, m1); step(q2, o2, m2); step(q3, o3, m3);
            }
            await(S_READY, 8);
            auto put = [&](u32 rec, const f32x16& o, float m) {
                ST(f32x4, rec) = (f32x4){o[0], o[1], o[2], o[3]}; ST(f32x4, rec + 1024) = (f32x4){o[4], o[5], o[6], o[7]}; ST(f32x2, rec + 2048) = (f32x2){o[8], m};
            };
            put(rec0, o0, m0); put(rec1, o1, m1); put(rec2, o2, m2);
            arrive(S_PART + a * 4);
        }
        u32 ro = a * L_TAB + tb * 9216 + lane * 16, oo = WS_OF + ((qb * 4 + tb) * 32 + hp * 2 + a) * 1024 + lane * 16;
        asm volatile("" : "+v"(ro), "+v"(oo));
        await(S_PART + a * 4, 4);
        {
            f32x4 pa[3], pb[3]; f32x2 lm[3];
#pragma unroll
            for (int i = 0; i < 3; ++i) lm[i] = LD(f32x2, ro + i * 3072 + 2048);
#pragma unroll
            for (int i = 0; i < 3; ++i) { pa[i] = LD(f32x4, ro + i * 3072); pb[i] = LD(f32x4, ro + i * 3072 + 1024); }
            SB();
            float l, acc[8];
            if (__builtin_expect(__all(m3 == lm[0].y && m3 == lm[1].y && m3 == lm[2].y), 1)) {
                l = (o3[8] + lm[0].x) + (lm[1].x + lm[2].x);
#pragma unroll
                for (int j = 0; j < 4; ++j) { acc[j] = (o3[j] + pa[0][j]) + (pa[1][j] + pa[2][j]); acc[4 + j] = (o3[4 + j] + pb[0][j]) + (pb[1][j] + pb[2][j]); }
            } else {
                const float mm = fmaxf(fmaxf(m3, lm[0].y), fmaxf(lm[1].y, lm[2].y)), s0 = __builtin_amdgcn_exp2f(m3 - mm);
                l = o3[8] * s0;
#pragma unroll
                for (int j = 0; j < 8; ++j) acc[j] = o3[j] * s0;
#pragma unroll
                for (int i = 0; i < 3; ++i) {
                    const float s = __builtin_amdgcn_exp2f(lm[i].y - mm);
                    l = fmaf(lm[i].x, s, l);
#pragma unroll
                    for (int j = 0; j < 4; ++j) { acc[j] = fmaf(pa[i][j], s, acc[j]); acc[4 + j] = fmaf(pb[i][j], s, acc[4 + j]); }
                }
            }
            const float rl = __builtin_amdgcn_rcpf(l);
            const u32x4 of = {pk2(acc[0] * rl, acc[1] * rl), pk2(acc[2] * rl, acc[3] * rl), pk2(acc[4] * rl, acc[5] * rl), pk2(acc[6] * rl, acc[7] * rl)};
            __builtin_amdgcn_raw_buffer_store_b128(of, wsr, oo, 0, SC1);
        }
        asm volatile("s_waitcnt vmcnt(0)" ::: "memory");
        if (lane == 0) __hip_atomic_store((GAS u64*)(ws + WS_OFLAG + ((qb * 4 + tb) * 32 + hp * 2 + a) * 64), tag << 16, RLX_AGENT);
    }

    if (a == 0) {
        const int rb = qb * 4 + (hp >> 3) * 2 + (tb & 1), cb = (hp & 7) * 2 + (tb >> 1), g1 = (lane >> 4) & 1, q4 = (lane >> 2) & 3;
        u32 wa = L_WO + (4 * h + q4) * 128 + ((((tb >> 1) ^ (q4 >> 1)) & 1) << 6) + (g1 << 5) + (lane & 3) * 8;
        asm volatile("" : "+v"(wa));
        {
            const u32 wo = (lane >> 3) * 1024 + (((lane & 7) ^ ((lane >> 2) & 4)) << 4);
#pragma unroll
            for (int i = 0; i < 4; ++i) dma4(wo, wo + 7168, wo + 14336, wo + 21504, p.wo + ((tb * 128 + i * 32) * 1024 + (hp & 7) * 128), L_WO + (tb * 128 + i * 32) * 128);
        }
        f32x16 acc = {};
        u32x4 yf[2];
        auto half = [&](int odd) {
            u32x4 wf[16], of[16];
#pragma unroll
            for (int i = 0; i < 16; ++i) wf[i] = trd2(wa + (2 * i + odd) * 2048, wa + (2 * i + odd) * 2048 + 1024);
            poll_tag(ws + WS_OFLAG + (rb * 32 + (lane & 15) * 2 + odd) * 64, lane < 16, tag);
#pragma unroll
            for (int i = 0; i < 16; ++i) of[i] = __builtin_amdgcn_raw_buffer_load_b128(wsr, WS_OF + (rb * 32 + odd) * 1024 + lane * 16, i * 2048, SC1);
            __builtin_amdgcn_sched_barrier(0);
#pragma unroll
            for (int i = 0; i < 16; ++i) acc = mfma32(wf[i], of[i], acc);
        };
        {
            poll_tag(ws + WS_YFLAG + ((cb * 32 + r) * 2 + (rb >> 5)) * 64, lane < 32, tag);
#pragma unroll
            for (int i = 0; i < 2; ++i)
                ST(u32x4, L_YT + tb * 2048 + i * 1024 + lane * 16) =
                    __builtin_amdgcn_raw_buffer_load_b128(wsr, WS_YT + ((cb * 32 + i * 16 + (lane >> 2)) * 2048 + rb * 32) * 2 + (lane & 3) * 16, 0, SC1);
            LGKM0();
            const u32 ya = L_YT + tb * 2048 + (4 * h + q4) * 64 + g1 * 32 + (lane & 3) * 8;
            yf[0] = trd2(ya, ya + 512); yf[1] = trd2(ya + 1024, ya + 1536);
        }
        arrive(S_WO); await(S_WO, 4);
        half(0);
        float gs = 0.f;
        {
            float gp[16];
#pragma unroll
            for (int j = 0; j < 16; ++j) gp[j] = __uint_as_float(__builtin_amdgcn_raw_buffer_load_b32(wsr, WS_GP + ((qb * 32 + h * 16) * 128 + (rb & 3) * 32 + r) * 4 + j * 512, 0, SC1));
#pragma unroll
            for (int j = 0; j < 16; ++j) gs += gp[j];
        }
        const float g = 1.f / (1.f + __expf(-halves_sum(gs)));
        float og = 1.f - g;
        f32x16 gy;
#pragma unroll
        for (int j = 0; j < 8; ++j) { const u32 y2 = yf[j >> 2][j & 3]; gy[2 * j] = g * bflo(y2); gy[2 * j + 1] = g * bfhi(y2); }
        u32 oo = ((rb * 32 + (lane >> 2)) * 512 + cb * 32) * 2 + (lane & 3) * 16;
        asm volatile("" : "+v"(gy), "+v"(og), "+v"(oo));
        half(1 - 0);
        const __amdgpu_buffer_rsrc_t outr = __builtin_amdgcn_make_buffer_rsrc(p.out, 0, 2048 * 512 * 2, 0x00020000);
        acc = acc * og + gy;
#pragma unroll
        for (int i = 0; i < 4; ++i) ST(u32x2, L_YT + tb * 2048 + r * 64 + i * 16 + h * 8) = (u32x2){pk2(acc[4 * i], acc[4 * i + 1]), pk2(acc[4 * i + 2], acc[4 * i + 3])};
        asm volatile("" ::: "memory");
#pragma unroll
        for (int i = 0; i < 2; ++i) __builtin_amdgcn_raw_buffer_store_b128(LD(u32x4, L_YT + tb * 2048 + i * 1024 + lane * 16), outr, oo + i * 16384, 0, 0);
        if (b == 0 && threadIdx.x == 0 * 256) *(GAS u64*)(ws + WS_NONCE) = nonce + 1;
    }
}

extern "C" void kernel_launch(void* const* d_in, const int* in_sizes, int n_in, void* d_out, int out_size, void* d_ws, size_t ws_size,
                              hipStream_t stream) {
    Params p;
    p.x = (const char*)d_in[0]; p.a_re = (const char*)d_in[1]; p.a_im = (const char*)d_in[2]; p.b_re = (const char*)d_in[3];
    p.b_im = (const char*)d_in[4]; p.c_re = (const char*)d_in[5]; p.c_im = (const char*)d_in[6]; p.log_dt = (const char*)d_in[7];
    p.dskip = (const char*)d_in[8]; p.wqkv = (const char*)d_in[10]; p.wo = (const char*)d_in[11]; p.gate_w = (const char*)d_in[12];
    p.out = (char*)d_out; p.ws = (char*)d_ws;
    s4_mha_mix<<<256, 512, 0, stream>>>(p);
}
```
